```python
import torch

def _softmax(x, axis=-1):
    return torch.softmax(x, dim=axis)
def _gelu(x):
    return torch.nn.functional.gelu(x, approximate="tanh")
def _silu(x):
    return torch.nn.functional.silu(x)
def _sigmoid(x):
    return torch.sigmoid(x)
def _layernorm(x, axis=-1, eps=1e-5):
    m = x.mean(dim=axis, keepdim=True)
    v = x.var(dim=axis, keepdim=True, unbiased=False)
    return (x - m) / torch.sqrt(v + eps)
def _scatter_add(tgt, idx, src, axis=0):
    return tgt.clone().index_add_(axis, idx, src)
def _segment_sum(src, seg, n):
    out = torch.zeros((n,) + src.shape[1:], dtype=src.dtype)
    return out.index_add_(0, seg, src)
def _pad(x, pw):
    flat = tuple(v for pair in reversed(pw) for v in pair)
    return torch.nn.functional.pad(x, flat)

kernel_name = 'gated_mix__mha_block__geglu_ffn__prompt'

def setup_inputs():

    _w = (lambda a: (lambda t: t.to(torch.bfloat16) if t.is_floating_point() else t)(torch.as_tensor(a)))
    def _A_setup():
        B, S, D, H = 1, 2048, 512, 32
        return {
            "x": _w((lambda *s: torch.randn(*s, dtype=torch.bfloat16) * 0.05)(B, S, D)),
            "wqkv": _w((lambda *s: torch.randn(*s, dtype=torch.bfloat16) * 0.8)(D, 3 * D)),
            "wo": _w((lambda *s: torch.randn(*s, dtype=torch.bfloat16) * 0.05)(D, D)),
        }
    _A_in = _A_setup()

    def _B_setup():
        B, S, D, F = 1, 2048, 512, 1376
        return {
            "x": _w((lambda *s: torch.randn(*s, dtype=torch.bfloat16) * 0.05)(B, S, D)),
            "w_gate": _w((lambda *s: torch.randn(*s, dtype=torch.bfloat16) * 0.1)(D, F)),
            "w_up": _w((lambda *s: torch.randn(*s, dtype=torch.bfloat16) * 0.1)(D, F)),
            "w_down": _w((lambda *s: torch.randn(*s, dtype=torch.bfloat16) * 0.1)(F, D)),
        }
    _B_in = _B_setup()

    return {
        "A_x": _A_in["x"],
        "A_wqkv": _A_in["wqkv"],
        "A_wo": _A_in["wo"],
        "B_x": _B_in["x"],
        "B_w_gate": _B_in["w_gate"],
        "B_w_up": _B_in["w_up"],
        "B_w_down": _B_in["w_down"],
        "gate_w": _w((lambda *s: torch.randn(*s, dtype=torch.bfloat16) * 0.05)(512) * 16.0),
    }

def reference(A_x, A_wqkv, A_wo, B_x, B_w_gate, B_w_up, B_w_down, gate_w):
    def _A_ref(x, wqkv, wo):
        B, S, D = x.shape
        H = 32
        Dh = D // H
        qkv = (lambda x, *d: x.permute(*d))((x @ wqkv).reshape(B, S, 3, H, Dh), 2, 0, 3, 1, 4)
        q, k, v = qkv[0], qkv[1], qkv[2]
        scores = torch.einsum("bhqd,bhkd->bhqk", q, k) * (Dh ** -0.5)
        p = _softmax(scores, axis=-1)
        o = torch.einsum("bhqk,bhkd->bhqd", p, v)
        return (lambda x, *d: x.permute(*d))(o, 0, 2, 1, 3).reshape(B, S, D) @ wo

    def _B_ref(x, w_gate, w_up, w_down):
        return (_gelu(x @ w_gate) * (x @ w_up)) @ w_down

    x = A_x
    g = _sigmoid((x * gate_w).sum(-1, keepdims=True))
    a = _A_ref(x, A_wqkv, A_wo)
    b = _B_ref(x, B_w_gate, B_w_up, B_w_down)
    return g * a + (1 - g) * b
```

```cpp
#include <hip/hip_runtime.h>
#include <hip/hip_bf16.h>
#include <stdint.h>

typedef __bf16 bf16x8 __attribute__((ext_vector_type(8)));
typedef __bf16 bf16x2 __attribute__((ext_vector_type(2)));
typedef short s16x4 __attribute__((ext_vector_type(4)));
typedef float f32x16 __attribute__((ext_vector_type(16)));
typedef float f32x2 __attribute__((ext_vector_type(2)));
typedef unsigned u32x4 __attribute__((ext_vector_type(4)));
typedef unsigned u32x2 __attribute__((ext_vector_type(2)));
#define LAS __attribute__((address_space(3)))
#define DI __device__ __forceinline__
#define MFMA(a, b, c) __builtin_amdgcn_mfma_f32_32x32x16_bf16((a), (b), (c), 0, 0, 0)
#define SBAR() __builtin_amdgcn_sched_barrier(0)

constexpr int SEQ = 2048, DM = 512, NH = 32, FF = 1376;
constexpr int NU = 86;
constexpr int KS3 = 118;
constexpr size_t WS_Q = 0;
constexpr size_t WS_KV = 2u << 20;
constexpr size_t WS_X = 6u << 20;
constexpr size_t WS_WGU = 8u << 20;
constexpr size_t WS_WOD = 11u << 20;
constexpr size_t WS_OH = 13u << 20;
constexpr size_t WS_G = 21u << 20;

DI unsigned cvtpk(float lo, float hi) { f32x2 v = {lo, hi}; bf16x2 b = __builtin_convertvector(v, bf16x2); return __builtin_bit_cast(unsigned, b); }
DI unsigned rfl(unsigned x) { return __builtin_amdgcn_readfirstlane(x); }
template <int IMM> DI void glds(const void* sbase, unsigned voff, unsigned ldsb) {
  asm volatile("s_mov_b32 m0, %2\n\ts_nop 0\n\tglobal_load_lds_dwordx4 %0, %1 offset:%3" ::"v"(voff), "s"(sbase), "s"(ldsb), "i"(IMM) : "memory");
}
#define WAIT_BAR(N) asm volatile("s_waitcnt vmcnt(" #N ") lgkmcnt(0)\n\ts_barrier" ::: "memory")
DI bf16x8 ldsr(const LAS char* p) { return *(const LAS bf16x8*)p; }
DI bf16x8 ldstr(const LAS char* p0, const LAS char* p1) {
  s16x4 a = __builtin_amdgcn_ds_read_tr16_b64_v4i16((LAS s16x4*)p0), b = __builtin_amdgcn_ds_read_tr16_b64_v4i16((LAS s16x4*)p1);
  return __builtin_bit_cast(bf16x8, __builtin_shufflevector(a, b, 0, 1, 2, 3, 4, 5, 6, 7));
}
DI u32x4 pack8(const f32x16& a, int o, float s) {
  return (u32x4){cvtpk(a[o] * s, a[o + 1] * s), cvtpk(a[o + 2] * s, a[o + 3] * s), cvtpk(a[o + 4] * s, a[o + 5] * s), cvtpk(a[o + 6] * s, a[o + 7] * s)};
}

__global__ __launch_bounds__(256, 1) void k1_qkv(const char* __restrict__ x, const char* __restrict__ wqkv, const char* __restrict__ wo,
                                                 const char* __restrict__ wg, const char* __restrict__ wu, const char* __restrict__ wd,
                                                 const char* __restrict__ gw, char* __restrict__ ws) {
  __shared__ __attribute__((aligned(1024))) char smem[163840];
  const LAS char* lds = (const LAS char*)smem;
  const unsigned lds0 = (unsigned)(uintptr_t)lds;
  const int tid = threadIdx.x, lane = tid & 63, w = rfl(tid >> 6), b = blockIdx.x;
  const int hg = b & 7, rb = b >> 3;
  const int r = lane & 31, h = lane >> 5, ru = w & 1, hp = w >> 1;
  const int gwv = b * 4 + w;
  const u32x4 gx0 = *(const u32x4*)(x + (size_t)(2 * gwv) * 1024 + lane * 16);
  const u32x4 gx1 = *(const u32x4*)(x + (size_t)(2 * gwv + 1) * 1024 + lane * 16);
  const u32x4 gwf = *(const u32x4*)(gw + lane * 16);

  const unsigned vx0 = (16 * w + (lane >> 3)) * 1024 + (((lane & 7) ^ (lane >> 4)) << 4);
  const unsigned vx1 = (16 * w + 8 + (lane >> 3)) * 1024 + (((lane & 7) ^ (4 + (lane >> 4))) << 4);
  const unsigned vw0 = (16 * w + (lane >> 3)) * 3072 + (((lane & 7) ^ (((lane >> 4) & 1) << 2)) << 4);
  const unsigned vw1 = vw0 + 8 * 3072;
  const char* xb = x + (size_t)rb * 64 * 1024;
  const char* wb = wqkv + hg * 128;
  auto stage = [&](int s) {
    const unsigned d = lds0 + (s & 3) * 32768 + w * 2048;
    const char* xs = xb + s * 128;
    const char* wsrc = wb + (size_t)s * 64 * 3072;
    glds<0>(xs, vx0, d); glds<0>(xs, vx1, d + 1024);
    glds<0>(wsrc, vw0, d + 8192); glds<0>(wsrc, vw1, d + 8192 + 1024);
    glds<1024>(wsrc, vw0, d + 16384 - 1024); glds<1024>(wsrc, vw1, d + 16384);
    glds<2048>(wsrc, vw0, d + 24576 - 2048); glds<2048>(wsrc, vw1, d + 24576 - 1024);
  };
  stage(0); stage(1); stage(2); stage(3);
  auto pt_load = [&](int j, unsigned d) {
    if (j < 704) {
      const int ks = j / 22, cg = j % 22;
      const unsigned ch = (cg == 21) ? (lane & 3) : (lane & 7);
      const unsigned v = (lane >> 3) * 2752 + ch * 16, so = ks * 16 * 2752 + cg * 128;
      glds<0>(wg + so, v, d); glds<0>(wg + so + 8 * 2752, v, d + 1024);
      glds<0>(wu + so, v, d + 2048); glds<0>(wu + so + 8 * 2752, v, d + 3072);
    } else {
      const int jd = j - 704, kk = jd >> 2, cq = jd & 3;
      const char* src = (kk < 32 ? wo + kk * 16384 : wd + (kk - 32) * 16384) + cq * 256;
      const unsigned v = (lane >> 4) * 1024 + (lane & 15) * 16;
      glds<0>(src, v, d); glds<0>(src + 4096, v, d + 1024); glds<0>(src + 8192, v, d + 2048); glds<0>(src + 12288, v, d + 3072);
    }
  };
  auto pt_store = [&](int j, const LAS char* d) {
    const int ch = (lane >> 4) & 1, q = (lane >> 2) & 3, p = lane & 3;
    if (j < 704) {
      const int ks = j / 22, cg = j % 22;
      const LAS char* a = d + ch * 2048 + (8 * h + q) * 128 + p * 8;
#pragma unroll
      for (int e = 0; e < 4; ++e) {
        const bf16x8 f = ldstr(a + e * 32, a + e * 32 + 512);
        if (4 * cg + e < NU) *(bf16x8*)(ws + WS_WGU + (size_t)((4 * cg + e) * 32 + ks) * 1024 + lane * 16) = f;
      }
    } else {
      const int jd = j - 704, kk = jd >> 2, cq = jd & 3;
      const LAS char* a = d + (4 * h + q) * 256 + (p & 1) * 32 + ch * 16 + (p >> 1) * 8;
#pragma unroll
      for (int e = 0; e < 4; ++e) {
        const bf16x8 f = ldstr(a + e * 64, a + e * 64 + 2048);
        *(bf16x8*)(ws + WS_WOD + (size_t)((4 * cq + e) * KS3 + kk) * 1024 + lane * 16) = f;
      }
    }
  };
  pt_load(gwv, lds0 + 131072 + w * 8192);
  if (gwv + 1024 < 1176) pt_load(gwv + 1024, lds0 + 131072 + w * 8192 + 4096);

  f32x16 aq = {}, ak = {}, av = {};
  const unsigned xo = (32 * ru + r) * 128 + ((h ^ ((r >> 1) & 7)) << 4);
  const unsigned wq_ = (lane >> 2) & 3, wp = lane & 3, wch = (lane >> 4) & 1;
  const unsigned wof = 8192 + (8 * h + wq_) * 128 + (((hp * 4 + wch * 2 + (wp >> 1)) ^ ((wq_ >> 1) << 2)) << 4) + (wp & 1) * 8;
#pragma unroll
  for (int s = 0; s < 8; ++s) {
    if (s == 0) WAIT_BAR(28); else if (s <= 3) WAIT_BAR(20); else if (s <= 5) WAIT_BAR(16); else if (s == 6) WAIT_BAR(8); else WAIT_BAR(0);
    if (s >= 1 && s + 3 <= 7) stage(s + 3);
    const LAS char* sb = lds + (s & 3) * 32768;
#pragma unroll
    for (int ks = 0; ks < 4; ++ks) {
      const bf16x8 xf = ldsr(sb + (xo ^ (ks * 32)));
      const LAS char* wa = sb + wof + ks * 2048;
      const bf16x8 fq = ldstr(wa, wa + 512), fk = ldstr(wa + 8192, wa + 8192 + 512), fv = ldstr(wa + 16384, wa + 16384 + 512);
      aq = MFMA(fq, xf, aq); ak = MFMA(fk, xf, ak); av = MFMA(xf, fv, av);
      if (s == hg && (ks >> 1) == hp) *(bf16x8*)(ws + WS_X + (size_t)((rb * 2 + ru) * 32 + s * 4 + ks) * 1024 + lane * 16) = xf;
    }
  }
  {
    const int rug = rb * 2 + ru, hd = hg * 4 + hp * 2;
    const float qs = 0.25f * 1.4426950408889634f;
    char* qp = ws + WS_Q + (size_t)(hd * 64 + rug) * 1024 + lane * 16;
    *(u32x4*)qp = pack8(aq, 0, qs); *(u32x4*)(qp + 65536) = pack8(aq, 8, qs);
    const int slot = (r & 19) | ((r & 4) << 1) | ((r & 8) >> 1);
    char* kp = ws + WS_KV + (size_t)(hd * 64 + rug) * 2048 + h * 512 + slot * 16;
    *(u32x4*)kp = pack8(ak, 0, 1.f); *(u32x4*)(kp + 131072) = pack8(ak, 8, 1.f);
    char* vp = ws + WS_KV + (size_t)((hd + (r >> 4)) * 64 + rug) * 2048 + 1024 + h * 512 + (r & 15) * 16;
    *(u32x4*)vp = pack8(av, 0, 1.f); *(u32x4*)(vp + 256) = pack8(av, 8, 1.f);
  }
  pt_store(gwv, lds + 131072 + w * 8192);
  if (gwv + 1024 < 1176) pt_store(gwv + 1024, lds + 131072 + w * 8192 + 4096);
  {
    float z0 = 0.f, z1 = 0.f;
#pragma unroll
    for (int i = 0; i < 4; ++i) {
      const float wl = __uint_as_float(gwf[i] << 16), wh = __uint_as_float(gwf[i] & 0xffff0000u);
      z0 = fmaf(__uint_as_float(gx0[i] << 16), wl, z0); z0 = fmaf(__uint_as_float(gx0[i] & 0xffff0000u), wh, z0);
      z1 = fmaf(__uint_as_float(gx1[i] << 16), wl, z1); z1 = fmaf(__uint_as_float(gx1[i] & 0xffff0000u), wh, z1);
    }
#pragma unroll
    for (int o = 32; o >= 1; o >>= 1) { z0 += __shfl_xor(z0, o); z1 += __shfl_xor(z1, o); }
    if (lane < 2) { const float z = lane ? z1 : z0; ((float*)(ws + WS_G))[2 * gwv + lane] = 1.f / (1.f + __expf(-z)); }
  }
}

#define AW 8
#define R_SA 32
#define R_SB 48
#define R_OA 64
#define R_OB 80
#define R_CA 96
#define R_CB 112
#define R_QA 16
#define R_QB 20
#define R_K 24
#define R_V0 8
#define R_V1 12
#define R_AD 28
#define R_IX 29
#define STR_(x) #x
#define STR(x) STR_(x)
#define VR(b, o, n) "v[" STR(b) "+" #o ":" STR(b) "+" #o "+" #n "-1]"
#define EX(b, i) "v_exp_f32 v[" STR(b) "+" #i "], v[" STR(b) "+" #i "]\n"
#define PV(o, v, s, i) "v_smfmac_f32_32x32x32_bf16 " VR(o, 0, 16) ", " VR(v, 0, 4) ", " VR(s, i, 8) ", v" STR(R_IX) "\n"
#define QK(s, q, c) "v_mfma_f32_32x32x16_bf16 " VR(s, 0, 16) ", " VR(R_K, 0, 4) ", " VR(q, 0, 4) ", " VR(c, 0, 16) "\n"
#define DSR(d, off) "ds_read_b128 " VR(d, 0, 4) ", v" STR(R_AD) " offset:" off "\n"
#define TILE(o)                                                                                              \
  PV(R_OB, R_V0, R_SB, 0) EX(R_SA, 0) EX(R_SA, 1) EX(R_SA, 2) PV(R_OB, R_V1, R_SB, 8)                          \
  "s_mov_b64 exec, %[msk]\n" DSR(R_V0, #o "+1024") DSR(R_V1, #o "+1280") "s_mov_b64 exec, -1\n"               \
  EX(R_SA, 3) EX(R_SA, 4) QK(R_SB, R_QB, R_CB) DSR(R_K, #o "+2048")                                          \
  EX(R_SA, 5) EX(R_SA, 6) EX(R_SA, 7) EX(R_SA, 8) EX(R_SA, 9) EX(R_SA, 10) EX(R_SA, 11) EX(R_SA, 12) EX(R_SA, 13) EX(R_SA, 14) EX(R_SA, 15) \
  "s_waitcnt lgkmcnt(1)\n"                                                                                   \
  PV(R_OA, R_V0, R_SA, 0) EX(R_SB, 0) EX(R_SB, 1) EX(R_SB, 2) PV(R_OA, R_V1, R_SA, 8)                          \
  EX(R_SB, 3) EX(R_SB, 4) "s_waitcnt lgkmcnt(0)\n" QK(R_SA, R_QA, R_CA)                                      \
  EX(R_SB, 5) EX(R_SB, 6) EX(R_SB, 7) EX(R_SB, 8) EX(R_SB, 9) EX(R_SB, 10) EX(R_SB, 11) EX(R_SB, 12) EX(R_SB, 13) EX(R_SB, 14) EX(R_SB, 15)

DI void attn_loop(f32x16& oa, f32x16& ob, const f32x16& ca, const f32x16& cb, const bf16x8& qa, const bf16x8& qb, unsigned ad, int nit) {
  u32x4 ones = {0x3f803f80u, 0x3f803f80u, 0x3f803f80u, 0x3f803f80u}, v0 = ones, v1 = ones, kf;
  f32x16 sa, sb = {};
  unsigned ix = 0xddddddddu;
  unsigned long long msk = 0x0000ffff0000ffffull;
  asm volatile(
      DSR(R_K, "0") "s_waitcnt lgkmcnt(0)\n" QK(R_SA, R_QA, R_CA) "s_nop 7\n s_nop 3\n"
      "1:\n" TILE(0) TILE(2048) TILE(4096) TILE(6144)
      "v_add_u32 v" STR(R_AD) ", 0x2000, v" STR(R_AD) "\n"
      "s_sub_u32 %[n], %[n], 1\n s_cmp_lg_u32 %[n], 0\n s_cbranch_scc1 1b\n"
      PV(R_OB, R_V0, R_SB, 0) PV(R_OB, R_V1, R_SB, 8) "s_nop 7\n s_nop 7\n"
      : "+{v[64:79]}"(oa), "+{v[80:95]}"(ob), "=&{v[32:47]}"(sa), "+{v[48:63]}"(sb), "=&{v[24:27]}"(kf), "+{v[8:11]}"(v0), "+{v[12:15]}"(v1),
        "+{v28}"(ad), [n] "+s"(nit)
      : "{v[96:111]}"(ca), "{v[112:127]}"(cb), "{v[16:19]}"(qa), "{v[20:23]}"(qb), "{v29}"(ix), [msk] "s"(msk)
      : "memory", "scc");
}
DI float max16(const f32x16& s) {
  float a = fmaxf(fmaxf(s[0], s[1]), s[2]), b = fmaxf(fmaxf(s[3], s[4]), s[5]);
  a = fmaxf(fmaxf(a, s[6]), s[7]); b = fmaxf(fmaxf(b, s[8]), s[9]); a = fmaxf(fmaxf(a, s[10]), s[11]); b = fmaxf(fmaxf(b, s[12]), s[13]);
  return fmaxf(fmaxf(a, s[14]), fmaxf(b, s[15]));
}
DI float halfmax(float m) { return fmaxf(m, __shfl_xor(m, 32)); }
DI float gelu_mul(float a, float u) {
  const float c1 = -2.f * 0.7978845608028654f * 1.4426950408889634f, c3 = c1 * 0.044715f;
  const float e = __builtin_amdgcn_exp2f(a * fmaf(a * a, c3, c1));
  return a * u * __builtin_amdgcn_rcpf(1.f + e);
}

__global__ __launch_bounds__((AW + 4) * 64, (AW + 4) / 4) void k2_attn(char* __restrict__ ws) {
  __shared__ __attribute__((aligned(1024))) char smem[131072 + 2048 + 20480];
  const LAS char* lds = (const LAS char*)smem;
  const unsigned lds0 = (unsigned)(uintptr_t)lds;
  const int tid = threadIdx.x, lane = tid & 63, w = rfl(tid >> 6), b = blockIdx.x;
  const int r = lane & 31;
  const float* gs = (const float*)(ws + WS_G);
  if (w < AW) {
    const int head = (b & 7) * 4 + ((b >> 3) & 3), qg = b >> 5;
    constexpr int PER = 128 / AW;
    const char* kv = ws + WS_KV + (size_t)head * 131072 + w * (PER * 1024);
#pragma unroll
    for (int i = 0; i < PER / 4; ++i) {
      const unsigned d = lds0 + w * (PER * 1024) + i * 4096;
      glds<0>(kv + i * 4096, lane * 16, d); glds<1024>(kv + i * 4096, lane * 16, d); glds<2048>(kv + i * 4096, lane * 16, d); glds<3072>(kv + i * 4096, lane * 16, d);
    }
    const int ru0 = qg * 8 + (w & 3) * 2, kh = w >> 2;
    const char* qp = ws + WS_Q + (size_t)(head * 64 + ru0) * 1024 + lane * 16;
    const bf16x8 qa = *(const bf16x8*)qp, qb = *(const bf16x8*)(qp + 1024);
    const float ga = gs[ru0 * 32 + r], gb = gs[ru0 * 32 + 32 + r];
    WAIT_BAR(0);
    constexpr int NT = 64 * 4 / AW;
    const LAS char* kb = lds + kh * (NT * 2048) + lane * 16;
    float ma, mb;
    { const bf16x8 kf = ldsr(kb); const f32x16 z = {}; ma = halfmax(max16(MFMA(kf, qa, z))); mb = halfmax(max16(MFMA(kf, qb, z))); }
    f32x16 oa, ob;
    for (int pass = 0;; ++pass) {
      f32x16 ca, cb;
#pragma unroll
      for (int i = 0; i < 16; ++i) { ca[i] = -ma; cb[i] = -mb; oa[i] = 0.f; ob[i] = 0.f; }
      attn_loop(oa, ob, ca, cb, qa, qb, lds0 + kh * (NT * 2048) + lane * 16, NT / 4);
      float chk = oa[8] + ob[8];
#pragma unroll
      for (int i = 0; i < 8; ++i) chk += (fabsf(oa[i]) + fabsf(ob[i])) * 0x1p-40f;
      if (pass || !__any(!(chk < 0x1p60f))) break;
      for (int t = 1; t < NT; ++t) { const bf16x8 kf = ldsr(kb + t * 2048); const f32x16 z = {}; ma = fmaxf(ma, max16(MFMA(kf, qa, z))); mb = fmaxf(mb, max16(MFMA(kf, qb, z))); }
      ma = halfmax(ma); mb = halfmax(mb);
    }
    char* op = ws + WS_OH + (size_t)(ru0 * KS3 + head) * 1024 + lane * 16;
    if (AW == 8) {
      LAS float* ex = (LAS float*)(lds + 131072 + 2048) + (w & 3) * 1280 + lane;
      const f32x16& give = kh ? oa : ob;
#pragma unroll
      for (int i = 0; i < 9; ++i) ex[kh * 640 + i * 64] = give[i];
      ex[kh * 640 + 576] = kh ? ma : mb;
      asm volatile("s_waitcnt lgkmcnt(0)\n\ts_barrier" ::: "memory");
      const float m1 = kh ? mb : ma, m2 = ex[(kh ^ 1) * 640 + 576], m = fmaxf(m1, m2);
      const float f1 = __builtin_amdgcn_exp2f(m1 - m), f2 = __builtin_amdgcn_exp2f(m2 - m);
      f32x16 o = kh ? ob : oa;
#pragma unroll
      for (int i = 0; i < 9; ++i) o[i] = o[i] * f1 + ex[(kh ^ 1) * 640 + i * 64] * f2;
      *(u32x4*)(op + kh * KS3 * 1024) = pack8(o, 0, (kh ? gb : ga) / o[8]);
    } else {
      *(u32x4*)op = pack8(oa, 0, ga / oa[8]);
      *(u32x4*)(op + KS3 * 1024) = pack8(ob, 0, gb / ob[8]);
    }
  } else {
    asm volatile("s_barrier" ::: "memory");
    const int gwv = w - AW, x8 = b & 7, i32 = b >> 3;
    const int rbk = (x8 >> 1) * 4 + (i32 & 3), cb = (x8 & 1) * 8 + (i32 >> 2);
    const int ru0 = rbk * 4 + (gwv & 1) * 2, u0 = cb * 6 + (gwv >> 1) * 3;
    if (u0 < NU) {
      const __amdgpu_buffer_rsrc_t rs = __builtin_amdgcn_make_buffer_rsrc(ws, 0, 32u << 20, 0x00020000);
      const unsigned xp = WS_X + ru0 * 32768, wp0 = WS_WGU + u0 * 32768, wp2 = WS_WGU + (u0 + 2 < NU ? u0 + 2 : u0) * 32768;
      auto bl = [&](unsigned so) { return __builtin_bit_cast(bf16x8, __builtin_amdgcn_raw_buffer_load_b128(rs, lane * 16, so, 0)); };
      const float g0 = 1.f - gs[ru0 * 32 + r], g1 = 1.f - gs[ru0 * 32 + 32 + r];
      f32x16 acc[2][3] = {};
      constexpr int PD = 2;
      bf16x8 fx[PD + 1][2], fw[PD + 1][3];
      auto ld = [&](int ks, int sl) {
        fx[sl][0] = bl(xp + ks * 1024); fx[sl][1] = bl(xp + 32768 + ks * 1024);
        fw[sl][0] = bl(wp0 + ks * 1024); fw[sl][1] = bl(wp0 + 32768 + ks * 1024); fw[sl][2] = bl(wp2 + ks * 1024);
      };
#pragma unroll
      for (int i = 0; i < PD; ++i) ld(i, i);
#pragma unroll
      for (int ks = 0; ks < 32; ++ks) {
        if (ks + PD < 32) ld(ks + PD, (ks + PD) % (PD + 1));
        SBAR();
        const int sl = ks % (PD + 1);
#pragma unroll
        for (int j = 0; j < 3; ++j)
#pragma unroll
          for (int i = 0; i < 2; ++i) acc[i][j] = MFMA(fw[sl][j], fx[sl][i], acc[i][j]);
        SBAR();
      }
#pragma unroll
      for (int j = 0; j < 3; ++j)
#pragma unroll
        for (int i = 0; i < 2; ++i) {
          f32x16 o;
#pragma unroll
          for (int e = 0; e < 8; ++e) o[e] = gelu_mul(acc[i][j][e], acc[i][j][e + 8]);
          if (u0 + j < NU) *(u32x4*)(ws + WS_OH + (size_t)((ru0 + i) * KS3 + 32 + u0 + j) * 1024 + lane * 16) = pack8(o, 0, i ? g1 : g0);
        }
    }
  }
}

__global__ __launch_bounds__(256, 1) void k3_out(const char* __restrict__ ws, char* __restrict__ out) {
  __shared__ __attribute__((aligned(1024))) char smem[163840];
  const LAS char* lds = (const LAS char*)smem;
  const unsigned lds0 = (unsigned)(uintptr_t)lds;
  const int tid = threadIdx.x, lane = tid & 63, w = rfl(tid >> 6), b = blockIdx.x;
  const int x8 = b & 7, i32 = b >> 3;
  const int rb = (x8 >> 1) * 8 + (i32 & 7), cb = (x8 & 1) * 4 + (i32 >> 3);
  const char* src = (w < 2) ? ws + WS_OH + (size_t)(rb * 2 + w) * KS3 * 1024 : ws + WS_WOD + (size_t)(cb * 2 + w - 2) * KS3 * 1024;
  auto stage = [&](int s) {
    const unsigned d = lds0 + (s % 5) * 32768 + w * 8192;
    const char* p = src + s * 8192;
    glds<0>(p, lane * 16, d); glds<1024>(p, lane * 16, d); glds<2048>(p, lane * 16, d); glds<3072>(p, lane * 16, d);
    glds<0>(p + 4096, lane * 16, d + 4096); glds<1024>(p + 4096, lane * 16, d + 4096);
    if (s < 14) { glds<2048>(p + 4096, lane * 16, d + 4096); glds<3072>(p + 4096, lane * 16, d + 4096); }
  };
  stage(0); stage(1); stage(2); stage(3); stage(4);
  f32x16 acc = {};
  const int ru = w & 1, nu = w >> 1;
#pragma unroll
  for (int s = 0; s < 15; ++s) {
    if (s == 0) WAIT_BAR(32); else if (s <= 10) WAIT_BAR(24); else if (s == 11) WAIT_BAR(22); else if (s == 12) WAIT_BAR(14); else if (s == 13) WAIT_BAR(6); else WAIT_BAR(0);
    if (s >= 1 && s + 4 <= 14) stage(s + 4);
    const LAS char* sb = lds + (s % 5) * 32768 + lane * 16;
#pragma unroll
    for (int ks = 0; ks < (s < 14 ? 8 : 6); ++ks) acc = MFMA(ldsr(sb + (2 + nu) * 8192 + ks * 1024), ldsr(sb + ru * 8192 + ks * 1024), acc);
  }
  char* op = out + (size_t)((rb * 2 + ru) * 32 + (lane & 31)) * 1024 + (cb * 2 + nu) * 64 + (lane >> 5) * 32;
  *(u32x4*)op = pack8(acc, 0, 1.f); *(u32x4*)(op + 16) = pack8(acc, 8, 1.f);
}

extern "C" void kernel_launch(void* const* d_in, const int* in_sizes, int n_in, void* d_out, int out_size, void* d_ws, size_t ws_size,
                              hipStream_t stream) {
  k1_qkv<<<256, 256, 0, stream>>>((const char*)d_in[0], (const char*)d_in[1], (const char*)d_in[2], (const char*)d_in[4], (const char*)d_in[5],
                                  (const char*)d_in[6], (const char*)d_in[7], (char*)d_ws);
  k2_attn<<<256, (AW + 4) * 64, 0, stream>>>((char*)d_ws);
  k3_out<<<256, 256, 0, stream>>>((const char*)d_ws, (char*)d_out);
}
```

```cpp
#include <hip/hip_runtime.h>
#include <hip/hip_bf16.h>
#include <stdint.h>

typedef __bf16 bf16x8 __attribute__((ext_vector_type(8)));
typedef __bf16 bf16x16 __attribute__((ext_vector_type(16)));
typedef __bf16 bf16x2 __attribute__((ext_vector_type(2)));
typedef short s16x4 __attribute__((ext_vector_type(4)));
typedef float f32x16 __attribute__((ext_vector_type(16)));
typedef float f32x8 __attribute__((ext_vector_type(8)));
typedef float f32x4 __attribute__((ext_vector_type(4)));
typedef float f32x2 __attribute__((ext_vector_type(2)));
typedef unsigned u32x4 __attribute__((ext_vector_type(4)));
typedef unsigned u32x2 __attribute__((ext_vector_type(2)));
typedef unsigned long long u64;
#define LAS __attribute__((address_space(3)))
#define DI __device__ __forceinline__
#define MFMA(a, b, c) __builtin_amdgcn_mfma_f32_32x32x16_bf16((a), (b), (c), 0, 0, 0)
#define SBAR() __builtin_amdgcn_sched_barrier(0)
#define STR_(x) #x
#define STR(x) STR_(x)

constexpr int NU = 86;
constexpr unsigned WS_EP = 0;
constexpr unsigned WS_FA = 4096;
constexpr unsigned WS_FB = WS_FA + 32768;
constexpr unsigned WS_FO = 1u << 17;
constexpr unsigned WS_G = 1u << 19;
constexpr unsigned WS_Q = 1u << 20;
constexpr unsigned WS_KV = 3u << 20;
constexpr unsigned WS_O = 7u << 20;
constexpr unsigned WS_H = 9u << 20;
constexpr unsigned WS_END = 15u << 20;
constexpr int SC1 = 16;

#define STAMP_DECL
#define STAMP(i)
#define STAMP_OUT()

DI unsigned cvtpk(float lo, float hi) { f32x2 v = {lo, hi}; bf16x2 b = __builtin_convertvector(v, bf16x2); return __builtin_bit_cast(unsigned, b); }
DI unsigned rfl(unsigned x) { return __builtin_amdgcn_readfirstlane(x); }
template <int IMM> DI void glds(const void* sbase, unsigned voff, unsigned ldsb) {
  asm volatile("s_mov_b32 m0, %2\n\ts_nop 0\n\tglobal_load_lds_dwordx4 %0, %1 offset:%3" ::"v"(voff), "s"(sbase), "s"(ldsb), "i"(IMM) : "memory");
}
DI void glds4_sc1(const void* sbase, unsigned voff, unsigned ldsb) {
  asm volatile("s_mov_b32 m0, %2\n\ts_nop 0\n\tglobal_load_lds_dwordx4 %0, %1 sc1\n\tglobal_load_lds_dwordx4 %0, %1 offset:1024 sc1\n\t"
               "global_load_lds_dwordx4 %0, %1 offset:2048 sc1\n\tglobal_load_lds_dwordx4 %0, %1 offset:3072 sc1" ::"v"(voff), "s"(sbase), "s"(ldsb) : "memory");
}
template <int N> DI void wait_vm() { asm volatile("s_waitcnt vmcnt(%0)" ::"i"(N) : "memory"); }
template <int N> DI void wait_bar() { asm volatile("s_waitcnt vmcnt(%0)\n\ts_barrier" ::"i"(N) : "memory"); }
template <int N> DI void wait_bar0() { asm volatile("s_waitcnt vmcnt(%0) lgkmcnt(0)\n\ts_barrier" ::"i"(N) : "memory"); }
DI void bar() { asm volatile("s_waitcnt lgkmcnt(0)\n\ts_barrier" ::: "memory"); }
DI bf16x8 ldsr(const LAS char* p) { return *(const LAS bf16x8*)p; }
DI bf16x8 ldstr(const LAS char* p0, const LAS char* p1) {
  s16x4 a = __builtin_amdgcn_ds_read_tr16_b64_v4i16((LAS s16x4*)p0), b = __builtin_amdgcn_ds_read_tr16_b64_v4i16((LAS s16x4*)p1);
  return __builtin_bit_cast(bf16x8, __builtin_shufflevector(a, b, 0, 1, 2, 3, 4, 5, 6, 7));
}
DI u32x4 pack8(const f32x16& a, int o, float s) {
  return (u32x4){cvtpk(a[o] * s, a[o + 1] * s), cvtpk(a[o + 2] * s, a[o + 3] * s), cvtpk(a[o + 4] * s, a[o + 5] * s), cvtpk(a[o + 6] * s, a[o + 7] * s)};
}
DI void st16_sc1(u32x4 v, const char* sbase, unsigned voff) { asm volatile("global_store_dwordx4 %0, %1, %2 sc1\n\ts_nop 1" ::"v"(voff), "v"(v), "s"(sbase) : "memory"); }
DI void flag_issue(u32x2& v, const char* ws, unsigned off) { asm volatile("global_load_dwordx2 %0, %1, %2 sc1" : "=v"(v) : "v"(off), "s"(ws) : "memory"); }
template <int N> DI void flag_wait(u32x2& v, const char* ws, unsigned off, u32x2 tag) {
  asm volatile("s_waitcnt vmcnt(%1)" : "+v"(v) : "i"(N) : "memory");
  for (int spin = 0; spin < (1 << 17); ++spin) {
    if (!__any(v[0] != tag[0] || v[1] != tag[1])) break;
    asm volatile("s_sleep 2\n\tglobal_load_dwordx2 %0, %1, %2 sc1\n\ts_waitcnt vmcnt(0)" : "=v"(v) : "v"(off), "s"(ws) : "memory");
  }
}

#define R_V0 8
#define R_V1 12
#define R_Q 16
#define R_K 20
#define R_AD 24
#define R_IX 25
#define R_SA 32
#define R_SB 48
#define R_O 64
#define R_C 80
#define VR(b, o, n) "v[" STR(b) "+" #o ":" STR(b) "+" #o "+" #n "-1]"
#define EX(b, i) "v_exp_f32 v[" STR(b) "+" #i "], v[" STR(b) "+" #i "]\n"
#define PV(v, s, i) "v_smfmac_f32_32x32x32_bf16 " VR(R_O, 0, 16) ", " VR(v, 0, 4) ", " VR(s, i, 8) ", v" STR(R_IX) "\n"
#define QK(s) "v_mfma_f32_32x32x16_bf16 " VR(s, 0, 16) ", " VR(R_K, 0, 4) ", " VR(R_Q, 0, 4) ", " VR(R_C, 0, 16) "\n"
#define DSR(d, off) "ds_read_b128 " VR(d, 0, 4) ", v" STR(R_AD) " offset:" off "\n"
#define SEG(cur, prv, o)                                                                                                              \
  PV(R_V0, prv, 0) EX(cur, 0) EX(cur, 1) EX(cur, 2) PV(R_V1, prv, 8)                                                                  \
  "s_mov_b64 exec, %[msk]\n" DSR(R_V0, #o "+1024") DSR(R_V1, #o "+1280") "s_mov_b64 exec, -1\n"                                       \
  EX(cur, 3) EX(cur, 4) "s_waitcnt lgkmcnt(2)\n" QK(prv) DSR(R_K, #o "+4096")                                                         \
  EX(cur, 5) EX(cur, 6) EX(cur, 7) EX(cur, 8) EX(cur, 9) EX(cur, 10) EX(cur, 11) EX(cur, 12) EX(cur, 13) EX(cur, 14) EX(cur, 15)      \
  "s_waitcnt lgkmcnt(1)\n"
struct AttnState { f32x16 o, sa, sb, c; u32x4 k, v0, v1; bf16x8 q; unsigned ad; };
#define ATTN_OPS(S)                                                                                                                   \
  : "+{v[64:79]}"(S.o), "+{v[32:47]}"(S.sa), "+{v[48:63]}"(S.sb), "+{v[20:23]}"(S.k), "+{v[8:11]}"(S.v0), "+{v[12:15]}"(S.v1), "+{v24}"(S.ad), [n] "+s"(nit) \
  : "{v[80:95]}"(S.c), "{v[16:19]}"(S.q), "{v25}"(ix), [msk] "s"(msk), [old] "s"(old)                                                  \
  : "memory", "scc"
DI void attn_16tiles(AttnState& S, int old) {
  unsigned ix = 0xddddddddu; u64 msk = 0x0000ffff0000ffffull; int nit = 2;
  asm volatile("s_nop 1\n1:\n s_cmp_eq_u32 %[old], 0\n s_cbranch_scc1 2f\n s_setprio 2\n2:\n"
               SEG(R_SA, R_SB, 0) SEG(R_SB, R_SA, 2048) SEG(R_SA, R_SB, 4096) SEG(R_SB, R_SA, 6144) SEG(R_SA, R_SB, 8192) SEG(R_SB, R_SA, 10240)
               "s_cbranch_scc1 3f\n s_setprio 0\n3:\n"
               SEG(R_SA, R_SB, 12288) SEG(R_SB, R_SA, 14336)
               "v_add_u32 v" STR(R_AD) ", 0x4000, v" STR(R_AD) "\n s_sub_u32 %[n], %[n], 1\n s_cmp_lg_u32 %[n], 0\n s_cbranch_scc1 1b\n"
               "s_waitcnt lgkmcnt(0)\n" ATTN_OPS(S));
}
DI void attn_begin(AttnState& S) {
  unsigned ix = 0xddddddddu; u64 msk = 0; int nit = 0, old = 0;
  asm volatile("s_nop 1\n" QK(R_SA) DSR(R_K, "2048") "s_waitcnt lgkmcnt(0)\n s_nop 7\n s_nop 1\n" ATTN_OPS(S));
}
DI void attn_end(AttnState& S) {
  unsigned ix = 0xddddddddu; u64 msk = 0; int nit = 0, old = 0;
  asm volatile("s_nop 1\n" PV(R_V0, R_SB, 0) PV(R_V1, R_SB, 8) "s_nop 7\n s_nop 4\n" ATTN_OPS(S));
}
DI float max16(const f32x16& s) {
  float a = fmaxf(fmaxf(s[0], s[1]), s[2]), b = fmaxf(fmaxf(s[3], s[4]), s[5]);
  a = fmaxf(fmaxf(a, s[6]), s[7]); b = fmaxf(fmaxf(b, s[8]), s[9]); a = fmaxf(fmaxf(a, s[10]), s[11]); b = fmaxf(fmaxf(b, s[12]), s[13]);
  return fmaxf(fmaxf(a, s[14]), fmaxf(b, s[15]));
}
DI float halfmax(float m) {
  auto r = __builtin_amdgcn_permlane32_swap(__float_as_uint(m), __float_as_uint(m), false, false);
  return fmaxf(__uint_as_float(r[0]), __uint_as_float(r[1]));
}
DI float gelu_mul(float a, float u) {
  const float c1 = -2.f * 0.7978845608028654f * 1.4426950408889634f, c3 = c1 * 0.044715f;
  const float e = __builtin_amdgcn_exp2f(a * fmaf(a * a, c3, c1));
  return a * u * __builtin_amdgcn_rcpf(1.f + e);
}

constexpr unsigned slotAB(int t) { return 20480u + ((t + 4) % 5) * 28672u; }
constexpr unsigned slotD(int d) { return d == 0 || d == 5 ? 131072u : (d >= 6 ? (d - 6) : (d - 1)) * 32768u; }

__global__ __launch_bounds__(512, 2) void fused_block(const char* __restrict__ x, const char* __restrict__ wqkv, const char* __restrict__ wo,
                                                      const char* __restrict__ wg, const char* __restrict__ wu, const char* __restrict__ wd,
                                                      const char* __restrict__ gw, char* __restrict__ out, char* ws) {
  __shared__ __attribute__((aligned(1024))) char smem[163840];
  const LAS char* lds = (const LAS char*)smem;
  const unsigned lds0 = (unsigned)(uintptr_t)lds;
  const int tid = threadIdx.x, lane = tid & 63, w = rfl(tid >> 6), b = blockIdx.x, x8 = b & 7, i32 = b >> 3;
  const int r = lane & 31, h = lane >> 5, tq = (lane >> 2) & 3, tp = lane & 3, tc = (lane >> 4) & 1;
  const __amdgpu_buffer_rsrc_t rs = __builtin_amdgcn_make_buffer_rsrc(ws, 0, WS_END, 0x00020000);
  STAMP_DECL; STAMP(0);

  const int rb = (x8 >> 2) * 8 + (i32 & 7), cb = (x8 & 3) * 4 + (i32 >> 3);
  const int ru = w & 3, cls = w >> 2, rug = rb * 4 + ru;
  const int head = x8 * 4 + (i32 & 3), qg = i32 >> 2;
  const unsigned vx0 = (16 * w + (lane >> 3)) * 1024 + (((lane & 7) ^ (lane >> 4)) << 4);
  const unsigned vx1 = (16 * w + 8 + (lane >> 3)) * 1024 + (((lane & 7) ^ (4 + (lane >> 4))) << 4);
  unsigned vw[3], vf[3];
#pragma unroll
  for (int n = 0; n < 3; ++n) {
    const unsigned c = 64 * n + lane, row = (c * 5462) >> 16, ch = c - row * 12;
    vw[n] = (16 * ru + row) * 3072 + ch * 16;
    vf[n] = (16 * ru + row) * 2752 + (cb >= 14 ? (ch & 3) : ch) * 16;
  }
  const char* xb = x + (size_t)rb * 131072;
  const char* wb = wqkv + cb * 192;
  const char* fb = (cls ? wu : wg) + (cb < 14 ? cb : 14) * 192;
  auto issue = [&](int t) {
    const unsigned d = lds0 + slotAB(t);
    if (t < 8) {
      glds<0>(xb + t * 128, vx0, d + w * 2048); glds<0>(xb + t * 128, vx1, d + w * 2048 + 1024);
      if (cls) {
#pragma unroll
        for (int n = 0; n < 3; ++n) glds<0>(wb + t * 64 * 3072, vw[n], d + 16384 + ru * 3072 + n * 1024);
      }
    } else {
#pragma unroll
      for (int n = 0; n < 3; ++n) glds<0>(fb + (t - 8) * 64 * 2752, vf[n], d + cls * 12320 + ru * 3072 + n * 1024);
    }
  };
#pragma unroll
  for (int t = 0; t < 5; ++t) issue(t);
  u32x2 tag;
  {
    u64 e; asm volatile("s_load_dwordx2 %0, %1, 0x0\n\ts_waitcnt lgkmcnt(0)" : "=s"(e) : "s"(ws) : "memory");
    e = (e ^ (e >> 29)) * 0x9E3779B97F4A7C15ull + 0x632BE59BD9B4E019ull;
    tag = (u32x2){(unsigned)e, (unsigned)(e >> 32)};
  }
  bf16x8 xf[32];
  const unsigned xo = (32 * ru + r) * 128 + ((h ^ ((r >> 1) & 7)) << 4);
  {
    f32x16 a0 = {}, a1 = {};
    const int u0 = 3 * cb + 2 * cls;
    const bool isv = u0 >= 32;
    const unsigned wof = 16384 + (8 * h + tq) * 192 + cls * 128 + tc * 32 + tp * 8;
#pragma unroll
    for (int t = 0; t < 8; ++t) {
      if (cls) { if (t == 0) wait_vm<20>(); else if (t <= 4) wait_vm<15>(); else if (t == 5) wait_vm<13>(); else if (t == 6) wait_vm<11>(); else wait_vm<9>(); }
      else { if (t == 0) wait_vm<8>(); else if (t <= 4) wait_vm<6>(); else if (t == 5) wait_vm<7>(); else if (t == 6) wait_vm<8>(); else wait_vm<9>(); }
      bar();
      if (t == 0) STAMP(1);
      if (t >= 1) issue(t + 4);
      const LAS char* sb = lds + slotAB(t);
#pragma unroll
      for (int ks = 0; ks < 4; ++ks) xf[4 * t + ks] = ldsr(sb + (xo ^ (ks * 32)));
      bf16x8 f0[4], f1[4];
#pragma unroll
      for (int ks = 0; ks < 4; ++ks) f0[ks] = ldstr(sb + wof + ks * 3072, sb + wof + ks * 3072 + 768);
      if (!cls) {
#pragma unroll
        for (int ks = 0; ks < 4; ++ks) f1[ks] = ldstr(sb + wof + ks * 3072 + 64, sb + wof + ks * 3072 + 768 + 64);
        if (isv) {
#pragma unroll
          for (int ks = 0; ks < 4; ++ks) { a0 = MFMA(xf[4 * t + ks], f0[ks], a0); a1 = MFMA(xf[4 * t + ks], f1[ks], a1); }
        } else {
#pragma unroll
          for (int ks = 0; ks < 4; ++ks) { a0 = MFMA(f0[ks], xf[4 * t + ks], a0); a1 = MFMA(f1[ks], xf[4 * t + ks], a1); }
        }
      } else if (isv) {
#pragma unroll
        for (int ks = 0; ks < 4; ++ks) a0 = MFMA(xf[4 * t + ks], f0[ks], a0);
      } else {
#pragma unroll
        for (int ks = 0; ks < 4; ++ks) a0 = MFMA(f0[ks], xf[4 * t + ks], a0);
      }
    }
    STAMP(2);
    auto put = [&](const f32x16& a, int u) {
      const int hd = (u & 15) * 2;
      unsigned vo, so, d2; float sc = 1.f;
      if (u < 16) { vo = lane * 16; so = WS_Q + (hd * 64 + rug) * 1024; d2 = 65536; sc = 0.25f * 1.4426950408889634f; }
      else if (u < 32) { vo = h * 512 + ((r & 19) | ((r & 4) << 1) | ((r & 8) >> 1)) * 16; so = WS_KV + (hd * 64 + rug) * 2048; d2 = 131072; }
      else { vo = (r >> 4) * 131072 + h * 512 + (r & 15) * 16; so = WS_KV + (hd * 64 + rug) * 2048 + 1024; d2 = 256; }
      st16_sc1(pack8(a, 0, sc), ws + so, vo);
      st16_sc1(pack8(a, 8, sc), ws + so + d2, vo);
    };
    put(a0, u0);
    if (!cls) put(a1, u0 + 1);
  }
  STAMP(3);
  AttnState S;
  const char* kvg = ws + WS_KV + head * 131072;
  {
    const int u0 = 6 * cb + 3 * cls;
    const bool act = u0 < NU;
    const unsigned fof = tc * 12320 + (8 * h + tq) * 192 + cls * 96 + tp * 8;
    f32x16 acc[3] = {};
    bf16x8 f[2][3];
    u32x2 fl;
    const unsigned fao = WS_FA + ((lane < 32 ? (lane & 15) : 2 * qg + (lane & 1)) * 16 + ((lane < 32 ? 16 + (lane & 16) : 0) + (head >> 1)) / 3) * 128;
    auto rd = [&](int g) {
      const LAS char* p = lds + slotAB(8 + (g >> 2)) + fof + (g & 3) * 3072;
#pragma unroll
      for (int e = 0; e < 3; ++e) f[g & 1][e] = ldstr(p + e * 32, p + e * 32 + 768);
    };
#pragma unroll
    for (int t = 8; t < 16; ++t) {
      if (t <= 9) { if (cls) wait_bar<8>(); else wait_bar<10>(); }
      else if (t <= 12) wait_bar<6>();
      else if (t == 13) { if (w == 0) flag_wait<0>(fl, ws, fao, tag); wait_bar<3>(); }
      else if (t == 14) wait_bar<9>();
      else wait_bar<4>();
      if (t <= 11) issue(t + 4);
      if (t == 10 && tid == 0) __builtin_amdgcn_raw_buffer_store_b64(tag, rs, 0, WS_FA + (rb * 16 + cb) * 128, SC1);
      if (t == 12 && w == 0) flag_issue(fl, ws, fao);
      if (t == 13) {
        asm volatile("global_load_dwordx4 %0, %1, %2 sc1" : "=v"(S.q) : "v"(lane * 16), "s"(ws + WS_Q + (head * 64 + qg * 8 + w) * 1024) : "memory");
        glds4_sc1(kvg + w * 4096, lane * 16, lds0 + w * 4096);
      }
      if (t >= 13) glds4_sc1(kvg + (t - 12) * 32768 + w * 4096, lane * 16, lds0 + (t - 12) * 32768 + w * 4096);
      if (t == 15) asm volatile("" : "+v"(S.q));
      if (act) {
        if (t == 8) rd(0);
#pragma unroll
        for (int ks = 0; ks < 4; ++ks) {
          const int g = (t - 8) * 4 + ks;
          if (g + 1 < 32) rd(g + 1);
          SBAR();
#pragma unroll
          for (int e = 0; e < 3; ++e) acc[e] = MFMA(f[g & 1][e], xf[g], acc[e]);
          SBAR();
        }
      }
    }
    STAMP(4);
    if (act) {
#pragma unroll
      for (int e = 0; e < 3; ++e) {
        f32x16 o;
#pragma unroll
        for (int j = 0; j < 8; ++j) o[j] = gelu_mul(acc[e][j], acc[e][j + 8]);
        if (u0 + e < NU) st16_sc1(pack8(o, 0, 1.f), ws + WS_H + (rug * NU + u0 + e) * 1024, lane * 16);
      }
    } else if (cb == 15 && !cls) {
      f32x16 z = {};
#pragma unroll
      for (int g = 0; g < 32; ++g) z = MFMA(*(const bf16x8*)(gw + g * 32 + h * 16), xf[g], z);
      if (h == 0) __builtin_amdgcn_raw_buffer_store_b32(__float_as_uint(1.f / (1.f + __expf(-z[0]))), rs, r * 4, WS_G + rug * 128, SC1);
    }
  }
  STAMP(5);
  const int rb2 = x8 * 4 + (i32 & 3), cb2 = i32 >> 2, ru2 = rb2 * 2 + (w & 1), kq = w >> 1;
  const unsigned vd = (lane >> 3) * 1024 + (((lane & 7) ^ (((lane >> 4) & 1) << 2)) << 4);
  auto issueD = [&](int d) {
    const unsigned dst = lds0 + slotD(d) + kq * 8192;
    if (d < 5) {
      const char* p = wd + cb2 * 128 + (kq * 22 + d * 4 + (w & 1) * 2) * 16384;
#pragma unroll
      for (int n = 0; n < 4; ++n) glds<0>(p + n * 8192, vd, dst + (w & 1) * 4096 + n * 1024);
    } else if (d == 5) {
      const char* p = wd + cb2 * 128 + (kq < 3 ? kq * 22 + 20 + (w & 1) : 0) * 16384;
#pragma unroll
      for (int n = 0; n < 2; ++n) glds<0>(p + n * 8192, vd, dst + (w & 1) * 2048 + n * 1024);
    } else {
      const char* p = wo + cb2 * 128 + (kq * 8 + (d - 6) * 4 + (w & 1) * 2) * 16384;
#pragma unroll
      for (int n = 0; n < 4; ++n) glds<0>(p + n * 8192, vd, dst + (w & 1) * 4096 + n * 1024);
    }
  };
  float mref;
  {
    S.ad = lds0 + lane * 16;
    const bf16x8 k0 = ldsr(lds + lane * 16);
    const f32x16 z = {};
    mref = halfmax(max16(MFMA(k0, S.q, z)));
    const unsigned one = ((lane & 27) == 16) ? 0x3f803f80u : 0u;
    S.k = __builtin_bit_cast(u32x4, k0); S.v0 = (u32x4){one, one, one, one}; S.v1 = S.v0;
#pragma unroll
    for (int j = 0; j < 16; ++j) { S.c[j] = -mref; S.o[j] = 0.f; S.sb[j] = 0.f; S.sa[j] = 0.f; }
  }
  if (cls) __builtin_amdgcn_s_setprio(1);
  attn_begin(S);
  STAMP(6);
  u32x2 flb;
  bf16x8 hf[22];
  float gate;
  const unsigned fbo = (b == 0 && lane >= 32) ? WS_FA + (w * 32 + lane - 32) * 128 : WS_FB + ((rb2 >> 1) * 16 + (lane & 15)) * 128;
  const int old = 1 - cls;
  attn_16tiles(S, old);
  wait_bar<0>();
  if (tid == 0) __builtin_amdgcn_raw_buffer_store_b64(tag, rs, 0, WS_FB + (rb * 16 + cb) * 128, SC1);
  issueD(0); issueD(1);
  STAMP(7);
  attn_16tiles(S, old);
  bar();
  issueD(2);
  flag_issue(flb, ws, fbo);
  attn_16tiles(S, old);
  bar();
  flag_wait<0>(flb, ws, fbo, tag);
  issueD(3);
#pragma unroll
  for (int j = 0; j < 22; ++j)
    hf[j] = __builtin_bit_cast(bf16x8, __builtin_amdgcn_raw_buffer_load_b128(rs, lane * 16, WS_H + (ru2 * NU + (kq * 22 + j < NU ? kq * 22 + j : 0)) * 1024, SC1));
  gate = __uint_as_float(__builtin_amdgcn_raw_buffer_load_b32(rs, r * 4, WS_G + ru2 * 128, SC1));
  attn_16tiles(S, old);
  attn_end(S);
  __builtin_amdgcn_s_setprio(0);
  STAMP(8);
  {
    float chk = S.o[8];
#pragma unroll
    for (int j = 0; j < 8; ++j) chk += fabsf(S.o[j]) * 0x1p-40f;
    if (__builtin_expect(__any(!(chk < 0x1p60f)), 0)) {
      const f32x16 z = {};
      const unsigned kvo = WS_KV + head * 131072;
#pragma unroll 1
      for (int t = 1; t < 64; ++t)
        mref = fmaxf(mref, max16(MFMA(__builtin_bit_cast(bf16x8, __builtin_amdgcn_raw_buffer_load_b128(rs, lane * 16, kvo + t * 2048, SC1)), S.q, z)));
      mref = halfmax(mref);
      const unsigned one = ((lane & 27) == 16) ? 0x3f803f80u : 0u;
      f32x16 c, o = {};
#pragma unroll
      for (int j = 0; j < 16; ++j) c[j] = -mref;
#pragma unroll 1
      for (int t = 0; t < 64; ++t) {
        f32x16 s = MFMA(__builtin_bit_cast(bf16x8, __builtin_amdgcn_raw_buffer_load_b128(rs, lane * 16, kvo + t * 2048, SC1)), S.q, c);
        u32x4 v0 = __builtin_amdgcn_raw_buffer_load_b128(rs, (lane & 47) * 16, kvo + t * 2048 + 1024, SC1);
        u32x4 v1 = __builtin_amdgcn_raw_buffer_load_b128(rs, (lane & 47) * 16, kvo + t * 2048 + 1280, SC1);
        if (lane & 16) { v0 = (u32x4){one, one, one, one}; v1 = v0; }
#pragma unroll
        for (int j = 0; j < 16; ++j) s[j] = __builtin_amdgcn_exp2f(s[j]);
        const f32x8 p0 = __builtin_shufflevector(s, s, 0, 1, 2, 3, 4, 5, 6, 7), p1 = __builtin_shufflevector(s, s, 8, 9, 10, 11, 12, 13, 14, 15);
        o = __builtin_amdgcn_smfmac_f32_32x32x32_bf16(__builtin_bit_cast(bf16x8, v0), __builtin_bit_cast(bf16x16, p0), o, 0xdddd, 0, 0);
        o = __builtin_amdgcn_smfmac_f32_32x32x32_bf16(__builtin_bit_cast(bf16x8, v1), __builtin_bit_cast(bf16x16, p1), o, 0xdddd, 0, 0);
      }
      S.o = o;
    }
#pragma unroll
    for (int j = 0; j < 22; ++j) asm volatile("" : "+v"(hf[j]));
    asm volatile("" : "+v"(gate));
    st16_sc1(pack8(S.o, 0, 1.f / S.o[8]), ws + WS_O + ((qg * 8 + w) * 32 + head) * 1024, lane * 16);
  }
  const unsigned dof = kq * 8192 + (4 * h + tq) * 128 + (((tc * 2 + (tp >> 1)) ^ ((tq >> 1) << 2)) << 4) + (tp & 1) * 8;
  f32x16 bb[2] = {}, aa[2] = {};
  auto stageD = [&](int d, int n, const bf16x8* act, f32x16* acc) {
    const LAS char* p = lds + slotD(d);
#pragma unroll
    for (int ks = 0; ks < n; ++ks) {
      acc[0] = MFMA(ldstr(p + dof + ks * 2048, p + dof + ks * 2048 + 1024), act[ks], acc[0]);
      acc[1] = MFMA(ldstr(p + (dof ^ 64) + ks * 2048, p + (dof ^ 64) + ks * 2048 + 1024), act[ks], acc[1]);
    }
  };
  wait_bar0<1>();
  STAMP(9);
  issueD(4);
  stageD(0, 4, hf, bb);
  wait_vm<4>();
  if (lane == 0) __builtin_amdgcn_raw_buffer_store_b64(tag, rs, 0, WS_FO + ((qg * 8 + w) * 32 + head) * 128, SC1);
  bar(); issueD(5);
  stageD(1, 4, hf + 4, bb);
  bar(); issueD(6);
  stageD(2, 4, hf + 8, bb);
  bar(); issueD(7);
  u32x2 flo;
  const unsigned foo = WS_FO + (ru2 * 32 + kq * 8 + (lane & 7)) * 128;
  flag_issue(flo, ws, foo);
  stageD(3, 4, hf + 12, bb);
  wait_bar0<9>();
  stageD(4, 4, hf + 16, bb);
  STAMP(10);
  flag_wait<0>(flo, ws, foo, tag);
  STAMP(11);
  bf16x8 of[8];
#pragma unroll
  for (int j = 0; j < 8; ++j) of[j] = __builtin_bit_cast(bf16x8, __builtin_amdgcn_raw_buffer_load_b128(rs, lane * 16, WS_O + (ru2 * 32 + kq * 8 + j) * 1024, SC1));
  SBAR();
  if (kq < 3) stageD(5, 2, hf + 20, bb);
  bar();
  stageD(6, 4, of, aa);
  stageD(7, 4, of + 4, aa);
  STAMP(12);
  {
    LAS char* red = (LAS char*)lds + 65536;
    LAS char* pw = red + (w * 32 + r) * 272 + h * 16;
#pragma unroll
    for (int n = 0; n < 2; ++n)
#pragma unroll
      for (int g = 0; g < 4; ++g) {
        f32x4 v;
#pragma unroll
        for (int j = 0; j < 4; ++j) v[j] = fmaf(gate, aa[n][4 * g + j] - bb[n][4 * g + j], bb[n][4 * g + j]);
        *(LAS f32x4*)(pw + n * 128 + g * 32) = v;
      }
    asm volatile("s_waitcnt lgkmcnt(0)\n\ts_barrier" ::: "memory");
    const int tok = w * 8 + (lane >> 3);
    const LAS char* pr = red + ((tok >> 5) * 32 + (tok & 31)) * 272 + (lane & 7) * 32;
    f32x4 s0 = {}, s1 = {};
#pragma unroll
    for (int q = 0; q < 4; ++q) { s0 += *(const LAS f32x4*)(pr + q * 64 * 272); s1 += *(const LAS f32x4*)(pr + q * 64 * 272 + 16); }
    *(u32x4*)(out + (size_t)(rb2 * 64 + tok) * 1024 + cb2 * 128 + (lane & 7) * 16) =
        (u32x4){cvtpk(s0[0], s0[1]), cvtpk(s0[2], s0[3]), cvtpk(s1[0], s1[1]), cvtpk(s1[2], s1[3])};
  }
  if (b == 0 && tid == 0) *(u32x2*)(ws + WS_EP) = tag;
  STAMP(13);
}

extern "C" void kernel_launch(void* const* d_in, const int* in_sizes, int n_in, void* d_out, int out_size, void* d_ws, size_t ws_size,
                              hipStream_t stream) {
  fused_block<<<256, 512, 0, stream>>>((const char*)d_in[0], (const char*)d_in[1], (const char*)d_in[2], (const char*)d_in[4], (const char*)d_in[5],
                                       (const char*)d_in[6], (const char*)d_in[7], (char*)d_out, (char*)d_ws);
}
```

```cpp
#include <hip/hip_runtime.h>
#include <hip/hip_bf16.h>
#include <stdint.h>
#include <type_traits>
#define ROT (((i32 & 7) + 2 * (i32 >> 3) + (x8 & 1)) & 7)

typedef __bf16 bf16x8 __attribute__((ext_vector_type(8)));
typedef __bf16 bf16x16 __attribute__((ext_vector_type(16)));
typedef __bf16 bf16x2 __attribute__((ext_vector_type(2)));
typedef short s16x4 __attribute__((ext_vector_type(4)));
typedef float f32x16 __attribute__((ext_vector_type(16)));
typedef float f32x8 __attribute__((ext_vector_type(8)));
typedef float f32x4 __attribute__((ext_vector_type(4)));
typedef float f32x2 __attribute__((ext_vector_type(2)));
typedef unsigned u32x4 __attribute__((ext_vector_type(4)));
typedef unsigned u32x2 __attribute__((ext_vector_type(2)));
typedef unsigned long long u64;
#define LAS __attribute__((address_space(3)))
#define DI __device__ __forceinline__
#define MFMA(a, b, c) __builtin_amdgcn_mfma_f32_32x32x16_bf16((a), (b), (c), 0, 0, 0)
#define SBAR() __builtin_amdgcn_sched_barrier(0)
#define STR_(x) #x
#define STR(x) STR_(x)

constexpr int NU = 86;
constexpr unsigned WS_EP = 0;
constexpr unsigned WS_FA = 4096;
constexpr unsigned WS_FB = WS_FA + 32768;
constexpr unsigned WS_FO = 1u << 17;
constexpr unsigned WS_G = 1u << 19;
constexpr unsigned WS_Q = 1u << 20;
constexpr unsigned WS_KV = 3u << 20;
constexpr unsigned WS_O = 7u << 20;
constexpr unsigned WS_H = 9u << 20;
constexpr unsigned WS_END = 15u << 20;
constexpr int SC1 = 16;

#define STAMP_DECL
#define STAMP(i)
#define STAMP_OUT()

DI unsigned cvtpk(float lo, float hi) { f32x2 v = {lo, hi}; bf16x2 b = __builtin_convertvector(v, bf16x2); return __builtin_bit_cast(unsigned, b); }
DI unsigned rfl(unsigned x) { return __builtin_amdgcn_readfirstlane(x); }
template <int IMM> DI void glds(const void* sbase, unsigned voff, unsigned ldsb) {
  asm volatile("s_mov_b32 m0, %2\n\ts_nop 0\n\tglobal_load_lds_dwordx4 %0, %1 offset:%3" ::"v"(voff), "s"(sbase), "s"(ldsb), "i"(IMM) : "memory");
}
DI void glds4_sc1(const void* sbase, unsigned voff, unsigned ldsb) {
  asm volatile("s_mov_b32 m0, %2\n\ts_nop 0\n\tglobal_load_lds_dwordx4 %0, %1 sc1\n\tglobal_load_lds_dwordx4 %0, %1 offset:1024 sc1\n\t"
               "global_load_lds_dwordx4 %0, %1 offset:2048 sc1\n\tglobal_load_lds_dwordx4 %0, %1 offset:3072 sc1" ::"v"(voff), "s"(sbase), "s"(ldsb) : "memory");
}
template <int N> DI void wait_vm() { asm volatile("s_waitcnt vmcnt(%0)" ::"i"(N) : "memory"); }
template <int N> DI void wait_bar() { asm volatile("s_waitcnt vmcnt(%0)\n\ts_barrier" ::"i"(N) : "memory"); }
template <int N> DI void wait_bar0() { asm volatile("s_waitcnt vmcnt(%0) lgkmcnt(0)\n\ts_barrier" ::"i"(N) : "memory"); }
DI void bar() { asm volatile("s_waitcnt lgkmcnt(0)\n\ts_barrier" ::: "memory"); }
DI bf16x8 ldsr(const LAS char* p) { return *(const LAS bf16x8*)p; }
DI bf16x8 ldstr(const LAS char* p0, const LAS char* p1) {
  s16x4 a = __builtin_amdgcn_ds_read_tr16_b64_v4i16((LAS s16x4*)p0), b = __builtin_amdgcn_ds_read_tr16_b64_v4i16((LAS s16x4*)p1);
  return __builtin_bit_cast(bf16x8, __builtin_shufflevector(a, b, 0, 1, 2, 3, 4, 5, 6, 7));
}
DI u32x4 pack8(const f32x16& a, int o, float s) {
  return (u32x4){cvtpk(a[o] * s, a[o + 1] * s), cvtpk(a[o + 2] * s, a[o + 3] * s), cvtpk(a[o + 4] * s, a[o + 5] * s), cvtpk(a[o + 6] * s, a[o + 7] * s)};
}
DI void st16_sc1(u32x4 v, const char* sbase, unsigned voff) { asm volatile("global_store_dwordx4 %0, %1, %2 sc1\n\ts_nop 1" ::"v"(voff), "v"(v), "s"(sbase) : "memory"); }
DI void flag_issue(u32x2& v, const char* ws, unsigned off) { asm volatile("global_load_dwordx2 %0, %1, %2 sc1" : "=v"(v) : "v"(off), "s"(ws) : "memory"); }
template <int N> DI void flag_wait(u32x2& v, const char* ws, unsigned off, u32x2 tag) {
  asm volatile("s_waitcnt vmcnt(%1)" : "+v"(v) : "i"(N) : "memory");
  for (int spin = 0; spin < (1 << 17); ++spin) {
    if (!__any(v[0] != tag[0] || v[1] != tag[1])) break;
    asm volatile("s_sleep 2\n\tglobal_load_dwordx2 %0, %1, %2 sc1\n\ts_waitcnt vmcnt(0)" : "=v"(v) : "v"(off), "s"(ws) : "memory");
  }
}

#define R_V0 8
#define R_V1 12
#define R_Q 16
#define R_K 20
#define R_AD 24
#define R_IX 25
#define R_SA 32
#define R_SB 48
#define R_O 64
#define R_C 80
#define VR(b, o, n) "v[" STR(b) "+" #o ":" STR(b) "+" #o "+" #n "-1]"
#define EX(b, i) "v_exp_f32 v[" STR(b) "+" #i "], v[" STR(b) "+" #i "]\n"
#define PV(v, s, i) "v_smfmac_f32_32x32x32_bf16 " VR(R_O, 0, 16) ", " VR(v, 0, 4) ", " VR(s, i, 8) ", v" STR(R_IX) "\n"
#define QK(s) "v_mfma_f32_32x32x16_bf16 " VR(s, 0, 16) ", " VR(R_K, 0, 4) ", " VR(R_Q, 0, 4) ", " VR(R_C, 0, 16) "\n"
#define DSR(d, off) "ds_read_b128 " VR(d, 0, 4) ", v" STR(R_AD) " offset:" off "\n"
#define SEG(cur, prv, o)                                                                                                              \
  PV(R_V0, prv, 0) EX(cur, 0) EX(cur, 1) EX(cur, 2) PV(R_V1, prv, 8)                                                                  \
  "s_mov_b64 exec, s[98:99]\n" DSR(R_V0, #o "+1024") DSR(R_V1, #o "+1280") "s_mov_b64 exec, -1\n"                                       \
  EX(cur, 3) EX(cur, 4) "s_waitcnt lgkmcnt(2)\n" QK(prv) DSR(R_K, #o "+4096")                                                         \
  EX(cur, 5) EX(cur, 6) EX(cur, 7) EX(cur, 8) EX(cur, 9) EX(cur, 10) EX(cur, 11) EX(cur, 12) EX(cur, 13) EX(cur, 14) EX(cur, 15)      \
  "s_waitcnt lgkmcnt(1)\n"
struct AttnState { f32x16 o, sa, sb, c; u32x4 k, v0, v1; bf16x8 q; unsigned ad, ix; u64 msk; };
#define ATTN_OPS(S)                                                                                                                   \
  : "+{v[64:79]}"(S.o), "+{v[32:47]}"(S.sa), "+{v[48:63]}"(S.sb), "+{v[20:23]}"(S.k), "+{v[8:11]}"(S.v0), "+{v[12:15]}"(S.v1), "+{v24}"(S.ad) \
  : "{v[80:95]}"(S.c), "{v[16:19]}"(S.q), "{v25}"(S.ix), "{s[98:99]}"(S.msk)                                                           \
  : "memory", "s96", "s97"
DI void attn_begin(AttnState& S) {
  asm volatile("s_nop 1\n" QK(R_SA) DSR(R_K, "2048") "s_waitcnt lgkmcnt(0)\n s_nop 7\n s_nop 1\n s_branch .Lattn_skip\n"
               ".Lattn_sub:\n" SEG(R_SA, R_SB, 0) SEG(R_SB, R_SA, 2048) SEG(R_SA, R_SB, 4096) SEG(R_SB, R_SA, 6144)
               "v_add_u32 v" STR(R_AD) ", 0x2000, v" STR(R_AD) "\n s_waitcnt lgkmcnt(0)\n s_setpc_b64 s[96:97]\n.Lattn_skip:\n" ATTN_OPS(S));
}
DI void attn_4tiles(AttnState& S) { asm volatile("s_call_b64 s[96:97], .Lattn_sub" ATTN_OPS(S)); }
DI void attn_end(AttnState& S) { asm volatile(PV(R_V0, R_SB, 0) PV(R_V1, R_SB, 8) "s_nop 7\n s_nop 4\n" ATTN_OPS(S)); }
DI float max16(const f32x16& s) {
  float a = fmaxf(fmaxf(s[0], s[1]), s[2]), b = fmaxf(fmaxf(s[3], s[4]), s[5]);
  a = fmaxf(fmaxf(a, s[6]), s[7]); b = fmaxf(fmaxf(b, s[8]), s[9]); a = fmaxf(fmaxf(a, s[10]), s[11]); b = fmaxf(fmaxf(b, s[12]), s[13]);
  return fmaxf(fmaxf(a, s[14]), fmaxf(b, s[15]));
}
DI float halfmax(float m) {
  auto r = __builtin_amdgcn_permlane32_swap(__float_as_uint(m), __float_as_uint(m), false, false);
  return fmaxf(__uint_as_float(r[0]), __uint_as_float(r[1]));
}
DI unsigned gelu_mul2(f32x2 a, f32x2 u) {
  const float c1 = -2.f * 0.7978845608028654f * 1.4426950408889634f, c3 = c1 * 0.044715f;
  const f32x2 t = a * __builtin_elementwise_fma(a * a, (f32x2){c3, c3}, (f32x2){c1, c1});
  const f32x2 d = (f32x2){__builtin_amdgcn_exp2f(t[0]), __builtin_amdgcn_exp2f(t[1])} + 1.f;
  const f32x2 o = a * u * (f32x2){__builtin_amdgcn_rcpf(d[0]), __builtin_amdgcn_rcpf(d[1])};
  return cvtpk(o[0], o[1]);
}

constexpr unsigned slotAB(int t) { return 20480u + ((t + 4) % 5) * 28672u; }
constexpr unsigned slotD(int d) { return d == 0 || d == 5 ? 131072u : (d >= 6 ? (d - 6) : (d - 1)) * 32768u; }

__global__ __launch_bounds__(512, 2) void fused_block(const char* __restrict__ x, const char* __restrict__ wqkv, const char* __restrict__ wo,
                                                      const char* __restrict__ wg, const char* __restrict__ wu, const char* __restrict__ wd,
                                                      const char* __restrict__ gw, char* __restrict__ out, char* ws) {
  __shared__ __attribute__((aligned(1024))) char smem[163840];
  const LAS char* lds = (const LAS char*)smem;
  const unsigned lds0 = (unsigned)(uintptr_t)lds;
  const int tid = threadIdx.x, lane = tid & 63, w = rfl(tid >> 6), b = blockIdx.x, x8 = b & 7, i32 = b >> 3;
  const int r = lane & 31, h = lane >> 5, tq = (lane >> 2) & 3, tp = lane & 3, tc = (lane >> 4) & 1;
  asm volatile("" ::"s"(x), "s"(wqkv), "s"(wo), "s"(wg), "s"(wu), "s"(wd), "s"(gw), "s"(out), "s"(ws));
  const __amdgpu_buffer_rsrc_t rs = __builtin_amdgcn_make_buffer_rsrc(ws, 0, WS_END, 0x00020000);
  STAMP_DECL; STAMP(0);

  const int rb = (x8 >> 2) * 8 + (i32 & 7), cb = (x8 & 3) * 4 + (i32 >> 3);
  const int ru = w & 3, cls = w >> 2, rug = rb * 4 + ru;
  const int head = x8 * 4 + (i32 & 3), qg = i32 >> 2;
  const int rot = ROT;
  unsigned vf[3];
  const char* fb = (cls ? wu : wg) + (cb < 14 ? cb : 14) * 192;
  auto issueB = [&](int t, int n) { glds<0>(fb + ((t + rot) & 7) * 64 * 2752, vf[n], lds0 + slotAB(t) + cls * 12320 + ru * 3072 + n * 1024); };
  bf16x8 xf[32];
  const unsigned xo = (32 * ru + r) * 128 + ((h ^ ((r >> 1) & 7)) << 4);
  u64 ep;
  asm volatile("s_load_dwordx2 %0, %1, 0x0" : "=s"(ep) : "s"(ws) : "memory");
  if (cls) {
    const unsigned vx0 = (32 * ru + (lane >> 3)) * 1024 + (((lane & 7) ^ (lane >> 4)) << 4);
    const unsigned vx1 = (32 * ru + 8 + (lane >> 3)) * 1024 + (((lane & 7) ^ (4 + (lane >> 4))) << 4);
    unsigned vw[3];
#pragma unroll
    for (int n = 0; n < 3; ++n) {
      const unsigned c = 64 * n + lane, row = (c * 5462) >> 16, ch = c - row * 12;
      vw[n] = (16 * ru + row) * 3072 + ch * 16;
      vf[n] = (16 * ru + row) * 2752 + (cb >= 14 ? (ch & 3) : ch) * 16;
    }
    const char* xb = x + (size_t)rb * 131072;
    const char* wb = wqkv + cb * 192;
    auto issueA = [&](int t) {
      const unsigned d = lds0 + slotAB(t);
      const int k = (t + rot) & 7;
      glds<0>(xb + k * 128, vx0, d + ru * 4096); glds<0>(xb + k * 128, vx1, d + ru * 4096 + 1024);
      glds<0>(xb + k * 128 + 16384, vx0, d + ru * 4096 + 2048); glds<0>(xb + k * 128 + 16384, vx1, d + ru * 4096 + 3072);
#pragma unroll
      for (int n = 0; n < 3; ++n) glds<0>(wb + k * 64 * 3072, vw[n], d + 16384 + ru * 3072 + n * 1024);
    };
    issueA(0); issueA(1);
#pragma unroll
    for (int t = 0; t < 8; ++t) {
      if (t <= 6) wait_bar0<7>(); else wait_bar0<3>();
      if (t == 0) STAMP(1);
#pragma unroll
      for (int ks = 0; ks < 4; ++ks) xf[4 * t + ks] = ldsr(lds + slotAB(t) + (xo ^ (ks * 32)));
      if (t + 2 < 8) issueA(t + 2);
      else { issueB(t + 2, 0); issueB(t + 2, 1); issueB(t + 2, 2); }
    }
    STAMP(2);
  } else {
#pragma unroll
    for (int n = 0; n < 3; ++n) {
      const unsigned c = 64 * n + lane, row = (c * 5462) >> 16, ch = c - row * 12;
      vf[n] = (16 * ru + row) * 2752 + (cb >= 14 ? (ch & 3) : ch) * 16;
    }
    f32x16 acc[3] = {};
    const unsigned wof = 16384 + (8 * h + tq) * 192 + tc * 32 + tp * 8;
    auto runA = [&](auto nv_) {
      constexpr int NV = decltype(nv_)::value;
      bf16x8 f[2][3];
      auto rdw = [&](int g, int e) { const LAS char* p = lds + slotAB(g >> 2) + wof + (g & 3) * 3072 + e * 64; f[g & 1][e] = ldstr(p, p + 768); };
      auto rdx = [&](int g) { xf[g] = ldsr(lds + slotAB(g >> 2) + (xo ^ ((g & 3) * 32))); };
      bar();
      STAMP(1);
      rdx(0); rdw(0, 0); rdw(0, 1); rdw(0, 2);
#pragma unroll
      for (int g = 0; g < 32; ++g) {
        const int t = g >> 2;
        if ((g & 3) == 3 && g < 31) bar();
        SBAR();
#pragma unroll
        for (int e = 0; e < 3; ++e) {
          acc[e] = e >= NV ? MFMA(xf[g], f[g & 1][e], acc[e]) : MFMA(f[g & 1][e], xf[g], acc[e]);
          SBAR();
          if (g < 31) { if (e == 0) rdx(g + 1); rdw(g + 1, e); }
          SBAR();
        }
        if (g >= 23 && g < 31 && ((g + 1) & 3) < 3) issueB(((g + 1) >> 2) + 2, (g + 1) & 3);
      }
    };
    if (cb < 10) runA(std::integral_constant<int, 3>{}); else if (cb == 10) runA(std::integral_constant<int, 2>{}); else runA(std::integral_constant<int, 0>{});
    STAMP(2);
#pragma unroll
    for (int e = 0; e < 3; ++e) {
      const int u = 3 * cb + e, hd = (u & 15) * 2;
      const unsigned vo = u < 16 ? lane * 16 : u < 32 ? h * 512 + ((r & 19) | ((r & 4) << 1) | ((r & 8) >> 1)) * 16 : (r >> 4) * 131072 + h * 512 + (r & 15) * 16;
      const unsigned so = u < 16 ? WS_Q + (hd * 64 + rug) * 1024 : WS_KV + (hd * 64 + rug) * 2048 + (u < 32 ? 0 : 1024);
      const unsigned d2 = u < 16 ? 65536 : u < 32 ? 131072 : 256;
      const float sc = u < 16 ? 0.25f * 1.4426950408889634f : 1.f;
      st16_sc1(pack8(acc[e], 0, sc), ws + so, vo);
      st16_sc1(pack8(acc[e], 8, sc), ws + so + d2, vo);
    }
  }
  asm volatile("" : "+s"(ep));
  ep = (ep ^ (ep >> 29)) * 0x9E3779B97F4A7C15ull + 0x632BE59BD9B4E019ull;
  const u32x2 tag = {(unsigned)ep, (unsigned)(ep >> 32)};
  STAMP(3);
  AttnState S;
  const char* kvg = ws + WS_KV + head * 131072;
  {
    const int u0 = 6 * cb + 3 * cls;
    const unsigned fof = tc * 12320 + (8 * h + tq) * 192 + cls * 96 + tp * 8;
    f32x16 acc[3] = {};
    const unsigned fao = WS_FA + ((lane < 32 ? (lane & 15) : 2 * qg + (lane & 1)) * 16 + ((lane < 32 ? 16 + (lane & 16) : 0) + (head >> 1)) / 3) * 128;
    auto runB = [&](auto act_) {
      constexpr bool ACT = decltype(act_)::value;
      bf16x8 f[2][3];
      u32x2 fl;
      auto rdw = [&](int g, int e) { const LAS char* p = lds + slotAB(8 + (g >> 2)) + fof + (g & 3) * 3072 + e * 32; f[g & 1][e] = ldstr(p, p + 768); };
      if (cls) wait_bar0<3>(); else wait_bar0<9>();
      if (ACT) { rdw(0, 0); rdw(0, 1); rdw(0, 2); }
      issueB(10, 0);
#pragma unroll
      for (int g = 0; g < 32; ++g) {
        const int t = 8 + (g >> 2);
        if ((g & 3) == 3 && g < 31) {
          if (t == 8) { if (cls) wait_bar0<4>(); else wait_bar0<10>(); }
          else if (t == 9) wait_bar0<5>();
          else if (t == 10) wait_bar0<6>();
          else if (t == 11) wait_bar0<7>();
          else if (t == 12) { if (w == 0) flag_wait<0>(fl, ws, fao, tag); wait_bar0<6>(); }
          else if (t == 13) wait_bar0<9>();
          else wait_bar0<4>();
          if (t == 9 && tid == 0) __builtin_amdgcn_raw_buffer_store_b64(tag, rs, 0, WS_FA + (rb * 16 + cb) * 128, SC1);
        }
        SBAR();
        if (ACT) {
#pragma unroll
          for (int e = 0; e < 3; ++e) {
            acc[e] = MFMA(f[g & 1][e], xf[g], acc[e]);
            SBAR();
            if (g < 31) rdw(g + 1, e);
            SBAR();
          }
        }
        if (g < 17) issueB(10 + (g + 1) / 3, (g + 1) % 3);
        if (g == 16 && w == 0) flag_issue(fl, ws, fao);
        if (g == 19) {
          asm volatile("global_load_dwordx4 %0, %1, %2 sc1" : "=v"(S.q) : "v"(lane * 16), "s"(ws + WS_Q + (head * 64 + qg * 8 + w) * 1024) : "memory");
          glds4_sc1(kvg + w * 4096, lane * 16, lds0 + w * 4096);
        }
        if (g == 21 || g == 23 || g == 27) { const int c = g == 21 ? 1 : g == 23 ? 2 : 3; glds4_sc1(kvg + c * 32768 + w * 4096, lane * 16, lds0 + c * 32768 + w * 4096); }
        if (g == 27) asm volatile("" : "+v"(S.q));
      }
    };
    if (u0 < NU) {
      runB(std::true_type{});
      STAMP(4);
      __builtin_amdgcn_s_setprio(3);
#pragma unroll
      for (int e = 0; e < 3; ++e) {
        u32x4 o;
#pragma unroll
        for (int j = 0; j < 4; ++j) o[j] = gelu_mul2((f32x2){acc[e][2 * j], acc[e][2 * j + 1]}, (f32x2){acc[e][2 * j + 8], acc[e][2 * j + 9]});
        if (e < 2 || u0 + e < NU) st16_sc1(o, ws + WS_H + (rug * NU + u0 + e) * 1024, lane * 16);
      }
    } else {
      runB(std::false_type{});
      STAMP(4);
      __builtin_amdgcn_s_setprio(3);
      if (!cls) {
        f32x16 z = {};
#pragma unroll
        for (int g = 0; g < 32; ++g) z = MFMA(*(const bf16x8*)(gw + ((g + 4 * rot) & 31) * 32 + h * 16), xf[g], z);
        if (h == 0) __builtin_amdgcn_raw_buffer_store_b32(__float_as_uint(1.f / (1.f + __expf(-z[0]))), rs, r * 4, WS_G + rug * 128, SC1);
      }
    }
  }
  STAMP(5);
  const int rb2 = x8 * 4 + (i32 & 3), cb2 = i32 >> 2, ru2 = rb2 * 2 + (w & 1), kq = w >> 1;
  const unsigned vd = (lane >> 3) * 1024 + (((lane & 7) ^ (((lane >> 4) & 1) << 2)) << 4);
  auto issueD = [&](int d, int n0, int n1) {
    const unsigned dst = lds0 + slotD(d) + kq * 8192 + (w & 1) * (d == 5 ? 2048 : 4096);
    const char* p = d < 5 ? wd + cb2 * 128 + (kq * 22 + d * 4 + (w & 1) * 2) * 16384
                  : d == 5 ? wd + cb2 * 128 + (kq < 3 ? kq * 22 + 20 + (w & 1) : 0) * 16384
                           : wo + cb2 * 128 + (kq * 8 + (d - 6) * 4 + (w & 1) * 2) * 16384;
#pragma unroll
    for (int n = n0; n < n1; ++n) glds<0>(p + n * 8192, vd, dst + n * 1024);
  };
  float mref;
  {
    S.ad = lds0 + lane * 16; S.ix = 0xddddddddu; S.msk = 0x0000ffff0000ffffull;
    asm volatile("" : "+v"(S.ix), "+s"(S.msk));
    const bf16x8 k0 = ldsr(lds + lane * 16);
    const f32x16 z = {};
    S.sa = MFMA(k0, S.q, z);
    mref = halfmax(max16(S.sa));
    const unsigned one = ((lane & 27) == 16) ? 0x3f803f80u : 0u;
    S.k = __builtin_bit_cast(u32x4, k0); S.v0 = (u32x4){one, one, one, one}; S.v1 = S.v0;
#pragma unroll
    for (int j = 0; j < 16; ++j) { S.c[j] = -mref; S.o[j] = 0.f; S.sb[j] = 0.f; }
  }
  attn_begin(S);
  STAMP(6);
  u32x2 flb;
  bf16x8 hf[22];
  float gate, gateq;
  const unsigned fbo = (b == 0 && lane >= 32) ? WS_FA + (w * 32 + lane - 32) * 128
                                              : WS_FB + ((lane & 16) ? ((qg * 8 + w) >> 2) * 16 + 15 : (rb2 >> 1) * 16 + (lane & 15)) * 128;
#pragma unroll
  for (int c = 0; c < 16; ++c) {
    if (c == 4) {
      wait_bar<0>();
      if (tid == 0) __builtin_amdgcn_raw_buffer_store_b64(tag, rs, 0, WS_FB + (rb * 16 + cb) * 128, SC1);
      STAMP(7);
    }
    if (c == 12) bar();
    if (c >= 4 && c < 8) issueD((c - 4) >> 1, (c & 1) * 2, (c & 1) * 2 + 2);
    if (c >= 12) issueD(2 + ((c - 12) >> 1), (c & 1) * 2, (c & 1) * 2 + 2);
    if (c == 7) flag_issue(flb, ws, fbo);
    if (c == 8) flag_wait<0>(flb, ws, fbo, tag);
    if (c >= 8 && c < 14) {
#pragma unroll
      for (int j = (c - 8) * 4; j < (c - 8) * 4 + 4 && j < 22; ++j)
        hf[j] = __builtin_bit_cast(bf16x8, __builtin_amdgcn_raw_buffer_load_b128(rs, lane * 16, WS_H + (ru2 * NU + (j < 20 || kq < 3 ? kq * 22 + j : 0)) * 1024, SC1));
    }
    if (c == 13) {
      gate = __uint_as_float(__builtin_amdgcn_raw_buffer_load_b32(rs, r * 4, WS_G + ru2 * 128, SC1));
      gateq = __uint_as_float(__builtin_amdgcn_raw_buffer_load_b32(rs, r * 4, WS_G + (qg * 8 + w) * 128, SC1));
    }
    if (c != 1) asm volatile("s_setprio %0" ::"i"(c == 0 ? 2 : 3 - (c & 3)));
    attn_4tiles(S);
  }
  attn_end(S);
  __builtin_amdgcn_s_setprio(0);
  STAMP(8);
  {
    float chk = S.o[8];
#pragma unroll
    for (int j = 0; j < 8; ++j) chk += fabsf(S.o[j]) * 0x1p-40f;
    if (__builtin_expect(__any(!(chk < 0x1p60f)), 0)) {
      const f32x16 z = {};
      const unsigned kvo = WS_KV + head * 131072;
#pragma unroll 1
      for (int t = 1; t < 64; ++t)
        mref = fmaxf(mref, max16(MFMA(__builtin_bit_cast(bf16x8, __builtin_amdgcn_raw_buffer_load_b128(rs, lane * 16, kvo + t * 2048, SC1)), S.q, z)));
      mref = halfmax(mref);
      const unsigned one = ((lane & 27) == 16) ? 0x3f803f80u : 0u;
      f32x16 c, o = {};
#pragma unroll
      for (int j = 0; j < 16; ++j) c[j] = -mref;
#pragma unroll 1
      for (int t = 0; t < 64; ++t) {
        f32x16 s = MFMA(__builtin_bit_cast(bf16x8, __builtin_amdgcn_raw_buffer_load_b128(rs, lane * 16, kvo + t * 2048, SC1)), S.q, c);
        u32x4 v0 = __builtin_amdgcn_raw_buffer_load_b128(rs, (lane & 47) * 16, kvo + t * 2048 + 1024, SC1);
        u32x4 v1 = __builtin_amdgcn_raw_buffer_load_b128(rs, (lane & 47) * 16, kvo + t * 2048 + 1280, SC1);
        if (lane & 16) { v0 = (u32x4){one, one, one, one}; v1 = v0; }
#pragma unroll
        for (int j = 0; j < 16; ++j) s[j] = __builtin_amdgcn_exp2f(s[j]);
        const f32x8 p0 = __builtin_shufflevector(s, s, 0, 1, 2, 3, 4, 5, 6, 7), p1 = __builtin_shufflevector(s, s, 8, 9, 10, 11, 12, 13, 14, 15);
        o = __builtin_amdgcn_smfmac_f32_32x32x32_bf16(__builtin_bit_cast(bf16x8, v0), __builtin_bit_cast(bf16x16, p0), o, 0xdddd, 0, 0);
        o = __builtin_amdgcn_smfmac_f32_32x32x32_bf16(__builtin_bit_cast(bf16x8, v1), __builtin_bit_cast(bf16x16, p1), o, 0xdddd, 0, 0);
      }
      S.o = o;
    }
#pragma unroll
    for (int j = 0; j < 22; ++j) asm volatile("" : "+v"(hf[j]));
    asm volatile("" : "+v"(gate));
    st16_sc1(pack8(S.o, 0, gateq * __builtin_amdgcn_rcpf(S.o[8])), ws + WS_O + ((qg * 8 + w) * 32 + head) * 1024, lane * 16);
  }
  const unsigned dof = kq * 8192 + (4 * h + tq) * 128 + (((tc * 2 + (tp >> 1)) ^ ((tq >> 1) << 2)) << 4) + (tp & 1) * 8;
  f32x16 acc[2] = {};
  bf16x8 f[2][2], of[8];
  u32x2 flo;
  const unsigned foo = WS_FO + (ru2 * 32 + kq * 8 + (lane & 7)) * 128;
  auto rdd = [&](int g, int n) {
    const LAS char* p = lds + (g < 22 ? slotD(g >> 2) + (g & 3) * 2048 : slotD(6 + ((g - 22) >> 2)) + ((g - 22) & 3) * 2048) + (dof ^ (n * 64));
    f[g & 1][n] = ldstr(p, p + 1024);
  };
  wait_bar0<1>();
  STAMP(9);
  rdd(0, 0); rdd(0, 1);
  issueD(4, 0, 4);
#pragma unroll
  for (int g = 0; g < 30; ++g) {
    if (g == 3) {
      wait_vm<4>();
      if (lane == 0) __builtin_amdgcn_raw_buffer_store_b64(tag, rs, 0, WS_FO + ((qg * 8 + w) * 32 + head) * 128, SC1);
    }
    if (g == 3 || g == 7 || g == 11) bar();
    if (g == 15) wait_bar0<9>();
    if (g == 17) {
      STAMP(10);
      flag_wait<0>(flo, ws, foo, tag);
      STAMP(11);
#pragma unroll
      for (int j = 0; j < 8; ++j) of[j] = __builtin_bit_cast(bf16x8, __builtin_amdgcn_raw_buffer_load_b128(rs, lane * 16, WS_O + (ru2 * 32 + kq * 8 + j) * 1024, SC1));
    }
    if (g == 21) bar();
    if (g == 22) {
      const float g1 = 1.f - gate;
#pragma unroll
      for (int j = 0; j < 16; ++j) { acc[0][j] *= g1; acc[1][j] *= g1; }
    }
    SBAR();
#pragma unroll
    for (int n = 0; n < 2; ++n) {
      if (g < 20 || g >= 22 || kq < 3) acc[n] = MFMA(f[g & 1][n], g < 22 ? hf[g] : of[g - 22], acc[n]);
      SBAR();
      if (g < 29) rdd(g + 1, n);
      SBAR();
    }
    if (g == 3) issueD(5, 0, 2);
    if (g == 7) issueD(6, 0, 4);
    if (g == 11) { issueD(7, 0, 4); flag_issue(flo, ws, foo); }
  }
  STAMP(12);
  {
    LAS char* red = (LAS char*)lds + 65536;
    LAS char* pw = red + (w * 32 + r) * 272 + h * 16;
#pragma unroll
    for (int n = 0; n < 2; ++n)
#pragma unroll
      for (int g = 0; g < 4; ++g) *(LAS f32x4*)(pw + n * 128 + g * 32) = (f32x4){acc[n][4 * g], acc[n][4 * g + 1], acc[n][4 * g + 2], acc[n][4 * g + 3]};
    bar();
    const int tok = w * 8 + (lane >> 3);
    const LAS char* pr = red + tok * 272 + (lane & 7) * 32;
    f32x4 s0 = *(const LAS f32x4*)pr, s1 = *(const LAS f32x4*)(pr + 16);
#pragma unroll
    for (int q = 1; q < 4; ++q) { s0 += *(const LAS f32x4*)(pr + q * 64 * 272); s1 += *(const LAS f32x4*)(pr + q * 64 * 272 + 16); }
    st16_sc1((u32x4){cvtpk(s0[0], s0[1]), cvtpk(s0[2], s0[3]), cvtpk(s1[0], s1[1]), cvtpk(s1[2], s1[3])}, out + (rb2 * 64 + w * 8) * 1024 + cb2 * 128,
             (lane >> 3) * 1024 + (lane & 7) * 16);
  }
  if (b == 0 && tid == 0) *(u32x2*)(ws + WS_EP) = tag;
  STAMP(13);
}

extern "C" void kernel_launch(void* const* d_in, const int* in_sizes, int n_in, void* d_out, int out_size, void* d_ws, size_t ws_size,
                              hipStream_t stream) {
  fused_block<<<256, 512, 0, stream>>>((const char*)d_in[0], (const char*)d_in[1], (const char*)d_in[2], (const char*)d_in[4], (const char*)d_in[5],
                                       (const char*)d_in[6], (const char*)d_in[7], (char*)d_out, (char*)d_ws);
}
```

```cpp
#include <hip/hip_runtime.h>
#include <stdint.h>

__global__ __launch_bounds__(512, 2) void fused_block(const void* x, const void* wqkv, const void* wo, const void* wg, const void* wu, const void* wd,
                                                      const void* gw, void* out, void* ws) {
#if defined(__HIP_DEVICE_COMPILE__)
  __shared__ __attribute__((aligned(1024))) char smem[163840];
  unsigned lds0 = (unsigned)(uintptr_t)(__attribute__((address_space(3))) char*)smem;
  uint64_t ka = (uint64_t)__builtin_amdgcn_kernarg_segment_ptr();
  unsigned wgid = blockIdx.x, tid = threadIdx.x;
  asm volatile(
    ".Lkbegin:\n"
    "s_load_dwordx2 s[4:5], s[0:1], 0x0\n"
    "s_load_dwordx2 s[28:29], s[0:1], 0x40\n"
    "s_load_dwordx2 s[12:13], s[0:1], 0x18\n"
    "s_load_dwordx2 s[16:17], s[0:1], 0x20\n"
    "v_lshrrev_b32 v6, 6, v0\n"
    "v_and_b32 v0, 63, v0\n"
    "v_readfirstlane_b32 s48, v6\n"
    "v_lshlrev_b32 v1, 4, v0\n"
    "s_and_b32 s49, s48, 3\n"
    "s_and_b32 s94, s2, 3\n"
    "s_lshr_b32 s95, s2, 6\n"
    "s_mul_i32 s96, s94, 3\n"
    "s_add_u32 s96, s96, s95\n"
    "s_add_u32 s97, s94, 12\n"
    "s_cmp_lt_u32 s95, 3\n"
    "s_cselect_b32 s50, s96, s97\n"
    "s_cselect_b32 s92, 48, 43\n"
    "s_lshl_b32 s92, s92, 6\n"
    "s_lshl_b32 s10, s92, 9\n"
    "s_and_b32 s96, s48, 1\n"
    "s_lshl_b32 s96, s96, 3\n"
    "s_add_u32 s96, s96, 24\n"
    "s_cmp_lt_u32 s95, 3\n"
    "s_cselect_b32 s96, 8, s96\n"
    "s_load_dwordx2 s[8:9], s[0:1], s96\n"
    "s_mov_b32 s6, 0x200000\n"
    "s_mov_b32 s26, 0x80000\n"
    "s_mov_b32 s14, 0x158000\n"
    "s_mov_b32 s18, 0x158000\n"
    "s_mov_b32 s22, 0x158000\n"
    "s_mov_b32 s38, 0x400\n"
    "s_mov_b32 s34, 0x200000\n"
    "s_mov_b32 s30, 0xf00000\n"
    "s_mov_b32 s7, 0x20000\n"
    "s_mov_b32 s11, 0x20000\n"
    "s_mov_b32 s15, 0x20000\n"
    "s_mov_b32 s19, 0x20000\n"
    "s_mov_b32 s23, 0x20000\n"
    "s_mov_b32 s27, 0x20000\n"
    "s_mov_b32 s31, 0x20000\n"
    "s_mov_b32 s35, 0x20000\n"
    "s_mov_b32 s39, 0x20000\n"
    "s_bfe_u32 s96, s2, 0x30003\n"
    "s_bfe_u32 s97, s2, 0x10002\n"
    "s_lshl3_add_u32 s51, s97, s96\n"
    "s_add_u32 s96, s96, s95\n"
    "s_and_b32 s97, s2, 1\n"
    "s_lshl1_add_u32 s96, s96, s97\n"
    "s_and_b32 s52, s96, 7\n"
    "s_lshl2_add_u32 s58, s51, s49\n"
    "s_cmp_lt_u32 s48, 4\n"
    "s_cbranch_scc1 .Lh\n"
    "v_lshrrev_b32 v6, 3, v0\n"
    "v_and_b32 v7, 7, v0\n"
    "v_lshrrev_b32 v8, 4, v0\n"
    "v_xor_b32 v8, v8, v7\n"
    "v_lshlrev_b32 v8, 4, v8\n"
    "v_lshl_or_b32 v32, v6, 10, v8\n"
    "v_xor_b32 v33, 64, v32\n"
    "v_add_u32 v34, 14336, v32\n"
    "v_add_u32 v35, 21504, v33\n"
    "v_add_u32 v33, 7168, v33\n"
    "s_lshl_b32 s59, s58, 15\n"
    "s_lshl_b32 s60, s52, 7\n"
    "s_lshl_b32 s64, s49, 12\n"
    "v_lshrrev_b32 v8, 5, v0\n"
    "v_bfe_u32 v9, v0, 3, 1\n"
    "v_lshl_or_b32 v8, v8, 1, v9\n"
    "v_bfe_u32 v9, v0, 4, 1\n"
    "v_lshl_or_b32 v8, v8, 1, v9\n"
    "v_lshlrev_b32 v8, 2, v8\n"
    "v_lshlrev_b32 v9, 4, v7\n"
    "v_mad_u32_u24 v36, v8, s92, v9\n"
    "s_sub_u32 s94, s92, 1056\n"
    "v_add_u32 v37, s94, v36\n"
    "v_add_u32 v38, s94, v37\n"
    "v_add_u32 v39, s94, v38\n"
    "s_and_b32 s94, s49, 1\n"
    "s_lshl1_add_u32 s94, s50, s94\n"
    "s_add_u32 s95, s50, 3\n"
    "s_cmp_lt_u32 s50, 12\n"
    "s_cselect_b32 s94, s94, s95\n"
    "s_lshl_b32 s94, s94, 7\n"
    "s_lshr_b32 s95, s49, 1\n"
    "s_mul_i32 s95, s95, s92\n"
    "s_lshl_b32 s95, s95, 5\n"
    "s_add_u32 s61, s94, s95\n"
    "s_lshl_b32 s63, s92, 6\n"
    "s_mul_i32 s62, s52, s63\n"
    "s_mul_i32 s65, s49, 4224\n"
    "s_add_u32 m0, s64, 0x7800\n"
    "s_or_b32 s96, s59, s60\n"
    "s_waitcnt lgkmcnt(0)\n"
    "buffer_load_dwordx4 v32, s[4:7], s96 offen offset:0 lds\n"
    "buffer_load_dwordx4 v33, s[4:7], s96 offen offset:1024 lds\n"
    "buffer_load_dwordx4 v34, s[4:7], s96 offen offset:2048 lds\n"
    "buffer_load_dwordx4 v35, s[4:7], s96 offen offset:3072 lds\n"
    "s_add_u32 m0, s65, 0xb800\n"
    "s_add_u32 s97, s61, s62\n"
    "buffer_load_dwordx4 v36, s[8:11], s97 offen offset:0 lds\n"
    "buffer_load_dwordx4 v37, s[8:11], s97 offen offset:1056 lds\n"
    "buffer_load_dwordx4 v38, s[8:11], s97 offen offset:2112 lds\n"
    "buffer_load_dwordx4 v39, s[8:11], s97 offen offset:3168 lds\n"
    "s_add_u32 s62, s62, s63\n"
    "s_add_u32 s60, s60, 128\n"
    "s_and_b32 s60, s60, 1023\n"
    "s_cselect_b32 s62, s62, 0\n"
    "s_and_b32 s94, s2, 7\n"
    "s_bfe_u32 s95, s2, 0x20003\n"
    "s_lshl2_add_u32 s53, s94, s95\n"
    "s_lshr_b32 s54, s2, 5\n"
    "s_mul_i32 s94, s50, 3\n"
    "s_add_u32 s94, s94, 40\n"
    "s_lshl1_add_u32 s95, s50, 54\n"
    "s_min_u32 s94, s94, s95\n"
    "s_mul_i32 s95, s50, 5\n"
    "s_cmp_lt_u32 s50, 12\n"
    "s_cselect_b32 s55, s95, s94\n"
    "s_min_u32 s94, s55, 80\n"
    "s_and_b32 s56, s94, 0xfe\n"
    "s_add_u32 s94, s52, 0\n"
    "s_and_b32 s94, s94, 7\n"
    "s_mul_i32 s94, s94, 11008\n"
    "s_lshl1_add_u32 s94, s56, s94\n"
    "s_lshl_b32 s67, s94, 4\n"
    "s_add_u32 s94, s52, 1\n"
    "s_and_b32 s94, s94, 7\n"
    "s_mul_i32 s94, s94, 11008\n"
    "s_lshl1_add_u32 s94, s56, s94\n"
    "s_lshl_b32 s68, s94, 4\n"
    "s_add_u32 s94, s52, 2\n"
    "s_and_b32 s94, s94, 7\n"
    "s_mul_i32 s94, s94, 11008\n"
    "s_lshl1_add_u32 s94, s56, s94\n"
    "s_lshl_b32 s69, s94, 4\n"
    "s_add_u32 s94, s52, 3\n"
    "s_and_b32 s94, s94, 7\n"
    "s_mul_i32 s94, s94, 11008\n"
    "s_lshl1_add_u32 s94, s56, s94\n"
    "s_lshl_b32 s70, s94, 4\n"
    "s_add_u32 s94, s52, 4\n"
    "s_and_b32 s94, s94, 7\n"
    "s_mul_i32 s94, s94, 11008\n"
    "s_lshl1_add_u32 s94, s56, s94\n"
    "s_lshl_b32 s71, s94, 4\n"
    "s_add_u32 s94, s52, 5\n"
    "s_and_b32 s94, s94, 7\n"
    "s_mul_i32 s94, s94, 11008\n"
    "s_lshl1_add_u32 s94, s56, s94\n"
    "s_lshl_b32 s72, s94, 4\n"
    "s_add_u32 s94, s52, 6\n"
    "s_and_b32 s94, s94, 7\n"
    "s_mul_i32 s94, s94, 11008\n"
    "s_lshl1_add_u32 s94, s56, s94\n"
    "s_lshl_b32 s73, s94, 4\n"
    "s_add_u32 s94, s52, 7\n"
    "s_and_b32 s94, s94, 7\n"
    "s_mul_i32 s94, s94, 11008\n"
    "s_lshl1_add_u32 s94, s56, s94\n"
    "s_lshl_b32 s74, s94, 4\n"
    "s_mul_i32 s66, s49, 3104\n"
    "s_lshr_b32 s57, s48, 1\n"
    "s_lshl_b32 s79, s48, 12\n"
    "s_lshl_b32 s94, s53, 17\n"
    "s_add_u32 s94, s94, s79\n"
    "s_add_u32 s78, s94, 0x300000\n"
    "s_lshl3_add_u32 s95, s54, s48\n"
    "s_lshl_b32 s94, s53, 6\n"
    "s_add_u32 s94, s94, s95\n"
    "s_lshl_b32 s94, s94, 10\n"
    "s_add_u32 s80, s94, 0x100000\n"
    "s_lshl_b32 s94, s95, 7\n"
    "s_add_u32 s89, s94, 0x80000\n"
    "s_lshl_b32 s94, s95, 5\n"
    "s_add_u32 s94, s94, s53\n"
    "s_lshl_b32 s90, s94, 10\n"
    "s_lshl4_add_u32 s94, s51, s50\n"
    "s_lshl_b32 s94, s94, 7\n"
    "s_add_u32 s77, s94, 0x1000\n"
    "s_mul_i32 s94, s58, 86\n"
    "s_lshl_b32 s94, s94, 10\n"
    "s_add_u32 s81, s94, 0x900000\n"
    "s_and_b32 s96, s48, 1\n"
    "s_lshl1_add_u32 s97, s53, s96\n"
    "s_lshl_b32 s94, s97, 7\n"
    "s_add_u32 s88, s94, 0x80000\n"
    "s_mul_i32 s94, s97, 86\n"
    "s_mul_i32 s95, s57, 22\n"
    "s_add_u32 s94, s94, s95\n"
    "s_lshl_b32 s94, s94, 10\n"
    "s_add_u32 s86, s94, 0x900000\n"
    "s_lshl2_add_u32 s94, s97, s57\n"
    "s_lshl_b32 s87, s94, 13\n"
    "s_lshl_b32 s97, s54, 7\n"
    "s_lshl1_add_u32 s94, s96, s95\n"
    "s_lshl_b32 s94, s94, 14\n"
    "s_add_u32 s82, s94, s97\n"
    "s_add_u32 s94, s95, s96\n"
    "s_add_u32 s94, s94, 20\n"
    "s_cmp_lt_u32 s57, 3\n"
    "s_cselect_b32 s94, s94, 0\n"
    "s_lshl_b32 s94, s94, 14\n"
    "s_add_u32 s83, s94, s97\n"
    "s_lshl2_add_u32 s94, s57, s96\n"
    "s_lshl_b32 s94, s94, 15\n"
    "s_add_u32 s84, s94, s97\n"
    "s_lshl_b32 s94, s57, 13\n"
    "s_lshl_b32 s95, s96, 11\n"
    "s_add_u32 s85, s94, s95\n"
    "s_lshl3_add_u32 s94, s53, s48\n"
    "s_lshl_b32 s94, s94, 13\n"
    "s_add_u32 s91, s94, s97\n"
    "s_mov_b32 s42, 0xffff\n"
    "s_mov_b32 s43, 0xffff\n"
    "s_mov_b32 s44, 0xbdd2d3e8\n"
    "s_mov_b32 s45, 0xc0135761\n"
    "s_cmp_lt_u32 s50, 4\n"
    "s_cselect_b32 s46, 0x3eb8aa3b, 1.0\n"
    "v_and_b32 v6, 15, v0\n"
    "v_lshrrev_b32 v7, 4, v0\n"
    "v_lshrrev_b32 v8, 1, v6\n"
    "v_xor_b32 v8, v8, v7\n"
    "v_lshlrev_b32 v8, 4, v8\n"
    "v_lshl_or_b32 v8, v6, 7, v8\n"
    "s_lshl_b32 s94, s49, 12\n"
    "v_or_b32 v16, s94, v8\n"
    "v_xor_b32 v19, 64, v16\n"
    "v_add_u32 v17, 0x10000, v16\n"
    "v_add_u32 v18, 0x20000, v16\n"
    "v_add_u32 v20, 0x10000, v19\n"
    "v_add_u32 v21, 0x20000, v19\n"
    "s_lshr_b32 s94, s49, 1\n"
    "s_and_b32 s95, s49, 1\n"
    "s_lshl2_add_u32 s94, s94, s95\n"
    "s_lshl_b32 s94, s94, 3\n"
    "s_movk_i32 s95, 2752\n"
    "v_add_u32 v8, 0, v0\n"
    "v_mul_u32_u24 v9, 5462, v8\n"
    "v_lshrrev_b32 v9, 16, v9\n"
    "v_mul_u32_u24 v10, 12, v9\n"
    "v_sub_u32 v8, v8, v10\n"
    "v_and_b32 v10, 8, v9\n"
    "v_add3_u32 v9, v9, v10, s94\n"
    "v_lshlrev_b32 v8, 4, v8\n"
    "v_mad_u32_u24 v8, v9, s95, v8\n"
    "v_subrev_u32 v28, 0, v8\n"
    "v_add_u32 v8, 64, v0\n"
    "v_mul_u32_u24 v9, 5462, v8\n"
    "v_lshrrev_b32 v9, 16, v9\n"
    "v_mul_u32_u24 v10, 12, v9\n"
    "v_sub_u32 v8, v8, v10\n"
    "v_and_b32 v10, 8, v9\n"
    "v_add3_u32 v9, v9, v10, s94\n"
    "v_lshlrev_b32 v8, 4, v8\n"
    "v_mad_u32_u24 v8, v9, s95, v8\n"
    "v_subrev_u32 v29, 1024, v8\n"
    "v_add_u32 v8, 128, v0\n"
    "v_mul_u32_u24 v9, 5462, v8\n"
    "v_lshrrev_b32 v9, 16, v9\n"
    "v_mul_u32_u24 v10, 12, v9\n"
    "v_sub_u32 v8, v8, v10\n"
    "v_and_b32 v10, 8, v9\n"
    "v_add3_u32 v9, v9, v10, s94\n"
    "v_lshlrev_b32 v8, 4, v8\n"
    "v_mad_u32_u24 v8, v9, s95, v8\n"
    "v_subrev_u32 v30, 2048, v8\n"
    "v_bfe_u32 v8, v0, 4, 1\n"
    "v_mul_u32_u24 v8, 3104, v8\n"
    "v_lshrrev_b32 v9, 5, v0\n"
    "v_bfe_u32 v10, v0, 2, 2\n"
    "v_lshl_or_b32 v9, v9, 3, v10\n"
    "v_mul_u32_u24 v9, 192, v9\n"
    "v_and_b32 v10, 3, v0\n"
    "v_lshlrev_b32 v10, 3, v10\n"
    "v_add3_u32 v8, v8, v9, v10\n"
    "s_sub_u32 s94, s55, s56\n"
    "s_cmp_lt_u32 s48, 4\n"
    "s_cselect_b32 s95, 3, 0\n"
    "s_add_u32 s94, s94, s95\n"
    "s_lshl_b32 s94, s94, 5\n"
    "v_add_u32 v25, s94, v8\n"
    "v_add_u32 v26, 0x10000, v25\n"
    "v_add_u32 v27, 0x20000, v25\n"
    "s_add_u32 m0, s64, 0xfa00\n"
    "s_or_b32 s96, s59, s60\n"
    "buffer_load_dwordx4 v32, s[4:7], s96 offen offset:0 lds\n"
    "buffer_load_dwordx4 v33, s[4:7], s96 offen offset:1024 lds\n"
    "buffer_load_dwordx4 v34, s[4:7], s96 offen offset:2048 lds\n"
    "buffer_load_dwordx4 v35, s[4:7], s96 offen offset:3072 lds\n"
    "s_add_u32 m0, s65, 0x13a00\n"
    "s_add_u32 s97, s61, s62\n"
    "buffer_load_dwordx4 v36, s[8:11], s97 offen offset:0 lds\n"
    "buffer_load_dwordx4 v37, s[8:11], s97 offen offset:1056 lds\n"
    "buffer_load_dwordx4 v38, s[8:11], s97 offen offset:2112 lds\n"
    "buffer_load_dwordx4 v39, s[8:11], s97 offen offset:3168 lds\n"
    "s_add_u32 s62, s62, s63\n"
    "s_add_u32 s60, s60, 128\n"
    "s_and_b32 s60, s60, 1023\n"
    "s_cselect_b32 s62, s62, 0\n"
    "s_load_dwordx2 s[94:95], s[28:29], 0x0\n"
    "s_load_dwordx2 s[24:25], s[0:1], 0x10\n"
    "s_load_dwordx2 s[20:21], s[0:1], 0x28\n"
    "s_load_dwordx2 s[36:37], s[0:1], 0x30\n"
    "s_load_dwordx2 s[32:33], s[0:1], 0x38\n"
    "s_waitcnt lgkmcnt(0)\n"
    "s_lshr_b64 s[96:97], s[94:95], 29\n"
    "s_xor_b64 s[94:95], s[94:95], s[96:97]\n"
    "s_mul_i32 s96, s94, 0x9e3779b9\n"
    "s_mul_hi_u32 s97, s94, 0x7f4a7c15\n"
    "s_add_u32 s96, s96, s97\n"
    "s_mul_i32 s97, s95, 0x7f4a7c15\n"
    "s_add_u32 s96, s96, s97\n"
    "s_mul_i32 s94, s94, 0x7f4a7c15\n"
    "s_add_u32 s40, s94, 0xd9b4e019\n"
    "s_addc_u32 s41, s96, 0x632be59b\n"
    "v_mov_b32 v4, s40\n"
    "v_mov_b32 v5, s41\n"
    "s_waitcnt vmcnt(8) lgkmcnt(0)\n"
    "s_barrier\n"
    "ds_read_b128 v[128:131], v16 offset:30720\n"
    "ds_read_b128 v[132:135], v16 offset:32768\n"
    "ds_read_b128 v[136:139], v19 offset:30720\n"
    "ds_read_b128 v[140:143], v19 offset:32768\n"
    "s_add_u32 m0, s64, 0x17c00\n"
    "s_or_b32 s96, s59, s60\n"
    "buffer_load_dwordx4 v32, s[4:7], s96 offen offset:0 lds\n"
    "buffer_load_dwordx4 v33, s[4:7], s96 offen offset:1024 lds\n"
    "buffer_load_dwordx4 v34, s[4:7], s96 offen offset:2048 lds\n"
    "buffer_load_dwordx4 v35, s[4:7], s96 offen offset:3072 lds\n"
    "s_add_u32 m0, s65, 0x1bc00\n"
    "s_add_u32 s97, s61, s62\n"
    "buffer_load_dwordx4 v36, s[8:11], s97 offen offset:0 lds\n"
    "buffer_load_dwordx4 v37, s[8:11], s97 offen offset:1056 lds\n"
    "buffer_load_dwordx4 v38, s[8:11], s97 offen offset:2112 lds\n"
    "buffer_load_dwordx4 v39, s[8:11], s97 offen offset:3168 lds\n"
    "s_add_u32 s62, s62, s63\n"
    "s_add_u32 s60, s60, 128\n"
    "s_and_b32 s60, s60, 1023\n"
    "s_cselect_b32 s62, s62, 0\n"
    "s_waitcnt vmcnt(8) lgkmcnt(0)\n"
    "s_barrier\n"
    "ds_read_b128 v[144:147], v16 offset:64000\n"
    "ds_read_b128 v[148:151], v17 offset:512\n"
    "ds_read_b128 v[152:155], v19 offset:64000\n"
    "ds_read_b128 v[156:159], v20 offset:512\n"
    "s_add_u32 m0, s64, 0x1fe00\n"
    "s_or_b32 s96, s59, s60\n"
    "buffer_load_dwordx4 v32, s[4:7], s96 offen offset:0 lds\n"
    "buffer_load_dwordx4 v33, s[4:7], s96 offen offset:1024 lds\n"
    "buffer_load_dwordx4 v34, s[4:7], s96 offen offset:2048 lds\n"
    "buffer_load_dwordx4 v35, s[4:7], s96 offen offset:3072 lds\n"
    "s_add_u32 m0, s65, 0x23e00\n"
    "s_add_u32 s97, s61, s62\n"
    "buffer_load_dwordx4 v36, s[8:11], s97 offen offset:0 lds\n"
    "buffer_load_dwordx4 v37, s[8:11], s97 offen offset:1056 lds\n"
    "buffer_load_dwordx4 v38, s[8:11], s97 offen offset:2112 lds\n"
    "buffer_load_dwordx4 v39, s[8:11], s97 offen offset:3168 lds\n"
    "s_add_u32 s62, s62, s63\n"
    "s_add_u32 s60, s60, 128\n"
    "s_and_b32 s60, s60, 1023\n"
    "s_cselect_b32 s62, s62, 0\n"
    "s_waitcnt vmcnt(8) lgkmcnt(0)\n"
    "s_barrier\n"
    "ds_read_b128 v[160:163], v17 offset:31744\n"
    "ds_read_b128 v[164:167], v17 offset:33792\n"
    "ds_read_b128 v[168:171], v20 offset:31744\n"
    "ds_read_b128 v[172:175], v20 offset:33792\n"
    "s_add_u32 m0, s64, 0x7800\n"
    "s_or_b32 s96, s59, s60\n"
    "buffer_load_dwordx4 v32, s[4:7], s96 offen offset:0 lds\n"
    "buffer_load_dwordx4 v33, s[4:7], s96 offen offset:1024 lds\n"
    "buffer_load_dwordx4 v34, s[4:7], s96 offen offset:2048 lds\n"
    "buffer_load_dwordx4 v35, s[4:7], s96 offen offset:3072 lds\n"
    "s_add_u32 m0, s65, 0xb800\n"
    "s_add_u32 s97, s61, s62\n"
    "buffer_load_dwordx4 v36, s[8:11], s97 offen offset:0 lds\n"
    "buffer_load_dwordx4 v37, s[8:11], s97 offen offset:1056 lds\n"
    "buffer_load_dwordx4 v38, s[8:11], s97 offen offset:2112 lds\n"
    "buffer_load_dwordx4 v39, s[8:11], s97 offen offset:3168 lds\n"
    "s_add_u32 s62, s62, s63\n"
    "s_add_u32 s60, s60, 128\n"
    "s_and_b32 s60, s60, 1023\n"
    "s_cselect_b32 s62, s62, 0\n"
    "s_waitcnt vmcnt(8) lgkmcnt(0)\n"
    "s_barrier\n"
    "ds_read_b128 v[176:179], v17 offset:65024\n"
    "ds_read_b128 v[180:183], v18 offset:1536\n"
    "ds_read_b128 v[184:187], v20 offset:65024\n"
    "ds_read_b128 v[188:191], v21 offset:1536\n"
    "s_add_u32 m0, s64, 0xfa00\n"
    "s_or_b32 s96, s59, s60\n"
    "buffer_load_dwordx4 v32, s[4:7], s96 offen offset:0 lds\n"
    "buffer_load_dwordx4 v33, s[4:7], s96 offen offset:1024 lds\n"
    "buffer_load_dwordx4 v34, s[4:7], s96 offen offset:2048 lds\n"
    "buffer_load_dwordx4 v35, s[4:7], s96 offen offset:3072 lds\n"
    "s_add_u32 m0, s65, 0x13a00\n"
    "s_add_u32 s97, s61, s62\n"
    "buffer_load_dwordx4 v36, s[8:11], s97 offen offset:0 lds\n"
    "buffer_load_dwordx4 v37, s[8:11], s97 offen offset:1056 lds\n"
    "buffer_load_dwordx4 v38, s[8:11], s97 offen offset:2112 lds\n"
    "buffer_load_dwordx4 v39, s[8:11], s97 offen offset:3168 lds\n"
    "s_add_u32 s62, s62, s63\n"
    "s_add_u32 s60, s60, 128\n"
    "s_and_b32 s60, s60, 1023\n"
    "s_cselect_b32 s62, s62, 0\n"
    "s_waitcnt vmcnt(8) lgkmcnt(0)\n"
    "s_barrier\n"
    "ds_read_b128 v[192:195], v16 offset:30720\n"
    "ds_read_b128 v[196:199], v16 offset:32768\n"
    "ds_read_b128 v[200:203], v19 offset:30720\n"
    "ds_read_b128 v[204:207], v19 offset:32768\n"
    "s_add_u32 m0, s64, 0x17c00\n"
    "s_or_b32 s96, s59, s60\n"
    "buffer_load_dwordx4 v32, s[4:7], s96 offen offset:0 lds\n"
    "buffer_load_dwordx4 v33, s[4:7], s96 offen offset:1024 lds\n"
    "buffer_load_dwordx4 v34, s[4:7], s96 offen offset:2048 lds\n"
    "buffer_load_dwordx4 v35, s[4:7], s96 offen offset:3072 lds\n"
    "s_add_u32 m0, s65, 0x1bc00\n"
    "s_add_u32 s97, s61, s62\n"
    "buffer_load_dwordx4 v36, s[8:11], s97 offen offset:0 lds\n"
    "buffer_load_dwordx4 v37, s[8:11], s97 offen offset:1056 lds\n"
    "buffer_load_dwordx4 v38, s[8:11], s97 offen offset:2112 lds\n"
    "buffer_load_dwordx4 v39, s[8:11], s97 offen offset:3168 lds\n"
    "s_add_u32 s62, s62, s63\n"
    "s_add_u32 s60, s60, 128\n"
    "s_and_b32 s60, s60, 1023\n"
    "s_cselect_b32 s62, s62, 0\n"
    "s_waitcnt vmcnt(8) lgkmcnt(0)\n"
    "s_barrier\n"
    "ds_read_b128 v[208:211], v16 offset:64000\n"
    "ds_read_b128 v[212:215], v17 offset:512\n"
    "ds_read_b128 v[216:219], v19 offset:64000\n"
    "ds_read_b128 v[220:223], v20 offset:512\n"
    "s_add_u32 m0, s64, 0x1fe00\n"
    "s_or_b32 s96, s59, s60\n"
    "buffer_load_dwordx4 v32, s[4:7], s96 offen offset:0 lds\n"
    "buffer_load_dwordx4 v33, s[4:7], s96 offen offset:1024 lds\n"
    "buffer_load_dwordx4 v34, s[4:7], s96 offen offset:2048 lds\n"
    "buffer_load_dwordx4 v35, s[4:7], s96 offen offset:3072 lds\n"
    "s_add_u32 m0, s65, 0x23e00\n"
    "s_add_u32 s97, s61, s62\n"
    "buffer_load_dwordx4 v36, s[8:11], s97 offen offset:0 lds\n"
    "buffer_load_dwordx4 v37, s[8:11], s97 offen offset:1056 lds\n"
    "buffer_load_dwordx4 v38, s[8:11], s97 offen offset:2112 lds\n"
    "buffer_load_dwordx4 v39, s[8:11], s97 offen offset:3168 lds\n"
    "s_waitcnt vmcnt(8) lgkmcnt(0)\n"
    "s_barrier\n"
    "ds_read_b128 v[224:227], v17 offset:31744\n"
    "ds_read_b128 v[228:231], v17 offset:33792\n"
    "ds_read_b128 v[232:235], v20 offset:31744\n"
    "ds_read_b128 v[236:239], v20 offset:33792\n"
    "s_add_u32 m0, s66, 0x9900\n"
    "s_nop 0\n"
    "buffer_load_dwordx4 v28, s[12:15], s67 offen offset:0 lds\n"
    "buffer_load_dwordx4 v29, s[12:15], s67 offen offset:1024 lds\n"
    "buffer_load_dwordx4 v30, s[12:15], s67 offen offset:2048 lds\n"
    "s_add_u32 m0, s66, 0xc980\n"
    "s_nop 0\n"
    "buffer_load_dwordx4 v28, s[16:19], s67 offen offset:0 lds\n"
    "buffer_load_dwordx4 v29, s[16:19], s67 offen offset:1024 lds\n"
    "buffer_load_dwordx4 v30, s[16:19], s67 offen offset:2048 lds\n"
    "s_waitcnt vmcnt(6) lgkmcnt(0)\n"
    "s_barrier\n"
    "ds_read_b128 v[240:243], v17 offset:65024\n"
    "ds_read_b128 v[244:247], v18 offset:1536\n"
    "ds_read_b128 v[248:251], v20 offset:65024\n"
    "ds_read_b128 v[252:255], v21 offset:1536\n"
    "s_add_u32 m0, s66, 0x11b00\n"
    "s_nop 0\n"
    "buffer_load_dwordx4 v28, s[12:15], s68 offen offset:0 lds\n"
    "buffer_load_dwordx4 v29, s[12:15], s68 offen offset:1024 lds\n"
    "buffer_load_dwordx4 v30, s[12:15], s68 offen offset:2048 lds\n"
    "s_add_u32 m0, s66, 0x14b80\n"
    "s_nop 0\n"
    "buffer_load_dwordx4 v28, s[16:19], s68 offen offset:0 lds\n"
    "buffer_load_dwordx4 v29, s[16:19], s68 offen offset:1024 lds\n"
    "buffer_load_dwordx4 v30, s[16:19], s68 offen offset:2048 lds\n"
    "s_waitcnt vmcnt(6) lgkmcnt(0)\n"
    "s_barrier\n"
    "ds_read_b64_tr_b16 v[32:33], v25 offset:39168\n"
    "ds_read_b64_tr_b16 v[34:35], v25 offset:39936\n"
    "ds_read_b64_tr_b16 v[36:37], v25 offset:51584\n"
    "ds_read_b64_tr_b16 v[38:39], v25 offset:52352\n"
    "ds_read_b64_tr_b16 v[40:41], v25 offset:39200\n"
    "ds_read_b64_tr_b16 v[42:43], v25 offset:39968\n"
    "ds_read_b64_tr_b16 v[44:45], v25 offset:51616\n"
    "ds_read_b64_tr_b16 v[46:47], v25 offset:52384\n"
    "s_waitcnt lgkmcnt(4)\n"
    "v_mfma_f32_16x16x32_bf16 v[64:67], v[32:35], v[128:131], 0\n"
    "ds_read_b64_tr_b16 v[48:49], v25 offset:39232\n"
    "ds_read_b64_tr_b16 v[50:51], v25 offset:40000\n"
    "v_mfma_f32_16x16x32_bf16 v[68:71], v[32:35], v[132:135], 0\n"
    "v_mfma_f32_16x16x32_bf16 v[72:75], v[36:39], v[128:131], 0\n"
    "ds_read_b64_tr_b16 v[52:53], v25 offset:51648\n"
    "ds_read_b64_tr_b16 v[54:55], v25 offset:52416\n"
    "v_mfma_f32_16x16x32_bf16 v[76:79], v[36:39], v[132:135], 0\n"
    "s_waitcnt lgkmcnt(4)\n"
    "v_mfma_f32_16x16x32_bf16 v[80:83], v[40:43], v[128:131], 0\n"
    "ds_read_b64_tr_b16 v[56:57], v25 offset:45376\n"
    "ds_read_b64_tr_b16 v[58:59], v25 offset:46144\n"
    "v_mfma_f32_16x16x32_bf16 v[84:87], v[40:43], v[132:135], 0\n"
    "v_mfma_f32_16x16x32_bf16 v[88:91], v[44:47], v[128:131], 0\n"
    "ds_read_b64_tr_b16 v[60:61], v25 offset:57792\n"
    "ds_read_b64_tr_b16 v[62:63], v25 offset:58560\n"
    "v_mfma_f32_16x16x32_bf16 v[92:95], v[44:47], v[132:135], 0\n"
    "s_waitcnt lgkmcnt(4)\n"
    "v_mfma_f32_16x16x32_bf16 v[96:99], v[48:51], v[128:131], 0\n"
    "ds_read_b64_tr_b16 v[32:33], v25 offset:45408\n"
    "ds_read_b64_tr_b16 v[34:35], v25 offset:46176\n"
    "v_mfma_f32_16x16x32_bf16 v[100:103], v[48:51], v[132:135], 0\n"
    "v_mfma_f32_16x16x32_bf16 v[104:107], v[52:55], v[128:131], 0\n"
    "ds_read_b64_tr_b16 v[36:37], v25 offset:57824\n"
    "ds_read_b64_tr_b16 v[38:39], v25 offset:58592\n"
    "v_mfma_f32_16x16x32_bf16 v[108:111], v[52:55], v[132:135], 0\n"
    "s_waitcnt lgkmcnt(4)\n"
    "v_mfma_f32_16x16x32_bf16 v[64:67], v[56:59], v[136:139], v[64:67]\n"
    "ds_read_b64_tr_b16 v[40:41], v25 offset:45440\n"
    "ds_read_b64_tr_b16 v[42:43], v25 offset:46208\n"
    "v_mfma_f32_16x16x32_bf16 v[68:71], v[56:59], v[140:143], v[68:71]\n"
    "v_mfma_f32_16x16x32_bf16 v[72:75], v[60:63], v[136:139], v[72:75]\n"
    "ds_read_b64_tr_b16 v[44:45], v25 offset:57856\n"
    "ds_read_b64_tr_b16 v[46:47], v25 offset:58624\n"
    "v_mfma_f32_16x16x32_bf16 v[76:79], v[60:63], v[140:143], v[76:79]\n"
    "s_waitcnt vmcnt(0)\n"
    "s_barrier\n"
    "s_waitcnt lgkmcnt(4)\n"
    "v_mfma_f32_16x16x32_bf16 v[80:83], v[32:35], v[136:139], v[80:83]\n"
    "ds_read_b64_tr_b16 v[48:49], v26 offset:6912\n"
    "ds_read_b64_tr_b16 v[50:51], v26 offset:7680\n"
    "v_mfma_f32_16x16x32_bf16 v[84:87], v[32:35], v[140:143], v[84:87]\n"
    "v_mfma_f32_16x16x32_bf16 v[88:91], v[36:39], v[136:139], v[88:91]\n"
    "ds_read_b64_tr_b16 v[52:53], v26 offset:19328\n"
    "ds_read_b64_tr_b16 v[54:55], v26 offset:20096\n"
    "v_mfma_f32_16x16x32_bf16 v[92:95], v[36:39], v[140:143], v[92:95]\n"
    "s_waitcnt lgkmcnt(4)\n"
    "v_mfma_f32_16x16x32_bf16 v[96:99], v[40:43], v[136:139], v[96:99]\n"
    "ds_read_b64_tr_b16 v[56:57], v26 offset:6944\n"
    "ds_read_b64_tr_b16 v[58:59], v26 offset:7712\n"
    "v_mfma_f32_16x16x32_bf16 v[100:103], v[40:43], v[140:143], v[100:103]\n"
    "v_mfma_f32_16x16x32_bf16 v[104:107], v[44:47], v[136:139], v[104:107]\n"
    "ds_read_b64_tr_b16 v[60:61], v26 offset:19360\n"
    "ds_read_b64_tr_b16 v[62:63], v26 offset:20128\n"
    "v_mfma_f32_16x16x32_bf16 v[108:111], v[44:47], v[140:143], v[108:111]\n"
    "s_waitcnt lgkmcnt(4)\n"
    "v_mfma_f32_16x16x32_bf16 v[64:67], v[48:51], v[144:147], v[64:67]\n"
    "ds_read_b64_tr_b16 v[32:33], v26 offset:6976\n"
    "ds_read_b64_tr_b16 v[34:35], v26 offset:7744\n"
    "v_mfma_f32_16x16x32_bf16 v[68:71], v[48:51], v[148:151], v[68:71]\n"
    "v_mfma_f32_16x16x32_bf16 v[72:75], v[52:55], v[144:147], v[72:75]\n"
    "ds_read_b64_tr_b16 v[36:37], v26 offset:19392\n"
    "ds_read_b64_tr_b16 v[38:39], v26 offset:20160\n"
    "v_mfma_f32_16x16x32_bf16 v[76:79], v[52:55], v[148:151], v[76:79]\n"
    "s_waitcnt lgkmcnt(4)\n"
    "v_mfma_f32_16x16x32_bf16 v[80:83], v[56:59], v[144:147], v[80:83]\n"
    "ds_read_b64_tr_b16 v[40:41], v26 offset:13120\n"
    "ds_read_b64_tr_b16 v[42:43], v26 offset:13888\n"
    "v_mfma_f32_16x16x32_bf16 v[84:87], v[56:59], v[148:151], v[84:87]\n"
    "v_mfma_f32_16x16x32_bf16 v[88:91], v[60:63], v[144:147], v[88:91]\n"
    "ds_read_b64_tr_b16 v[44:45], v26 offset:25536\n"
    "ds_read_b64_tr_b16 v[46:47], v26 offset:26304\n"
    "v_mfma_f32_16x16x32_bf16 v[92:95], v[60:63], v[148:151], v[92:95]\n"
    "s_waitcnt lgkmcnt(4)\n"
    "v_mfma_f32_16x16x32_bf16 v[96:99], v[32:35], v[144:147], v[96:99]\n"
    "ds_read_b64_tr_b16 v[48:49], v26 offset:13152\n"
    "ds_read_b64_tr_b16 v[50:51], v26 offset:13920\n"
    "v_mfma_f32_16x16x32_bf16 v[100:103], v[32:35], v[148:151], v[100:103]\n"
    "v_mfma_f32_16x16x32_bf16 v[104:107], v[36:39], v[144:147], v[104:107]\n"
    "ds_read_b64_tr_b16 v[52:53], v26 offset:25568\n"
    "ds_read_b64_tr_b16 v[54:55], v26 offset:26336\n"
    "v_mfma_f32_16x16x32_bf16 v[108:111], v[36:39], v[148:151], v[108:111]\n"
    "s_waitcnt lgkmcnt(4)\n"
    "v_mfma_f32_16x16x32_bf16 v[64:67], v[40:43], v[152:155], v[64:67]\n"
    "ds_read_b64_tr_b16 v[56:57], v26 offset:13184\n"
    "ds_read_b64_tr_b16 v[58:59], v26 offset:13952\n"
    "v_mfma_f32_16x16x32_bf16 v[68:71], v[40:43], v[156:159], v[68:71]\n"
    "v_mfma_f32_16x16x32_bf16 v[72:75], v[44:47], v[152:155], v[72:75]\n"
    "ds_read_b64_tr_b16 v[60:61], v26 offset:25600\n"
    "ds_read_b64_tr_b16 v[62:63], v26 offset:26368\n"
    "v_mfma_f32_16x16x32_bf16 v[76:79], v[44:47], v[156:159], v[76:79]\n"
    "s_barrier\n"
    "s_waitcnt lgkmcnt(4)\n"
    "v_mfma_f32_16x16x32_bf16 v[80:83], v[48:51], v[152:155], v[80:83]\n"
    "ds_read_b64_tr_b16 v[32:33], v26 offset:40192\n"
    "ds_read_b64_tr_b16 v[34:35], v26 offset:40960\n"
    "v_mfma_f32_16x16x32_bf16 v[84:87], v[48:51], v[156:159], v[84:87]\n"
    "v_mfma_f32_16x16x32_bf16 v[88:91], v[52:55], v[152:155], v[88:91]\n"
    "ds_read_b64_tr_b16 v[36:37], v26 offset:52608\n"
    "ds_read_b64_tr_b16 v[38:39], v26 offset:53376\n"
    "v_mfma_f32_16x16x32_bf16 v[92:95], v[52:55], v[156:159], v[92:95]\n"
    "s_waitcnt lgkmcnt(4)\n"
    "v_mfma_f32_16x16x32_bf16 v[96:99], v[56:59], v[152:155], v[96:99]\n"
    "ds_read_b64_tr_b16 v[40:41], v26 offset:40224\n"
    "ds_read_b64_tr_b16 v[42:43], v26 offset:40992\n"
    "v_mfma_f32_16x16x32_bf16 v[100:103], v[56:59], v[156:159], v[100:103]\n"
    "v_mfma_f32_16x16x32_bf16 v[104:107], v[60:63], v[152:155], v[104:107]\n"
    "ds_read_b64_tr_b16 v[44:45], v26 offset:52640\n"
    "ds_read_b64_tr_b16 v[46:47], v26 offset:53408\n"
    "v_mfma_f32_16x16x32_bf16 v[108:111], v[60:63], v[156:159], v[108:111]\n"
    "s_waitcnt lgkmcnt(4)\n"
    "v_mfma_f32_16x16x32_bf16 v[64:67], v[32:35], v[160:163], v[64:67]\n"
    "ds_read_b64_tr_b16 v[48:49], v26 offset:40256\n"
    "ds_read_b64_tr_b16 v[50:51], v26 offset:41024\n"
    "v_mfma_f32_16x16x32_bf16 v[68:71], v[32:35], v[164:167], v[68:71]\n"
    "v_mfma_f32_16x16x32_bf16 v[72:75], v[36:39], v[160:163], v[72:75]\n"
    "ds_read_b64_tr_b16 v[52:53], v26 offset:52672\n"
    "ds_read_b64_tr_b16 v[54:55], v26 offset:53440\n"
    "v_mfma_f32_16x16x32_bf16 v[76:79], v[36:39], v[164:167], v[76:79]\n"
    "s_waitcnt lgkmcnt(4)\n"
    "v_mfma_f32_16x16x32_bf16 v[80:83], v[40:43], v[160:163], v[80:83]\n"
    "ds_read_b64_tr_b16 v[56:57], v26 offset:46400\n"
    "ds_read_b64_tr_b16 v[58:59], v26 offset:47168\n"
    "v_mfma_f32_16x16x32_bf16 v[84:87], v[40:43], v[164:167], v[84:87]\n"
    "v_mfma_f32_16x16x32_bf16 v[88:91], v[44:47], v[160:163], v[88:91]\n"
    "ds_read_b64_tr_b16 v[60:61], v26 offset:58816\n"
    "ds_read_b64_tr_b16 v[62:63], v26 offset:59584\n"
    "v_mfma_f32_16x16x32_bf16 v[92:95], v[44:47], v[164:167], v[92:95]\n"
    "s_waitcnt lgkmcnt(4)\n"
    "v_mfma_f32_16x16x32_bf16 v[96:99], v[48:51], v[160:163], v[96:99]\n"
    "ds_read_b64_tr_b16 v[32:33], v26 offset:46432\n"
    "ds_read_b64_tr_b16 v[34:35], v26 offset:47200\n"
    "v_mfma_f32_16x16x32_bf16 v[100:103], v[48:51], v[164:167], v[100:103]\n"
    "v_mfma_f32_16x16x32_bf16 v[104:107], v[52:55], v[160:163], v[104:107]\n"
    "ds_read_b64_tr_b16 v[36:37], v26 offset:58848\n"
    "ds_read_b64_tr_b16 v[38:39], v26 offset:59616\n"
    "v_mfma_f32_16x16x32_bf16 v[108:111], v[52:55], v[164:167], v[108:111]\n"
    "s_waitcnt lgkmcnt(4)\n"
    "v_mfma_f32_16x16x32_bf16 v[64:67], v[56:59], v[168:171], v[64:67]\n"
    "ds_read_b64_tr_b16 v[40:41], v26 offset:46464\n"
    "ds_read_b64_tr_b16 v[42:43], v26 offset:47232\n"
    "v_mfma_f32_16x16x32_bf16 v[68:71], v[56:59], v[172:175], v[68:71]\n"
    "v_mfma_f32_16x16x32_bf16 v[72:75], v[60:63], v[168:171], v[72:75]\n"
    "ds_read_b64_tr_b16 v[44:45], v26 offset:58880\n"
    "ds_read_b64_tr_b16 v[46:47], v26 offset:59648\n"
    "v_mfma_f32_16x16x32_bf16 v[76:79], v[60:63], v[172:175], v[76:79]\n"
    "s_barrier\n"
    "s_waitcnt lgkmcnt(4)\n"
    "v_mfma_f32_16x16x32_bf16 v[80:83], v[32:35], v[168:171], v[80:83]\n"
    "ds_read_b64_tr_b16 v[48:49], v27 offset:7936\n"
    "ds_read_b64_tr_b16 v[50:51], v27 offset:8704\n"
    "v_mfma_f32_16x16x32_bf16 v[84:87], v[32:35], v[172:175], v[84:87]\n"
    "v_mfma_f32_16x16x32_bf16 v[88:91], v[36:39], v[168:171], v[88:91]\n"
    "ds_read_b64_tr_b16 v[52:53], v27 offset:20352\n"
    "ds_read_b64_tr_b16 v[54:55], v27 offset:21120\n"
    "v_mfma_f32_16x16x32_bf16 v[92:95], v[36:39], v[172:175], v[92:95]\n"
    "s_waitcnt lgkmcnt(4)\n"
    "v_mfma_f32_16x16x32_bf16 v[96:99], v[40:43], v[168:171], v[96:99]\n"
    "ds_read_b64_tr_b16 v[56:57], v27 offset:7968\n"
    "ds_read_b64_tr_b16 v[58:59], v27 offset:8736\n"
    "v_mfma_f32_16x16x32_bf16 v[100:103], v[40:43], v[172:175], v[100:103]\n"
    "v_mfma_f32_16x16x32_bf16 v[104:107], v[44:47], v[168:171], v[104:107]\n"
    "ds_read_b64_tr_b16 v[60:61], v27 offset:20384\n"
    "ds_read_b64_tr_b16 v[62:63], v27 offset:21152\n"
    "v_mfma_f32_16x16x32_bf16 v[108:111], v[44:47], v[172:175], v[108:111]\n"
    "s_waitcnt lgkmcnt(4)\n"
    "v_mfma_f32_16x16x32_bf16 v[64:67], v[48:51], v[176:179], v[64:67]\n"
    "ds_read_b64_tr_b16 v[32:33], v27 offset:8000\n"
    "ds_read_b64_tr_b16 v[34:35], v27 offset:8768\n"
    "v_mfma_f32_16x16x32_bf16 v[68:71], v[48:51], v[180:183], v[68:71]\n"
    "v_mfma_f32_16x16x32_bf16 v[72:75], v[52:55], v[176:179], v[72:75]\n"
    "ds_read_b64_tr_b16 v[36:37], v27 offset:20416\n"
    "ds_read_b64_tr_b16 v[38:39], v27 offset:21184\n"
    "v_mfma_f32_16x16x32_bf16 v[76:79], v[52:55], v[180:183], v[76:79]\n"
    "s_waitcnt lgkmcnt(4)\n"
    "v_mfma_f32_16x16x32_bf16 v[80:83], v[56:59], v[176:179], v[80:83]\n"
    "ds_read_b64_tr_b16 v[40:41], v27 offset:14144\n"
    "ds_read_b64_tr_b16 v[42:43], v27 offset:14912\n"
    "v_mfma_f32_16x16x32_bf16 v[84:87], v[56:59], v[180:183], v[84:87]\n"
    "v_mfma_f32_16x16x32_bf16 v[88:91], v[60:63], v[176:179], v[88:91]\n"
    "ds_read_b64_tr_b16 v[44:45], v27 offset:26560\n"
    "ds_read_b64_tr_b16 v[46:47], v27 offset:27328\n"
    "v_mfma_f32_16x16x32_bf16 v[92:95], v[60:63], v[180:183], v[92:95]\n"
    "s_waitcnt lgkmcnt(4)\n"
    "v_mfma_f32_16x16x32_bf16 v[96:99], v[32:35], v[176:179], v[96:99]\n"
    "ds_read_b64_tr_b16 v[48:49], v27 offset:14176\n"
    "ds_read_b64_tr_b16 v[50:51], v27 offset:14944\n"
    "v_mfma_f32_16x16x32_bf16 v[100:103], v[32:35], v[180:183], v[100:103]\n"
    "v_mfma_f32_16x16x32_bf16 v[104:107], v[36:39], v[176:179], v[104:107]\n"
    "ds_read_b64_tr_b16 v[52:53], v27 offset:26592\n"
    "ds_read_b64_tr_b16 v[54:55], v27 offset:27360\n"
    "v_mfma_f32_16x16x32_bf16 v[108:111], v[36:39], v[180:183], v[108:111]\n"
    "s_waitcnt lgkmcnt(4)\n"
    "v_mfma_f32_16x16x32_bf16 v[64:67], v[40:43], v[184:187], v[64:67]\n"
    "ds_read_b64_tr_b16 v[56:57], v27 offset:14208\n"
    "ds_read_b64_tr_b16 v[58:59], v27 offset:14976\n"
    "v_mfma_f32_16x16x32_bf16 v[68:71], v[40:43], v[188:191], v[68:71]\n"
    "v_mfma_f32_16x16x32_bf16 v[72:75], v[44:47], v[184:187], v[72:75]\n"
    "ds_read_b64_tr_b16 v[60:61], v27 offset:26624\n"
    "ds_read_b64_tr_b16 v[62:63], v27 offset:27392\n"
    "v_mfma_f32_16x16x32_bf16 v[76:79], v[44:47], v[188:191], v[76:79]\n"
    "s_barrier\n"
    "s_waitcnt lgkmcnt(4)\n"
    "v_mfma_f32_16x16x32_bf16 v[80:83], v[48:51], v[184:187], v[80:83]\n"
    "ds_read_b64_tr_b16 v[32:33], v25 offset:39168\n"
    "ds_read_b64_tr_b16 v[34:35], v25 offset:39936\n"
    "v_mfma_f32_16x16x32_bf16 v[84:87], v[48:51], v[188:191], v[84:87]\n"
    "v_mfma_f32_16x16x32_bf16 v[88:91], v[52:55], v[184:187], v[88:91]\n"
    "ds_read_b64_tr_b16 v[36:37], v25 offset:51584\n"
    "ds_read_b64_tr_b16 v[38:39], v25 offset:52352\n"
    "v_mfma_f32_16x16x32_bf16 v[92:95], v[52:55], v[188:191], v[92:95]\n"
    "s_waitcnt lgkmcnt(4)\n"
    "v_mfma_f32_16x16x32_bf16 v[96:99], v[56:59], v[184:187], v[96:99]\n"
    "ds_read_b64_tr_b16 v[40:41], v25 offset:39200\n"
    "ds_read_b64_tr_b16 v[42:43], v25 offset:39968\n"
    "v_mfma_f32_16x16x32_bf16 v[100:103], v[56:59], v[188:191], v[100:103]\n"
    "v_mfma_f32_16x16x32_bf16 v[104:107], v[60:63], v[184:187], v[104:107]\n"
    "ds_read_b64_tr_b16 v[44:45], v25 offset:51616\n"
    "ds_read_b64_tr_b16 v[46:47], v25 offset:52384\n"
    "v_mfma_f32_16x16x32_bf16 v[108:111], v[60:63], v[188:191], v[108:111]\n"
    "s_waitcnt lgkmcnt(4)\n"
    "v_mfma_f32_16x16x32_bf16 v[64:67], v[32:35], v[192:195], v[64:67]\n"
    "ds_read_b64_tr_b16 v[48:49], v25 offset:39232\n"
    "ds_read_b64_tr_b16 v[50:51], v25 offset:40000\n"
    "v_mfma_f32_16x16x32_bf16 v[68:71], v[32:35], v[196:199], v[68:71]\n"
    "v_add_u32 v128, 0x10000, v1\n"
    "v_mov_b32 v129, 0xdddddddd\n"
    "v_and_b32 v6, 15, v0\n"
    "v_lshrrev_b32 v7, 4, v0\n"
    "v_lshlrev_b32 v134, 4, v6\n"
    "v_mfma_f32_16x16x32_bf16 v[72:75], v[36:39], v[192:195], v[72:75]\n"
    "ds_read_b64_tr_b16 v[52:53], v25 offset:51648\n"
    "ds_read_b64_tr_b16 v[54:55], v25 offset:52416\n"
    "v_mfma_f32_16x16x32_bf16 v[76:79], v[36:39], v[196:199], v[76:79]\n"
    "v_lshl_or_b32 v134, v7, 9, v134\n"
    "v_lshlrev_b32 v159, 4, v6\n"
    "v_lshl_add_u32 v159, v7, 2, v159\n"
    "v_add_u32 v159, 4, v159\n"
    "v_and_b32 v138, 1, v0\n"
    "s_waitcnt lgkmcnt(4)\n"
    "v_mfma_f32_16x16x32_bf16 v[80:83], v[40:43], v[192:195], v[80:83]\n"
    "ds_read_b64_tr_b16 v[56:57], v25 offset:45376\n"
    "ds_read_b64_tr_b16 v[58:59], v25 offset:46144\n"
    "v_mfma_f32_16x16x32_bf16 v[84:87], v[40:43], v[196:199], v[84:87]\n"
    "v_lshl_add_u32 v138, s54, 1, v138\n"
    "v_lshlrev_b32 v138, 4, v138\n"
    "v_cmp_gt_u32 vcc, 32, v0\n"
    "s_lshr_b32 s94, s53, 3\n"
    "s_add_u32 s94, s94, 32\n"
    "v_mfma_f32_16x16x32_bf16 v[88:91], v[44:47], v[192:195], v[88:91]\n"
    "ds_read_b64_tr_b16 v[60:61], v25 offset:57792\n"
    "ds_read_b64_tr_b16 v[62:63], v25 offset:58560\n"
    "v_mfma_f32_16x16x32_bf16 v[92:95], v[44:47], v[196:199], v[92:95]\n"
    "v_cndmask_b32 v159, v138, v159, vcc\n"
    "v_add_lshl_u32 v159, v159, s94, 7\n"
    "s_lshr_b32 s94, s53, 1\n"
    "v_lshl_add_u32 v135, s94, 4, v6\n"
    "s_lshr_b32 s94, s48, 2\n"
    "s_waitcnt lgkmcnt(4)\n"
    "v_mfma_f32_16x16x32_bf16 v[96:99], v[48:51], v[192:195], v[96:99]\n"
    "ds_read_b64_tr_b16 v[32:33], v25 offset:45408\n"
    "ds_read_b64_tr_b16 v[34:35], v25 offset:46176\n"
    "v_mfma_f32_16x16x32_bf16 v[100:103], v[48:51], v[196:199], v[100:103]\n"
    "s_lshl1_add_u32 s94, s54, s94\n"
    "s_lshl4_add_u32 s94, s94, 15\n"
    "v_mov_b32 v138, s94\n"
    "v_cndmask_b32 v135, v138, v135, s[42:43]\n"
    "v_and_b32 v142, 31, v0\n"
    "v_mfma_f32_16x16x32_bf16 v[104:107], v[52:55], v[192:195], v[104:107]\n"
    "ds_read_b64_tr_b16 v[36:37], v25 offset:57824\n"
    "ds_read_b64_tr_b16 v[38:39], v25 offset:58592\n"
    "v_mfma_f32_16x16x32_bf16 v[108:111], v[52:55], v[196:199], v[108:111]\n"
    "v_lshl_add_u32 v138, s48, 5, v142\n"
    "s_cmp_eq_u32 s2, 0\n"
    "s_cselect_b32 s94, 32, 64\n"
    "v_cmp_gt_u32 vcc, s94, v0\n"
    "s_movk_i32 s94, 288\n"
    "v_lshlrev_b32 v142, 2, v142\n"
    "s_waitcnt lgkmcnt(4)\n"
    "v_mfma_f32_16x16x32_bf16 v[64:67], v[56:59], v[200:203], v[64:67]\n"
    "ds_read_b64_tr_b16 v[40:41], v25 offset:45440\n"
    "ds_read_b64_tr_b16 v[42:43], v25 offset:46208\n"
    "v_mfma_f32_16x16x32_bf16 v[68:71], v[56:59], v[204:207], v[68:71]\n"
    "v_cndmask_b32 v135, v138, v135, vcc\n"
    "v_add_lshl_u32 v135, v135, s94, 7\n"
    "v_lshlrev_b32 v140, 2, v6\n"
    "v_and_b32 v138, 7, v0\n"
    "s_lshr_b32 s94, s87, 10\n"
    "v_mfma_f32_16x16x32_bf16 v[72:75], v[60:63], v[200:203], v[72:75]\n"
    "ds_read_b64_tr_b16 v[44:45], v25 offset:57856\n"
    "ds_read_b64_tr_b16 v[46:47], v25 offset:58624\n"
    "v_mfma_f32_16x16x32_bf16 v[76:79], v[60:63], v[204:207], v[76:79]\n"
    "s_add_u32 s94, s94, 1024\n"
    "v_add_lshl_u32 v136, v138, s94, 7\n"
    "v_lshrrev_b32 v158, 3, v0\n"
    "v_lshlrev_b32 v157, 5, v138\n"
    "s_movk_i32 s95, 272\n"
    "s_barrier\n"
    "s_waitcnt lgkmcnt(4)\n"
    "v_mfma_f32_16x16x32_bf16 v[80:83], v[32:35], v[200:203], v[80:83]\n"
    "ds_read_b64_tr_b16 v[48:49], v26 offset:6912\n"
    "ds_read_b64_tr_b16 v[50:51], v26 offset:7680\n"
    "v_mfma_f32_16x16x32_bf16 v[84:87], v[32:35], v[204:207], v[84:87]\n"
    "v_mad_u32_u24 v157, v158, s95, v157\n"
    "s_mul_i32 s94, s48, 2176\n"
    "s_add_u32 s94, s94, 0x10000\n"
    "v_add_u32 v157, s94, v157\n"
    "v_lshlrev_b32 v156, 4, v7\n"
    "v_mfma_f32_16x16x32_bf16 v[88:91], v[36:39], v[200:203], v[88:91]\n"
    "ds_read_b64_tr_b16 v[52:53], v26 offset:19328\n"
    "ds_read_b64_tr_b16 v[54:55], v26 offset:20096\n"
    "v_mfma_f32_16x16x32_bf16 v[92:95], v[36:39], v[204:207], v[92:95]\n"
    "v_mad_u32_u24 v156, v6, s95, v156\n"
    "s_mul_i32 s94, s48, 8704\n"
    "s_add_u32 s94, s94, 0x10000\n"
    "v_add_u32 v156, s94, v156\n"
    "v_lshlrev_b32 v158, 10, v158\n"
    "s_waitcnt lgkmcnt(4)\n"
    "v_mfma_f32_16x16x32_bf16 v[96:99], v[40:43], v[200:203], v[96:99]\n"
    "ds_read_b64_tr_b16 v[56:57], v26 offset:6944\n"
    "ds_read_b64_tr_b16 v[58:59], v26 offset:7712\n"
    "v_mfma_f32_16x16x32_bf16 v[100:103], v[40:43], v[204:207], v[100:103]\n"
    "v_lshl_or_b32 v158, v138, 4, v158\n"
    "v_and_b32 v130, 16, v0\n"
    "v_lshlrev_b32 v130, 1, v130\n"
    "v_xor_b32 v130, v130, v158\n"
    "v_xor_b32 v131, 64, v130\n"
    "v_mfma_f32_16x16x32_bf16 v[104:107], v[44:47], v[200:203], v[104:107]\n"
    "ds_read_b64_tr_b16 v[60:61], v26 offset:19360\n"
    "ds_read_b64_tr_b16 v[62:63], v26 offset:20128\n"
    "v_mfma_f32_16x16x32_bf16 v[108:111], v[44:47], v[204:207], v[108:111]\n"
    "v_add_u32 v132, 14336, v130\n"
    "v_add_u32 v133, 21504, v131\n"
    "v_add_u32 v131, 7168, v131\n"
    "v_bfe_u32 v138, v0, 2, 2\n"
    "v_lshl_or_b32 v144, v7, 3, v138\n"
    "s_waitcnt lgkmcnt(4)\n"
    "v_mfma_f32_16x16x32_bf16 v[64:67], v[48:51], v[208:211], v[64:67]\n"
    "ds_read_b64_tr_b16 v[32:33], v26 offset:6976\n"
    "ds_read_b64_tr_b16 v[34:35], v26 offset:7744\n"
    "v_mfma_f32_16x16x32_bf16 v[68:71], v[48:51], v[212:215], v[68:71]\n"
    "v_lshlrev_b32 v144, 7, v144\n"
    "v_bfe_u32 v138, v0, 3, 2\n"
    "v_lshl_or_b32 v144, v138, 5, v144\n"
    "v_and_b32 v138, 3, v0\n"
    "v_lshl_or_b32 v144, v138, 3, v144\n"
    "v_mfma_f32_16x16x32_bf16 v[72:75], v[52:55], v[208:211], v[72:75]\n"
    "ds_read_b64_tr_b16 v[36:37], v26 offset:19392\n"
    "ds_read_b64_tr_b16 v[38:39], v26 offset:20160\n"
    "v_mfma_f32_16x16x32_bf16 v[76:79], v[52:55], v[212:215], v[76:79]\n"
    "s_lshl_b32 s94, s57, 13\n"
    "v_or_b32 v144, s94, v144\n"
    "v_xor_b32 v147, 32, v144\n"
    "v_xor_b32 v150, 64, v144\n"
    "v_xor_b32 v153, 96, v144\n"
    "s_waitcnt lgkmcnt(4)\n"
    "v_mfma_f32_16x16x32_bf16 v[80:83], v[56:59], v[208:211], v[80:83]\n"
    "ds_read_b64_tr_b16 v[40:41], v26 offset:13120\n"
    "ds_read_b64_tr_b16 v[42:43], v26 offset:13888\n"
    "v_mfma_f32_16x16x32_bf16 v[84:87], v[56:59], v[212:215], v[84:87]\n"
    "v_add_u32 v145, 0x10000, v144\n"
    "v_add_u32 v146, 0x20000, v144\n"
    "v_add_u32 v148, 0x10000, v147\n"
    "v_add_u32 v149, 0x20000, v147\n"
    "v_add_u32 v151, 0x10000, v150\n"
    "v_mfma_f32_16x16x32_bf16 v[88:91], v[60:63], v[208:211], v[88:91]\n"
    "ds_read_b64_tr_b16 v[44:45], v26 offset:25536\n"
    "ds_read_b64_tr_b16 v[46:47], v26 offset:26304\n"
    "v_mfma_f32_16x16x32_bf16 v[92:95], v[60:63], v[212:215], v[92:95]\n"
    "v_add_u32 v152, 0x20000, v150\n"
    "v_add_u32 v154, 0x10000, v153\n"
    "v_add_u32 v155, 0x20000, v153\n"
    "v_and_b32 v138, 27, v0\n"
    "v_cmp_eq_u32 vcc, 16, v138\n"
    "s_waitcnt lgkmcnt(4)\n"
    "v_mfma_f32_16x16x32_bf16 v[96:99], v[32:35], v[208:211], v[96:99]\n"
    "ds_read_b64_tr_b16 v[48:49], v26 offset:13152\n"
    "ds_read_b64_tr_b16 v[50:51], v26 offset:13920\n"
    "v_mfma_f32_16x16x32_bf16 v[100:103], v[32:35], v[212:215], v[100:103]\n"
    "v_mov_b32 v138, 0x3f803f80\n"
    "v_mov_b64 v[176:177], 0\n"
    "v_cndmask_b32 v168, 0, v138, vcc\n"
    "v_mov_b32 v169, v168\n"
    "v_mov_b64 v[170:171], v[168:169]\n"
    "v_mfma_f32_16x16x32_bf16 v[104:107], v[36:39], v[208:211], v[104:107]\n"
    "ds_read_b64_tr_b16 v[52:53], v26 offset:25568\n"
    "ds_read_b64_tr_b16 v[54:55], v26 offset:26336\n"
    "v_mfma_f32_16x16x32_bf16 v[108:111], v[36:39], v[212:215], v[108:111]\n"
    "v_mov_b64 v[172:173], v[168:169]\n"
    "v_mov_b64 v[174:175], v[168:169]\n"
    "v_mov_b64 v[178:179], 0\n"
    "v_mov_b64 v[180:181], 0\n"
    "v_mov_b64 v[182:183], 0\n"
    "s_waitcnt lgkmcnt(4)\n"
    "v_mfma_f32_16x16x32_bf16 v[64:67], v[40:43], v[216:219], v[64:67]\n"
    "ds_read_b64_tr_b16 v[56:57], v26 offset:13184\n"
    "ds_read_b64_tr_b16 v[58:59], v26 offset:13952\n"
    "v_mfma_f32_16x16x32_bf16 v[68:71], v[40:43], v[220:223], v[68:71]\n"
    "v_mov_b64 v[184:185], 0\n"
    "v_mov_b64 v[186:187], 0\n"
    "v_mov_b64 v[188:189], 0\n"
    "v_mov_b64 v[190:191], 0\n"
    "v_mfma_f32_16x16x32_bf16 v[72:75], v[44:47], v[216:219], v[72:75]\n"
    "ds_read_b64_tr_b16 v[60:61], v26 offset:25600\n"
    "ds_read_b64_tr_b16 v[62:63], v26 offset:26368\n"
    "v_mfma_f32_16x16x32_bf16 v[76:79], v[44:47], v[220:223], v[76:79]\n"
    "s_barrier\n"
    "s_waitcnt lgkmcnt(4)\n"
    "v_mfma_f32_16x16x32_bf16 v[80:83], v[48:51], v[216:219], v[80:83]\n"
    "ds_read_b64_tr_b16 v[32:33], v26 offset:40192\n"
    "ds_read_b64_tr_b16 v[34:35], v26 offset:40960\n"
    "v_mfma_f32_16x16x32_bf16 v[84:87], v[48:51], v[220:223], v[84:87]\n"
    "v_mfma_f32_16x16x32_bf16 v[88:91], v[52:55], v[216:219], v[88:91]\n"
    "ds_read_b64_tr_b16 v[36:37], v26 offset:52608\n"
    "ds_read_b64_tr_b16 v[38:39], v26 offset:53376\n"
    "v_mfma_f32_16x16x32_bf16 v[92:95], v[52:55], v[220:223], v[92:95]\n"
    "s_waitcnt lgkmcnt(4)\n"
    "v_mfma_f32_16x16x32_bf16 v[96:99], v[56:59], v[216:219], v[96:99]\n"
    "ds_read_b64_tr_b16 v[40:41], v26 offset:40224\n"
    "ds_read_b64_tr_b16 v[42:43], v26 offset:40992\n"
    "v_mfma_f32_16x16x32_bf16 v[100:103], v[56:59], v[220:223], v[100:103]\n"
    "v_mfma_f32_16x16x32_bf16 v[104:107], v[60:63], v[216:219], v[104:107]\n"
    "ds_read_b64_tr_b16 v[44:45], v26 offset:52640\n"
    "ds_read_b64_tr_b16 v[46:47], v26 offset:53408\n"
    "v_mfma_f32_16x16x32_bf16 v[108:111], v[60:63], v[220:223], v[108:111]\n"
    "s_waitcnt lgkmcnt(4)\n"
    "v_mfma_f32_16x16x32_bf16 v[64:67], v[32:35], v[224:227], v[64:67]\n"
    "ds_read_b64_tr_b16 v[48:49], v26 offset:40256\n"
    "ds_read_b64_tr_b16 v[50:51], v26 offset:41024\n"
    "v_mfma_f32_16x16x32_bf16 v[68:71], v[32:35], v[228:231], v[68:71]\n"
    "s_cmp_eq_u32 s48, 4\n"
    "s_cbranch_scc0 .Lskip_1\n"
    "buffer_load_dwordx2 v[138:139], v159, s[28:31], 0 offen sc1\n"
    ".Lskip_1:\n"
    "v_mfma_f32_16x16x32_bf16 v[72:75], v[36:39], v[224:227], v[72:75]\n"
    "ds_read_b64_tr_b16 v[52:53], v26 offset:52672\n"
    "ds_read_b64_tr_b16 v[54:55], v26 offset:53440\n"
    "v_mfma_f32_16x16x32_bf16 v[76:79], v[36:39], v[228:231], v[76:79]\n"
    "s_waitcnt lgkmcnt(4)\n"
    "v_mfma_f32_16x16x32_bf16 v[80:83], v[40:43], v[224:227], v[80:83]\n"
    "ds_read_b64_tr_b16 v[56:57], v26 offset:46400\n"
    "ds_read_b64_tr_b16 v[58:59], v26 offset:47168\n"
    "v_mfma_f32_16x16x32_bf16 v[84:87], v[40:43], v[228:231], v[84:87]\n"
    "v_mfma_f32_16x16x32_bf16 v[88:91], v[44:47], v[224:227], v[88:91]\n"
    "ds_read_b64_tr_b16 v[60:61], v26 offset:58816\n"
    "ds_read_b64_tr_b16 v[62:63], v26 offset:59584\n"
    "v_mfma_f32_16x16x32_bf16 v[92:95], v[44:47], v[228:231], v[92:95]\n"
    "s_waitcnt lgkmcnt(4)\n"
    "v_mfma_f32_16x16x32_bf16 v[96:99], v[48:51], v[224:227], v[96:99]\n"
    "ds_read_b64_tr_b16 v[32:33], v26 offset:46432\n"
    "ds_read_b64_tr_b16 v[34:35], v26 offset:47200\n"
    "v_mfma_f32_16x16x32_bf16 v[100:103], v[48:51], v[228:231], v[100:103]\n"
    "v_mfma_f32_16x16x32_bf16 v[104:107], v[52:55], v[224:227], v[104:107]\n"
    "ds_read_b64_tr_b16 v[36:37], v26 offset:58848\n"
    "ds_read_b64_tr_b16 v[38:39], v26 offset:59616\n"
    "v_mfma_f32_16x16x32_bf16 v[108:111], v[52:55], v[228:231], v[108:111]\n"
    "s_waitcnt lgkmcnt(4)\n"
    "v_mfma_f32_16x16x32_bf16 v[64:67], v[56:59], v[232:235], v[64:67]\n"
    "ds_read_b64_tr_b16 v[40:41], v26 offset:46464\n"
    "ds_read_b64_tr_b16 v[42:43], v26 offset:47232\n"
    "v_mfma_f32_16x16x32_bf16 v[68:71], v[56:59], v[236:239], v[68:71]\n"
    "v_mfma_f32_16x16x32_bf16 v[72:75], v[60:63], v[232:235], v[72:75]\n"
    "ds_read_b64_tr_b16 v[44:45], v26 offset:58880\n"
    "ds_read_b64_tr_b16 v[46:47], v26 offset:59648\n"
    "v_mfma_f32_16x16x32_bf16 v[76:79], v[60:63], v[236:239], v[76:79]\n"
    "s_cmp_eq_u32 s48, 4\n"
    "s_cbranch_scc0 .Lskip_2\n"
    "s_waitcnt vmcnt(0)\n"
    "v_cmp_ne_u64 vcc, s[40:41], v[138:139]\n"
    "s_mov_b32 s94, 0x20000\n"
    ".Lpoll_3:\n"
    "s_cbranch_vccz .Ldone_4\n"
    "s_sleep 1\n"
    "buffer_load_dwordx2 v[138:139], v159, s[28:31], 0 offen sc1\n"
    "s_waitcnt vmcnt(0)\n"
    "v_cmp_ne_u64 vcc, s[40:41], v[138:139]\n"
    "s_sub_u32 s94, s94, 1\n"
    "s_cmp_lg_u32 s94, 0\n"
    "s_cbranch_scc1 .Lpoll_3\n"
    ".Ldone_4:\n"
    ".Lskip_2:\n"
    "s_barrier\n"
    "s_setprio 1\n"
    "s_waitcnt lgkmcnt(4)\n"
    "v_mfma_f32_16x16x32_bf16 v[80:83], v[32:35], v[232:235], v[80:83]\n"
    "ds_read_b64_tr_b16 v[48:49], v27 offset:7936\n"
    "ds_read_b64_tr_b16 v[50:51], v27 offset:8704\n"
    "v_mfma_f32_16x16x32_bf16 v[84:87], v[32:35], v[236:239], v[84:87]\n"
    "buffer_load_dwordx4 v[160:163], v1, s[28:31], s80 offen sc1\n"
    "s_add_u32 m0, s79, 0x0\n"
    "s_nop 0\n"
    "buffer_load_dwordx4 v1, s[28:31], s78 offen offset:0 sc1 lds\n"
    "buffer_load_dwordx4 v1, s[28:31], s78 offen offset:1024 sc1 lds\n"
    "buffer_load_dwordx4 v1, s[28:31], s78 offen offset:2048 sc1 lds\n"
    "buffer_load_dwordx4 v1, s[28:31], s78 offen offset:3072 sc1 lds\n"
    "v_mfma_f32_16x16x32_bf16 v[88:91], v[36:39], v[232:235], v[88:91]\n"
    "ds_read_b64_tr_b16 v[52:53], v27 offset:20352\n"
    "ds_read_b64_tr_b16 v[54:55], v27 offset:21120\n"
    "v_mfma_f32_16x16x32_bf16 v[92:95], v[36:39], v[236:239], v[92:95]\n"
    "s_waitcnt lgkmcnt(4)\n"
    "v_mfma_f32_16x16x32_bf16 v[96:99], v[40:43], v[232:235], v[96:99]\n"
    "ds_read_b64_tr_b16 v[56:57], v27 offset:7968\n"
    "ds_read_b64_tr_b16 v[58:59], v27 offset:8736\n"
    "v_mfma_f32_16x16x32_bf16 v[100:103], v[40:43], v[236:239], v[100:103]\n"
    "v_mfma_f32_16x16x32_bf16 v[104:107], v[44:47], v[232:235], v[104:107]\n"
    "ds_read_b64_tr_b16 v[60:61], v27 offset:20384\n"
    "ds_read_b64_tr_b16 v[62:63], v27 offset:21152\n"
    "v_mfma_f32_16x16x32_bf16 v[108:111], v[44:47], v[236:239], v[108:111]\n"
    "s_waitcnt lgkmcnt(4)\n"
    "v_mfma_f32_16x16x32_bf16 v[64:67], v[48:51], v[240:243], v[64:67]\n"
    "ds_read_b64_tr_b16 v[32:33], v27 offset:8000\n"
    "ds_read_b64_tr_b16 v[34:35], v27 offset:8768\n"
    "v_mfma_f32_16x16x32_bf16 v[68:71], v[48:51], v[244:247], v[68:71]\n"
    "v_mfma_f32_16x16x32_bf16 v[72:75], v[52:55], v[240:243], v[72:75]\n"
    "ds_read_b64_tr_b16 v[36:37], v27 offset:20416\n"
    "ds_read_b64_tr_b16 v[38:39], v27 offset:21184\n"
    "v_mfma_f32_16x16x32_bf16 v[76:79], v[52:55], v[244:247], v[76:79]\n"
    "s_waitcnt lgkmcnt(4)\n"
    "v_mfma_f32_16x16x32_bf16 v[80:83], v[56:59], v[240:243], v[80:83]\n"
    "ds_read_b64_tr_b16 v[40:41], v27 offset:14144\n"
    "ds_read_b64_tr_b16 v[42:43], v27 offset:14912\n"
    "v_mfma_f32_16x16x32_bf16 v[84:87], v[56:59], v[244:247], v[84:87]\n"
    "v_mfma_f32_16x16x32_bf16 v[88:91], v[60:63], v[240:243], v[88:91]\n"
    "ds_read_b64_tr_b16 v[44:45], v27 offset:26560\n"
    "ds_read_b64_tr_b16 v[46:47], v27 offset:27328\n"
    "v_mfma_f32_16x16x32_bf16 v[92:95], v[60:63], v[244:247], v[92:95]\n"
    "s_waitcnt lgkmcnt(4)\n"
    "v_mfma_f32_16x16x32_bf16 v[96:99], v[32:35], v[240:243], v[96:99]\n"
    "ds_read_b64_tr_b16 v[48:49], v27 offset:14176\n"
    "ds_read_b64_tr_b16 v[50:51], v27 offset:14944\n"
    "v_mfma_f32_16x16x32_bf16 v[100:103], v[32:35], v[244:247], v[100:103]\n"
    "v_mfma_f32_16x16x32_bf16 v[104:107], v[36:39], v[240:243], v[104:107]\n"
    "ds_read_b64_tr_b16 v[52:53], v27 offset:26592\n"
    "ds_read_b64_tr_b16 v[54:55], v27 offset:27360\n"
    "v_mfma_f32_16x16x32_bf16 v[108:111], v[36:39], v[244:247], v[108:111]\n"
    "s_waitcnt lgkmcnt(4)\n"
    "v_mfma_f32_16x16x32_bf16 v[64:67], v[40:43], v[248:251], v[64:67]\n"
    "ds_read_b64_tr_b16 v[56:57], v27 offset:14208\n"
    "ds_read_b64_tr_b16 v[58:59], v27 offset:14976\n"
    "v_mfma_f32_16x16x32_bf16 v[68:71], v[40:43], v[252:255], v[68:71]\n"
    "v_mfma_f32_16x16x32_bf16 v[72:75], v[44:47], v[248:251], v[72:75]\n"
    "ds_read_b64_tr_b16 v[60:61], v27 offset:26624\n"
    "ds_read_b64_tr_b16 v[62:63], v27 offset:27392\n"
    "v_mfma_f32_16x16x32_bf16 v[76:79], v[44:47], v[252:255], v[76:79]\n"
    "s_waitcnt lgkmcnt(4)\n"
    "v_mfma_f32_16x16x32_bf16 v[80:83], v[48:51], v[248:251], v[80:83]\n"
    "v_mfma_f32_16x16x32_bf16 v[84:87], v[48:51], v[252:255], v[84:87]\n"
    "v_mfma_f32_16x16x32_bf16 v[88:91], v[52:55], v[248:251], v[88:91]\n"
    "v_mfma_f32_16x16x32_bf16 v[92:95], v[52:55], v[252:255], v[92:95]\n"
    "s_waitcnt lgkmcnt(0)\n"
    "v_mfma_f32_16x16x32_bf16 v[96:99], v[56:59], v[248:251], v[96:99]\n"
    "v_mfma_f32_16x16x32_bf16 v[100:103], v[56:59], v[252:255], v[100:103]\n"
    "v_mfma_f32_16x16x32_bf16 v[104:107], v[60:63], v[248:251], v[104:107]\n"
    "v_mfma_f32_16x16x32_bf16 v[108:111], v[60:63], v[252:255], v[108:111]\n"
    "s_add_u32 m0, s79, 0x8000\n"
    "s_add_u32 s78, s78, 0x8000\n"
    "buffer_load_dwordx4 v1, s[28:31], s78 offen offset:0 sc1 lds\n"
    "buffer_load_dwordx4 v1, s[28:31], s78 offen offset:1024 sc1 lds\n"
    "buffer_load_dwordx4 v1, s[28:31], s78 offen offset:2048 sc1 lds\n"
    "buffer_load_dwordx4 v1, s[28:31], s78 offen offset:3072 sc1 lds\n"
    "s_lshl_b32 s94, s55, 10\n"
    "s_add_u32 s96, s81, s94\n"
    "v_mov_b32 v40, s45\n"
    "v_pk_mul_f32 v[32:33], v[64:65], v[64:65]\n"
    "v_pk_mul_f32 v[34:35], v[66:67], v[66:67]\n"
    "v_pk_mul_f32 v[36:37], v[68:69], v[68:69]\n"
    "v_pk_mul_f32 v[38:39], v[70:71], v[70:71]\n"
    "v_pk_fma_f32 v[32:33], v[32:33], s[44:45], v[40:41] op_sel_hi:[1,0,0]\n"
    "v_pk_fma_f32 v[34:35], v[34:35], s[44:45], v[40:41] op_sel_hi:[1,0,0]\n"
    "v_pk_fma_f32 v[36:37], v[36:37], s[44:45], v[40:41] op_sel_hi:[1,0,0]\n"
    "v_pk_fma_f32 v[38:39], v[38:39], s[44:45], v[40:41] op_sel_hi:[1,0,0]\n"
    "v_pk_mul_f32 v[32:33], v[32:33], v[64:65]\n"
    "v_pk_mul_f32 v[34:35], v[34:35], v[66:67]\n"
    "v_pk_mul_f32 v[36:37], v[36:37], v[68:69]\n"
    "v_pk_mul_f32 v[38:39], v[38:39], v[70:71]\n"
    "v_exp_f32 v32, v32\n"
    "v_exp_f32 v33, v33\n"
    "v_exp_f32 v34, v34\n"
    "v_exp_f32 v35, v35\n"
    "v_exp_f32 v36, v36\n"
    "v_exp_f32 v37, v37\n"
    "v_exp_f32 v38, v38\n"
    "v_exp_f32 v39, v39\n"
    "v_pk_add_f32 v[32:33], v[32:33], 1.0 op_sel_hi:[1,0]\n"
    "v_pk_add_f32 v[34:35], v[34:35], 1.0 op_sel_hi:[1,0]\n"
    "v_pk_add_f32 v[36:37], v[36:37], 1.0 op_sel_hi:[1,0]\n"
    "v_pk_add_f32 v[38:39], v[38:39], 1.0 op_sel_hi:[1,0]\n"
    "v_rcp_f32 v32, v32\n"
    "v_rcp_f32 v33, v33\n"
    "v_rcp_f32 v34, v34\n"
    "v_rcp_f32 v35, v35\n"
    "v_rcp_f32 v36, v36\n"
    "v_rcp_f32 v37, v37\n"
    "v_rcp_f32 v38, v38\n"
    "v_rcp_f32 v39, v39\n"
    "v_pk_mul_f32 v[72:73], v[72:73], v[64:65]\n"
    "v_pk_mul_f32 v[74:75], v[74:75], v[66:67]\n"
    "v_pk_mul_f32 v[76:77], v[76:77], v[68:69]\n"
    "v_pk_mul_f32 v[78:79], v[78:79], v[70:71]\n"
    "v_pk_mul_f32 v[32:33], v[32:33], v[72:73]\n"
    "v_pk_mul_f32 v[34:35], v[34:35], v[74:75]\n"
    "v_pk_mul_f32 v[36:37], v[36:37], v[76:77]\n"
    "v_pk_mul_f32 v[38:39], v[38:39], v[78:79]\n"
    "v_cvt_pk_bf16_f32 v8, v32, v33\n"
    "v_cvt_pk_bf16_f32 v10, v36, v37\n"
    "v_cvt_pk_bf16_f32 v9, v34, v35\n"
    "v_cvt_pk_bf16_f32 v11, v38, v39\n"
    "v_permlane16_swap_b32 v8, v10\n"
    "s_nop 0\n"
    "v_permlane16_swap_b32 v9, v11\n"
    "buffer_store_dwordx4 v[8:11], v1, s[28:31], s96 offen offset:0 sc1\n"
    "v_pk_mul_f32 v[32:33], v[80:81], v[80:81]\n"
    "v_pk_mul_f32 v[34:35], v[82:83], v[82:83]\n"
    "v_pk_mul_f32 v[36:37], v[84:85], v[84:85]\n"
    "v_pk_mul_f32 v[38:39], v[86:87], v[86:87]\n"
    "v_pk_fma_f32 v[32:33], v[32:33], s[44:45], v[40:41] op_sel_hi:[1,0,0]\n"
    "v_pk_fma_f32 v[34:35], v[34:35], s[44:45], v[40:41] op_sel_hi:[1,0,0]\n"
    "v_pk_fma_f32 v[36:37], v[36:37], s[44:45], v[40:41] op_sel_hi:[1,0,0]\n"
    "v_pk_fma_f32 v[38:39], v[38:39], s[44:45], v[40:41] op_sel_hi:[1,0,0]\n"
    "v_pk_mul_f32 v[32:33], v[32:33], v[80:81]\n"
    "v_pk_mul_f32 v[34:35], v[34:35], v[82:83]\n"
    "v_pk_mul_f32 v[36:37], v[36:37], v[84:85]\n"
    "v_pk_mul_f32 v[38:39], v[38:39], v[86:87]\n"
    "v_exp_f32 v32, v32\n"
    "v_exp_f32 v33, v33\n"
    "v_exp_f32 v34, v34\n"
    "v_exp_f32 v35, v35\n"
    "v_exp_f32 v36, v36\n"
    "v_exp_f32 v37, v37\n"
    "v_exp_f32 v38, v38\n"
    "v_exp_f32 v39, v39\n"
    "v_pk_add_f32 v[32:33], v[32:33], 1.0 op_sel_hi:[1,0]\n"
    "v_pk_add_f32 v[34:35], v[34:35], 1.0 op_sel_hi:[1,0]\n"
    "v_pk_add_f32 v[36:37], v[36:37], 1.0 op_sel_hi:[1,0]\n"
    "v_pk_add_f32 v[38:39], v[38:39], 1.0 op_sel_hi:[1,0]\n"
    "v_rcp_f32 v32, v32\n"
    "v_rcp_f32 v33, v33\n"
    "v_rcp_f32 v34, v34\n"
    "v_rcp_f32 v35, v35\n"
    "v_rcp_f32 v36, v36\n"
    "v_rcp_f32 v37, v37\n"
    "v_rcp_f32 v38, v38\n"
    "v_rcp_f32 v39, v39\n"
    "v_pk_mul_f32 v[88:89], v[88:89], v[80:81]\n"
    "v_pk_mul_f32 v[90:91], v[90:91], v[82:83]\n"
    "v_pk_mul_f32 v[92:93], v[92:93], v[84:85]\n"
    "v_pk_mul_f32 v[94:95], v[94:95], v[86:87]\n"
    "v_pk_mul_f32 v[32:33], v[32:33], v[88:89]\n"
    "v_pk_mul_f32 v[34:35], v[34:35], v[90:91]\n"
    "v_pk_mul_f32 v[36:37], v[36:37], v[92:93]\n"
    "v_pk_mul_f32 v[38:39], v[38:39], v[94:95]\n"
    "v_cvt_pk_bf16_f32 v12, v32, v33\n"
    "v_cvt_pk_bf16_f32 v14, v36, v37\n"
    "v_cvt_pk_bf16_f32 v13, v34, v35\n"
    "v_cvt_pk_bf16_f32 v15, v38, v39\n"
    "v_permlane16_swap_b32 v12, v14\n"
    "s_nop 0\n"
    "v_permlane16_swap_b32 v13, v15\n"
    "buffer_store_dwordx4 v[12:15], v1, s[28:31], s96 offen offset:1024 sc1\n"
    "v_pk_mul_f32 v[32:33], v[96:97], v[96:97]\n"
    "v_pk_mul_f32 v[34:35], v[98:99], v[98:99]\n"
    "v_pk_mul_f32 v[36:37], v[100:101], v[100:101]\n"
    "v_pk_mul_f32 v[38:39], v[102:103], v[102:103]\n"
    "v_pk_fma_f32 v[32:33], v[32:33], s[44:45], v[40:41] op_sel_hi:[1,0,0]\n"
    "v_pk_fma_f32 v[34:35], v[34:35], s[44:45], v[40:41] op_sel_hi:[1,0,0]\n"
    "v_pk_fma_f32 v[36:37], v[36:37], s[44:45], v[40:41] op_sel_hi:[1,0,0]\n"
    "v_pk_fma_f32 v[38:39], v[38:39], s[44:45], v[40:41] op_sel_hi:[1,0,0]\n"
    "v_pk_mul_f32 v[32:33], v[32:33], v[96:97]\n"
    "v_pk_mul_f32 v[34:35], v[34:35], v[98:99]\n"
    "v_pk_mul_f32 v[36:37], v[36:37], v[100:101]\n"
    "v_pk_mul_f32 v[38:39], v[38:39], v[102:103]\n"
    "v_exp_f32 v32, v32\n"
    "v_exp_f32 v33, v33\n"
    "v_exp_f32 v34, v34\n"
    "v_exp_f32 v35, v35\n"
    "v_exp_f32 v36, v36\n"
    "v_exp_f32 v37, v37\n"
    "v_exp_f32 v38, v38\n"
    "v_exp_f32 v39, v39\n"
    "v_pk_add_f32 v[32:33], v[32:33], 1.0 op_sel_hi:[1,0]\n"
    "v_pk_add_f32 v[34:35], v[34:35], 1.0 op_sel_hi:[1,0]\n"
    "v_pk_add_f32 v[36:37], v[36:37], 1.0 op_sel_hi:[1,0]\n"
    "v_pk_add_f32 v[38:39], v[38:39], 1.0 op_sel_hi:[1,0]\n"
    "v_rcp_f32 v32, v32\n"
    "v_rcp_f32 v33, v33\n"
    "v_rcp_f32 v34, v34\n"
    "v_rcp_f32 v35, v35\n"
    "v_rcp_f32 v36, v36\n"
    "v_rcp_f32 v37, v37\n"
    "v_rcp_f32 v38, v38\n"
    "v_rcp_f32 v39, v39\n"
    "v_pk_mul_f32 v[104:105], v[104:105], v[96:97]\n"
    "v_pk_mul_f32 v[106:107], v[106:107], v[98:99]\n"
    "v_pk_mul_f32 v[108:109], v[108:109], v[100:101]\n"
    "v_pk_mul_f32 v[110:111], v[110:111], v[102:103]\n"
    "v_pk_mul_f32 v[32:33], v[32:33], v[104:105]\n"
    "v_pk_mul_f32 v[34:35], v[34:35], v[106:107]\n"
    "v_pk_mul_f32 v[36:37], v[36:37], v[108:109]\n"
    "v_pk_mul_f32 v[38:39], v[38:39], v[110:111]\n"
    "v_cvt_pk_bf16_f32 v8, v32, v33\n"
    "v_cvt_pk_bf16_f32 v10, v36, v37\n"
    "v_cvt_pk_bf16_f32 v9, v34, v35\n"
    "v_cvt_pk_bf16_f32 v11, v38, v39\n"
    "v_permlane16_swap_b32 v8, v10\n"
    "s_nop 0\n"
    "v_permlane16_swap_b32 v9, v11\n"
    "s_cmp_lt_u32 s50, 14\n"
    "s_cbranch_scc0 .Lskip_5\n"
    "buffer_store_dwordx4 v[8:11], v1, s[28:31], s96 offen offset:2048 sc1\n"
    ".Lskip_5:\n"
    "s_waitcnt vmcnt(6)\n"
    "s_branch .Ljoin\n"
    ".Lh:\n"
    "s_cmp_eq_u32 s48, 0\n"
    "s_cbranch_scc0 .Lskip_6\n"
    "s_getpc_b64 s[96:97]\n"
    ".Lpc:\n"
    "s_sub_u32 s96, s96, .Lpc-.Lkbegin\n"
    "s_subb_u32 s97, s97, 0\n"
    "s_bfe_u32 s94, s2, 0x30003\n"
    "v_lshl_add_u32 v8, s94, 6, v0\n"
    "v_lshlrev_b32 v8, 7, v8\n"
    "v_min_u32 v8, .Lkend-.Lkbegin-4, v8\n"
    "s_mov_b32 m0, 0\n"
    "s_nop 0\n"
    "global_load_lds_dword v8, s[96:97]\n"
    ".Lskip_6:\n"
    "s_and_b32 s94, s2, 7\n"
    "s_bfe_u32 s95, s2, 0x20003\n"
    "s_lshl2_add_u32 s53, s94, s95\n"
    "s_lshr_b32 s54, s2, 5\n"
    "s_mul_i32 s94, s50, 3\n"
    "s_add_u32 s94, s94, 40\n"
    "s_lshl1_add_u32 s95, s50, 54\n"
    "s_min_u32 s94, s94, s95\n"
    "s_mul_i32 s95, s50, 5\n"
    "s_cmp_lt_u32 s50, 12\n"
    "s_cselect_b32 s55, s95, s94\n"
    "s_min_u32 s94, s55, 80\n"
    "s_and_b32 s56, s94, 0xfe\n"
    "s_add_u32 s94, s52, 0\n"
    "s_and_b32 s94, s94, 7\n"
    "s_mul_i32 s94, s94, 11008\n"
    "s_lshl1_add_u32 s94, s56, s94\n"
    "s_lshl_b32 s67, s94, 4\n"
    "s_add_u32 s94, s52, 1\n"
    "s_and_b32 s94, s94, 7\n"
    "s_mul_i32 s94, s94, 11008\n"
    "s_lshl1_add_u32 s94, s56, s94\n"
    "s_lshl_b32 s68, s94, 4\n"
    "s_add_u32 s94, s52, 2\n"
    "s_and_b32 s94, s94, 7\n"
    "s_mul_i32 s94, s94, 11008\n"
    "s_lshl1_add_u32 s94, s56, s94\n"
    "s_lshl_b32 s69, s94, 4\n"
    "s_add_u32 s94, s52, 3\n"
    "s_and_b32 s94, s94, 7\n"
    "s_mul_i32 s94, s94, 11008\n"
    "s_lshl1_add_u32 s94, s56, s94\n"
    "s_lshl_b32 s70, s94, 4\n"
    "s_add_u32 s94, s52, 4\n"
    "s_and_b32 s94, s94, 7\n"
    "s_mul_i32 s94, s94, 11008\n"
    "s_lshl1_add_u32 s94, s56, s94\n"
    "s_lshl_b32 s71, s94, 4\n"
    "s_add_u32 s94, s52, 5\n"
    "s_and_b32 s94, s94, 7\n"
    "s_mul_i32 s94, s94, 11008\n"
    "s_lshl1_add_u32 s94, s56, s94\n"
    "s_lshl_b32 s72, s94, 4\n"
    "s_add_u32 s94, s52, 6\n"
    "s_and_b32 s94, s94, 7\n"
    "s_mul_i32 s94, s94, 11008\n"
    "s_lshl1_add_u32 s94, s56, s94\n"
    "s_lshl_b32 s73, s94, 4\n"
    "s_add_u32 s94, s52, 7\n"
    "s_and_b32 s94, s94, 7\n"
    "s_mul_i32 s94, s94, 11008\n"
    "s_lshl1_add_u32 s94, s56, s94\n"
    "s_lshl_b32 s74, s94, 4\n"
    "s_mul_i32 s66, s49, 3104\n"
    "s_lshr_b32 s57, s48, 1\n"
    "s_lshl_b32 s79, s48, 12\n"
    "s_lshl_b32 s94, s53, 17\n"
    "s_add_u32 s94, s94, s79\n"
    "s_add_u32 s78, s94, 0x300000\n"
    "s_lshl3_add_u32 s95, s54, s48\n"
    "s_lshl_b32 s94, s53, 6\n"
    "s_add_u32 s94, s94, s95\n"
    "s_lshl_b32 s94, s94, 10\n"
    "s_add_u32 s80, s94, 0x100000\n"
    "s_lshl_b32 s94, s95, 7\n"
    "s_add_u32 s89, s94, 0x80000\n"
    "s_lshl_b32 s94, s95, 5\n"
    "s_add_u32 s94, s94, s53\n"
    "s_lshl_b32 s90, s94, 10\n"
    "s_lshl4_add_u32 s94, s51, s50\n"
    "s_lshl_b32 s94, s94, 7\n"
    "s_add_u32 s77, s94, 0x1000\n"
    "s_mul_i32 s94, s58, 86\n"
    "s_lshl_b32 s94, s94, 10\n"
    "s_add_u32 s81, s94, 0x900000\n"
    "s_and_b32 s96, s48, 1\n"
    "s_lshl1_add_u32 s97, s53, s96\n"
    "s_lshl_b32 s94, s97, 7\n"
    "s_add_u32 s88, s94, 0x80000\n"
    "s_mul_i32 s94, s97, 86\n"
    "s_mul_i32 s95, s57, 22\n"
    "s_add_u32 s94, s94, s95\n"
    "s_lshl_b32 s94, s94, 10\n"
    "s_add_u32 s86, s94, 0x900000\n"
    "s_lshl2_add_u32 s94, s97, s57\n"
    "s_lshl_b32 s87, s94, 13\n"
    "s_lshl_b32 s97, s54, 7\n"
    "s_lshl1_add_u32 s94, s96, s95\n"
    "s_lshl_b32 s94, s94, 14\n"
    "s_add_u32 s82, s94, s97\n"
    "s_add_u32 s94, s95, s96\n"
    "s_add_u32 s94, s94, 20\n"
    "s_cmp_lt_u32 s57, 3\n"
    "s_cselect_b32 s94, s94, 0\n"
    "s_lshl_b32 s94, s94, 14\n"
    "s_add_u32 s83, s94, s97\n"
    "s_lshl2_add_u32 s94, s57, s96\n"
    "s_lshl_b32 s94, s94, 15\n"
    "s_add_u32 s84, s94, s97\n"
    "s_lshl_b32 s94, s57, 13\n"
    "s_lshl_b32 s95, s96, 11\n"
    "s_add_u32 s85, s94, s95\n"
    "s_lshl3_add_u32 s94, s53, s48\n"
    "s_lshl_b32 s94, s94, 13\n"
    "s_add_u32 s91, s94, s97\n"
    "s_mov_b32 s42, 0xffff\n"
    "s_mov_b32 s43, 0xffff\n"
    "s_mov_b32 s44, 0xbdd2d3e8\n"
    "s_mov_b32 s45, 0xc0135761\n"
    "s_cmp_lt_u32 s50, 4\n"
    "s_cselect_b32 s46, 0x3eb8aa3b, 1.0\n"
    "v_and_b32 v6, 15, v0\n"
    "v_lshrrev_b32 v7, 4, v0\n"
    "v_lshrrev_b32 v8, 1, v6\n"
    "v_xor_b32 v8, v8, v7\n"
    "v_lshlrev_b32 v8, 4, v8\n"
    "v_lshl_or_b32 v8, v6, 7, v8\n"
    "s_lshl_b32 s94, s49, 12\n"
    "v_or_b32 v16, s94, v8\n"
    "v_xor_b32 v19, 64, v16\n"
    "v_add_u32 v17, 0x10000, v16\n"
    "v_add_u32 v18, 0x20000, v16\n"
    "v_add_u32 v20, 0x10000, v19\n"
    "v_add_u32 v21, 0x20000, v19\n"
    "s_lshr_b32 s94, s49, 1\n"
    "s_and_b32 s95, s49, 1\n"
    "s_lshl2_add_u32 s94, s94, s95\n"
    "s_lshl_b32 s94, s94, 3\n"
    "s_movk_i32 s95, 2752\n"
    "v_add_u32 v8, 0, v0\n"
    "v_mul_u32_u24 v9, 5462, v8\n"
    "v_lshrrev_b32 v9, 16, v9\n"
    "v_mul_u32_u24 v10, 12, v9\n"
    "v_sub_u32 v8, v8, v10\n"
    "v_and_b32 v10, 8, v9\n"
    "v_add3_u32 v9, v9, v10, s94\n"
    "v_lshlrev_b32 v8, 4, v8\n"
    "v_mad_u32_u24 v8, v9, s95, v8\n"
    "v_subrev_u32 v28, 0, v8\n"
    "v_add_u32 v8, 64, v0\n"
    "v_mul_u32_u24 v9, 5462, v8\n"
    "v_lshrrev_b32 v9, 16, v9\n"
    "v_mul_u32_u24 v10, 12, v9\n"
    "v_sub_u32 v8, v8, v10\n"
    "v_and_b32 v10, 8, v9\n"
    "v_add3_u32 v9, v9, v10, s94\n"
    "v_lshlrev_b32 v8, 4, v8\n"
    "v_mad_u32_u24 v8, v9, s95, v8\n"
    "v_subrev_u32 v29, 1024, v8\n"
    "v_add_u32 v8, 128, v0\n"
    "v_mul_u32_u24 v9, 5462, v8\n"
    "v_lshrrev_b32 v9, 16, v9\n"
    "v_mul_u32_u24 v10, 12, v9\n"
    "v_sub_u32 v8, v8, v10\n"
    "v_and_b32 v10, 8, v9\n"
    "v_add3_u32 v9, v9, v10, s94\n"
    "v_lshlrev_b32 v8, 4, v8\n"
    "v_mad_u32_u24 v8, v9, s95, v8\n"
    "v_subrev_u32 v30, 2048, v8\n"
    "v_bfe_u32 v8, v0, 4, 1\n"
    "v_mul_u32_u24 v8, 3104, v8\n"
    "v_lshrrev_b32 v9, 5, v0\n"
    "v_bfe_u32 v10, v0, 2, 2\n"
    "v_lshl_or_b32 v9, v9, 3, v10\n"
    "v_mul_u32_u24 v9, 192, v9\n"
    "v_and_b32 v10, 3, v0\n"
    "v_lshlrev_b32 v10, 3, v10\n"
    "v_add3_u32 v8, v8, v9, v10\n"
    "s_sub_u32 s94, s55, s56\n"
    "s_cmp_lt_u32 s48, 4\n"
    "s_cselect_b32 s95, 3, 0\n"
    "s_add_u32 s94, s94, s95\n"
    "s_lshl_b32 s94, s94, 5\n"
    "v_add_u32 v25, s94, v8\n"
    "v_add_u32 v26, 0x10000, v25\n"
    "v_add_u32 v27, 0x20000, v25\n"
    "v_bfe_u32 v8, v0, 2, 2\n"
    "v_mul_u32_u24 v8, 1056, v8\n"
    "v_lshrrev_b32 v9, 5, v0\n"
    "v_bfe_u32 v10, v0, 4, 1\n"
    "v_lshl_or_b32 v9, v9, 2, v10\n"
    "v_lshl_add_u32 v8, v9, 7, v8\n"
    "v_and_b32 v9, 3, v0\n"
    "v_lshl_add_u32 v22, v9, 3, v8\n"
    "v_add_u32 v23, 0x10000, v22\n"
    "v_add_u32 v24, 0x20000, v22\n"
    "v_and_b32 v8, 43, v0\n"
    "v_bfe_u32 v9, v0, 2, 1\n"
    "v_lshl_or_b32 v8, v9, 4, v8\n"
    "v_bfe_u32 v10, v0, 4, 1\n"
    "v_lshl_or_b32 v8, v10, 2, v8\n"
    "v_and_b32 v10, 51, v0\n"
    "v_lshl_or_b32 v10, v9, 3, v10\n"
    "v_bfe_u32 v9, v0, 3, 1\n"
    "v_lshl_or_b32 v10, v9, 2, v10\n"
    "s_cmp_lt_u32 s50, 8\n"
    "s_cselect_b64 vcc, -1, 0\n"
    "s_cselect_b32 s96, 0, 1024\n"
    "v_cndmask_b32 v8, v10, v8, vcc\n"
    "s_lshl_b32 s94, s58, 11\n"
    "s_add_u32 s94, s94, s96\n"
    "s_add_u32 s94, s94, 0x300000\n"
    "s_lshl_b32 s95, s58, 10\n"
    "s_add_u32 s95, s95, 0x100000\n"
    "s_cmp_lt_u32 s50, 4\n"
    "s_cselect_b64 vcc, -1, 0\n"
    "s_cselect_b32 s94, s95, s94\n"
    "s_cselect_b32 s96, 16, 17\n"
    "v_cndmask_b32 v8, v8, v0, vcc\n"
    "v_lshlrev_b32 v31, 4, v8\n"
    "s_lshl_b32 s76, 1, s96\n"
    "s_and_b32 s95, s50, 3\n"
    "s_mul_i32 s95, s95, s76\n"
    "s_lshl3_add_u32 s75, s95, s94\n"
    "s_waitcnt lgkmcnt(0)\n"
    "s_load_dwordx2 s[94:95], s[28:29], 0x0\n"
    "s_load_dwordx2 s[24:25], s[0:1], 0x10\n"
    "s_load_dwordx2 s[20:21], s[0:1], 0x28\n"
    "s_load_dwordx2 s[36:37], s[0:1], 0x30\n"
    "s_load_dwordx2 s[32:33], s[0:1], 0x38\n"
    "s_waitcnt lgkmcnt(0)\n"
    "s_lshr_b64 s[96:97], s[94:95], 29\n"
    "s_xor_b64 s[94:95], s[94:95], s[96:97]\n"
    "s_mul_i32 s96, s94, 0x9e3779b9\n"
    "s_mul_hi_u32 s97, s94, 0x7f4a7c15\n"
    "s_add_u32 s96, s96, s97\n"
    "s_mul_i32 s97, s95, 0x7f4a7c15\n"
    "s_add_u32 s96, s96, s97\n"
    "s_mul_i32 s94, s94, 0x7f4a7c15\n"
    "s_add_u32 s40, s94, 0xd9b4e019\n"
    "s_addc_u32 s41, s96, 0x632be59b\n"
    "v_mov_b32 v4, s40\n"
    "v_mov_b32 v5, s41\n"
    "s_and_b32 s94, s50, 12\n"
    "s_cmp_eq_u32 s94, 8\n"
    "s_cbranch_scc1 .Lhv\n"
    "s_waitcnt lgkmcnt(0)\n"
    "s_barrier\n"
    "ds_read_b128 v[128:131], v16 offset:30720\n"
    "ds_read_b128 v[132:135], v16 offset:32768\n"
    "ds_read_b64_tr_b16 v[32:33], v22 offset:47104\n"
    "ds_read_b64_tr_b16 v[34:35], v22 offset:47360\n"
    "ds_read_b64_tr_b16 v[36:37], v22 offset:47136\n"
    "ds_read_b64_tr_b16 v[38:39], v22 offset:47392\n"
    "ds_read_b64_tr_b16 v[40:41], v22 offset:47168\n"
    "ds_read_b64_tr_b16 v[42:43], v22 offset:47424\n"
    "ds_read_b64_tr_b16 v[44:45], v22 offset:47200\n"
    "ds_read_b64_tr_b16 v[46:47], v22 offset:47456\n"
    "ds_read_b64_tr_b16 v[48:49], v22 offset:51328\n"
    "ds_read_b64_tr_b16 v[50:51], v22 offset:51584\n"
    "s_waitcnt lgkmcnt(6)\n"
    "v_mfma_f32_16x16x32_bf16 v[64:67], v[32:35], v[128:131], 0\n"
    "ds_read_b64_tr_b16 v[52:53], v22 offset:51360\n"
    "ds_read_b64_tr_b16 v[54:55], v22 offset:51616\n"
    "v_mfma_f32_16x16x32_bf16 v[68:71], v[32:35], v[132:135], 0\n"
    "v_mfma_f32_16x16x32_bf16 v[72:75], v[36:39], v[128:131], 0\n"
    "ds_read_b64_tr_b16 v[56:57], v22 offset:51392\n"
    "ds_read_b64_tr_b16 v[58:59], v22 offset:51648\n"
    "v_mfma_f32_16x16x32_bf16 v[76:79], v[36:39], v[132:135], 0\n"
    "s_waitcnt lgkmcnt(6)\n"
    "v_mfma_f32_16x16x32_bf16 v[80:83], v[40:43], v[128:131], 0\n"
    "ds_read_b64_tr_b16 v[60:61], v22 offset:51424\n"
    "ds_read_b64_tr_b16 v[62:63], v22 offset:51680\n"
    "v_mfma_f32_16x16x32_bf16 v[84:87], v[40:43], v[132:135], 0\n"
    "v_mfma_f32_16x16x32_bf16 v[88:91], v[44:47], v[128:131], 0\n"
    "ds_read_b128 v[136:139], v19 offset:30720\n"
    "ds_read_b64_tr_b16 v[32:33], v22 offset:55552\n"
    "ds_read_b64_tr_b16 v[34:35], v22 offset:55808\n"
    "v_mfma_f32_16x16x32_bf16 v[92:95], v[44:47], v[132:135], 0\n"
    "s_waitcnt lgkmcnt(7)\n"
    "v_mfma_f32_16x16x32_bf16 v[96:99], v[48:51], v[128:131], 0\n"
    "ds_read_b128 v[140:143], v19 offset:32768\n"
    "ds_read_b64_tr_b16 v[36:37], v22 offset:55584\n"
    "ds_read_b64_tr_b16 v[38:39], v22 offset:55840\n"
    "v_mfma_f32_16x16x32_bf16 v[100:103], v[48:51], v[132:135], 0\n"
    "v_mfma_f32_16x16x32_bf16 v[104:107], v[52:55], v[128:131], 0\n"
    "ds_read_b64_tr_b16 v[40:41], v22 offset:55616\n"
    "ds_read_b64_tr_b16 v[42:43], v22 offset:55872\n"
    "v_mfma_f32_16x16x32_bf16 v[108:111], v[52:55], v[132:135], 0\n"
    "s_waitcnt lgkmcnt(8)\n"
    "v_mfma_f32_16x16x32_bf16 v[112:115], v[56:59], v[128:131], 0\n"
    "ds_read_b64_tr_b16 v[44:45], v22 offset:55648\n"
    "ds_read_b64_tr_b16 v[46:47], v22 offset:55904\n"
    "v_mfma_f32_16x16x32_bf16 v[116:119], v[56:59], v[132:135], 0\n"
    "v_mfma_f32_16x16x32_bf16 v[120:123], v[60:63], v[128:131], 0\n"
    "ds_read_b64_tr_b16 v[48:49], v22 offset:59776\n"
    "ds_read_b64_tr_b16 v[50:51], v22 offset:60032\n"
    "v_mfma_f32_16x16x32_bf16 v[124:127], v[60:63], v[132:135], 0\n"
    "s_waitcnt lgkmcnt(6)\n"
    "v_mfma_f32_16x16x32_bf16 v[64:67], v[32:35], v[136:139], v[64:67]\n"
    "ds_read_b64_tr_b16 v[52:53], v22 offset:59808\n"
    "ds_read_b64_tr_b16 v[54:55], v22 offset:60064\n"
    "v_mfma_f32_16x16x32_bf16 v[68:71], v[32:35], v[140:143], v[68:71]\n"
    "v_mfma_f32_16x16x32_bf16 v[72:75], v[36:39], v[136:139], v[72:75]\n"
    "ds_read_b64_tr_b16 v[56:57], v22 offset:59840\n"
    "ds_read_b64_tr_b16 v[58:59], v22 offset:60096\n"
    "v_mfma_f32_16x16x32_bf16 v[76:79], v[36:39], v[140:143], v[76:79]\n"
    "s_waitcnt lgkmcnt(6)\n"
    "v_mfma_f32_16x16x32_bf16 v[80:83], v[40:43], v[136:139], v[80:83]\n"
    "ds_read_b64_tr_b16 v[60:61], v22 offset:59872\n"
    "ds_read_b64_tr_b16 v[62:63], v22 offset:60128\n"
    "v_mfma_f32_16x16x32_bf16 v[84:87], v[40:43], v[140:143], v[84:87]\n"
    "s_barrier\n"
    "v_mfma_f32_16x16x32_bf16 v[88:91], v[44:47], v[136:139], v[88:91]\n"
    "ds_read_b128 v[144:147], v16 offset:64000\n"
    "ds_read_b64_tr_b16 v[32:33], v23 offset:14848\n"
    "ds_read_b64_tr_b16 v[34:35], v23 offset:15104\n"
    "v_mfma_f32_16x16x32_bf16 v[92:95], v[44:47], v[140:143], v[92:95]\n"
    "s_waitcnt lgkmcnt(7)\n"
    "v_mfma_f32_16x16x32_bf16 v[96:99], v[48:51], v[136:139], v[96:99]\n"
    "ds_read_b128 v[148:151], v17 offset:512\n"
    "ds_read_b64_tr_b16 v[36:37], v23 offset:14880\n"
    "ds_read_b64_tr_b16 v[38:39], v23 offset:15136\n"
    "v_mfma_f32_16x16x32_bf16 v[100:103], v[48:51], v[140:143], v[100:103]\n"
    "v_mfma_f32_16x16x32_bf16 v[104:107], v[52:55], v[136:139], v[104:107]\n"
    "ds_read_b64_tr_b16 v[40:41], v23 offset:14912\n"
    "ds_read_b64_tr_b16 v[42:43], v23 offset:15168\n"
    "v_mfma_f32_16x16x32_bf16 v[108:111], v[52:55], v[140:143], v[108:111]\n"
    "s_waitcnt lgkmcnt(8)\n"
    "v_mfma_f32_16x16x32_bf16 v[112:115], v[56:59], v[136:139], v[112:115]\n"
    "ds_read_b64_tr_b16 v[44:45], v23 offset:14944\n"
    "ds_read_b64_tr_b16 v[46:47], v23 offset:15200\n"
    "v_mfma_f32_16x16x32_bf16 v[116:119], v[56:59], v[140:143], v[116:119]\n"
    "v_mfma_f32_16x16x32_bf16 v[120:123], v[60:63], v[136:139], v[120:123]\n"
    "ds_read_b64_tr_b16 v[48:49], v23 offset:19072\n"
    "ds_read_b64_tr_b16 v[50:51], v23 offset:19328\n"
    "v_mfma_f32_16x16x32_bf16 v[124:127], v[60:63], v[140:143], v[124:127]\n"
    "s_waitcnt lgkmcnt(6)\n"
    "v_mfma_f32_16x16x32_bf16 v[64:67], v[32:35], v[144:147], v[64:67]\n"
    "ds_read_b64_tr_b16 v[52:53], v23 offset:19104\n"
    "ds_read_b64_tr_b16 v[54:55], v23 offset:19360\n"
    "v_mfma_f32_16x16x32_bf16 v[68:71], v[32:35], v[148:151], v[68:71]\n"
    "v_mfma_f32_16x16x32_bf16 v[72:75], v[36:39], v[144:147], v[72:75]\n"
    "ds_read_b64_tr_b16 v[56:57], v23 offset:19136\n"
    "ds_read_b64_tr_b16 v[58:59], v23 offset:19392\n"
    "v_mfma_f32_16x16x32_bf16 v[76:79], v[36:39], v[148:151], v[76:79]\n"
    "s_waitcnt lgkmcnt(6)\n"
    "v_mfma_f32_16x16x32_bf16 v[80:83], v[40:43], v[144:147], v[80:83]\n"
    "ds_read_b64_tr_b16 v[60:61], v23 offset:19168\n"
    "ds_read_b64_tr_b16 v[62:63], v23 offset:19424\n"
    "v_mfma_f32_16x16x32_bf16 v[84:87], v[40:43], v[148:151], v[84:87]\n"
    "v_mfma_f32_16x16x32_bf16 v[88:91], v[44:47], v[144:147], v[88:91]\n"
    "ds_read_b128 v[152:155], v19 offset:64000\n"
    "ds_read_b64_tr_b16 v[32:33], v23 offset:23296\n"
    "ds_read_b64_tr_b16 v[34:35], v23 offset:23552\n"
    "v_mfma_f32_16x16x32_bf16 v[92:95], v[44:47], v[148:151], v[92:95]\n"
    "s_waitcnt lgkmcnt(7)\n"
    "v_mfma_f32_16x16x32_bf16 v[96:99], v[48:51], v[144:147], v[96:99]\n"
    "ds_read_b128 v[156:159], v20 offset:512\n"
    "ds_read_b64_tr_b16 v[36:37], v23 offset:23328\n"
    "ds_read_b64_tr_b16 v[38:39], v23 offset:23584\n"
    "v_mfma_f32_16x16x32_bf16 v[100:103], v[48:51], v[148:151], v[100:103]\n"
    "v_mfma_f32_16x16x32_bf16 v[104:107], v[52:55], v[144:147], v[104:107]\n"
    "ds_read_b64_tr_b16 v[40:41], v23 offset:23360\n"
    "ds_read_b64_tr_b16 v[42:43], v23 offset:23616\n"
    "v_mfma_f32_16x16x32_bf16 v[108:111], v[52:55], v[148:151], v[108:111]\n"
    "s_waitcnt lgkmcnt(8)\n"
    "v_mfma_f32_16x16x32_bf16 v[112:115], v[56:59], v[144:147], v[112:115]\n"
    "ds_read_b64_tr_b16 v[44:45], v23 offset:23392\n"
    "ds_read_b64_tr_b16 v[46:47], v23 offset:23648\n"
    "v_mfma_f32_16x16x32_bf16 v[116:119], v[56:59], v[148:151], v[116:119]\n"
    "v_mfma_f32_16x16x32_bf16 v[120:123], v[60:63], v[144:147], v[120:123]\n"
    "ds_read_b64_tr_b16 v[48:49], v23 offset:27520\n"
    "ds_read_b64_tr_b16 v[50:51], v23 offset:27776\n"
    "v_mfma_f32_16x16x32_bf16 v[124:127], v[60:63], v[148:151], v[124:127]\n"
    "s_waitcnt lgkmcnt(6)\n"
    "v_mfma_f32_16x16x32_bf16 v[64:67], v[32:35], v[152:155], v[64:67]\n"
    "ds_read_b64_tr_b16 v[52:53], v23 offset:27552\n"
    "ds_read_b64_tr_b16 v[54:55], v23 offset:27808\n"
    "v_mfma_f32_16x16x32_bf16 v[68:71], v[32:35], v[156:159], v[68:71]\n"
    "v_mfma_f32_16x16x32_bf16 v[72:75], v[36:39], v[152:155], v[72:75]\n"
    "ds_read_b64_tr_b16 v[56:57], v23 offset:27584\n"
    "ds_read_b64_tr_b16 v[58:59], v23 offset:27840\n"
    "v_mfma_f32_16x16x32_bf16 v[76:79], v[36:39], v[156:159], v[76:79]\n"
    "s_waitcnt lgkmcnt(6)\n"
    "v_mfma_f32_16x16x32_bf16 v[80:83], v[40:43], v[152:155], v[80:83]\n"
    "ds_read_b64_tr_b16 v[60:61], v23 offset:27616\n"
    "ds_read_b64_tr_b16 v[62:63], v23 offset:27872\n"
    "v_mfma_f32_16x16x32_bf16 v[84:87], v[40:43], v[156:159], v[84:87]\n"
    "s_barrier\n"
    "v_mfma_f32_16x16x32_bf16 v[88:91], v[44:47], v[152:155], v[88:91]\n"
    "ds_read_b128 v[160:163], v17 offset:31744\n"
    "ds_read_b64_tr_b16 v[32:33], v23 offset:48128\n"
    "ds_read_b64_tr_b16 v[34:35], v23 offset:48384\n"
    "v_mfma_f32_16x16x32_bf16 v[92:95], v[44:47], v[156:159], v[92:95]\n"
    "s_waitcnt lgkmcnt(7)\n"
    "v_mfma_f32_16x16x32_bf16 v[96:99], v[48:51], v[152:155], v[96:99]\n"
    "ds_read_b128 v[164:167], v17 offset:33792\n"
    "ds_read_b64_tr_b16 v[36:37], v23 offset:48160\n"
    "ds_read_b64_tr_b16 v[38:39], v23 offset:48416\n"
    "v_mfma_f32_16x16x32_bf16 v[100:103], v[48:51], v[156:159], v[100:103]\n"
    "v_mfma_f32_16x16x32_bf16 v[104:107], v[52:55], v[152:155], v[104:107]\n"
    "ds_read_b64_tr_b16 v[40:41], v23 offset:48192\n"
    "ds_read_b64_tr_b16 v[42:43], v23 offset:48448\n"
    "v_mfma_f32_16x16x32_bf16 v[108:111], v[52:55], v[156:159], v[108:111]\n"
    "s_waitcnt lgkmcnt(8)\n"
    "v_mfma_f32_16x16x32_bf16 v[112:115], v[56:59], v[152:155], v[112:115]\n"
    "ds_read_b64_tr_b16 v[44:45], v23 offset:48224\n"
    "ds_read_b64_tr_b16 v[46:47], v23 offset:48480\n"
    "v_mfma_f32_16x16x32_bf16 v[116:119], v[56:59], v[156:159], v[116:119]\n"
    "v_mfma_f32_16x16x32_bf16 v[120:123], v[60:63], v[152:155], v[120:123]\n"
    "ds_read_b64_tr_b16 v[48:49], v23 offset:52352\n"
    "ds_read_b64_tr_b16 v[50:51], v23 offset:52608\n"
    "v_mfma_f32_16x16x32_bf16 v[124:127], v[60:63], v[156:159], v[124:127]\n"
    "s_waitcnt lgkmcnt(6)\n"
    "v_mfma_f32_16x16x32_bf16 v[64:67], v[32:35], v[160:163], v[64:67]\n"
    "ds_read_b64_tr_b16 v[52:53], v23 offset:52384\n"
    "ds_read_b64_tr_b16 v[54:55], v23 offset:52640\n"
    "v_mfma_f32_16x16x32_bf16 v[68:71], v[32:35], v[164:167], v[68:71]\n"
    "v_mfma_f32_16x16x32_bf16 v[72:75], v[36:39], v[160:163], v[72:75]\n"
    "ds_read_b64_tr_b16 v[56:57], v23 offset:52416\n"
    "ds_read_b64_tr_b16 v[58:59], v23 offset:52672\n"
    "v_mfma_f32_16x16x32_bf16 v[76:79], v[36:39], v[164:167], v[76:79]\n"
    "s_waitcnt lgkmcnt(6)\n"
    "v_mfma_f32_16x16x32_bf16 v[80:83], v[40:43], v[160:163], v[80:83]\n"
    "ds_read_b64_tr_b16 v[60:61], v23 offset:52448\n"
    "ds_read_b64_tr_b16 v[62:63], v23 offset:52704\n"
    "v_mfma_f32_16x16x32_bf16 v[84:87], v[40:43], v[164:167], v[84:87]\n"
    "v_mfma_f32_16x16x32_bf16 v[88:91], v[44:47], v[160:163], v[88:91]\n"
    "ds_read_b128 v[168:171], v20 offset:31744\n"
    "ds_read_b64_tr_b16 v[32:33], v23 offset:56576\n"
    "ds_read_b64_tr_b16 v[34:35], v23 offset:56832\n"
    "v_mfma_f32_16x16x32_bf16 v[92:95], v[44:47], v[164:167], v[92:95]\n"
    "s_waitcnt lgkmcnt(7)\n"
    "v_mfma_f32_16x16x32_bf16 v[96:99], v[48:51], v[160:163], v[96:99]\n"
    "ds_read_b128 v[172:175], v20 offset:33792\n"
    "ds_read_b64_tr_b16 v[36:37], v23 offset:56608\n"
    "ds_read_b64_tr_b16 v[38:39], v23 offset:56864\n"
    "v_mfma_f32_16x16x32_bf16 v[100:103], v[48:51], v[164:167], v[100:103]\n"
    "v_mfma_f32_16x16x32_bf16 v[104:107], v[52:55], v[160:163], v[104:107]\n"
    "ds_read_b64_tr_b16 v[40:41], v23 offset:56640\n"
    "ds_read_b64_tr_b16 v[42:43], v23 offset:56896\n"
    "v_mfma_f32_16x16x32_bf16 v[108:111], v[52:55], v[164:167], v[108:111]\n"
    "s_waitcnt lgkmcnt(8)\n"
    "v_mfma_f32_16x16x32_bf16 v[112:115], v[56:59], v[160:163], v[112:115]\n"
    "ds_read_b64_tr_b16 v[44:45], v23 offset:56672\n"
    "ds_read_b64_tr_b16 v[46:47], v23 offset:56928\n"
    "v_mfma_f32_16x16x32_bf16 v[116:119], v[56:59], v[164:167], v[116:119]\n"
    "v_mfma_f32_16x16x32_bf16 v[120:123], v[60:63], v[160:163], v[120:123]\n"
    "ds_read_b64_tr_b16 v[48:49], v23 offset:60800\n"
    "ds_read_b64_tr_b16 v[50:51], v23 offset:61056\n"
    "v_mfma_f32_16x16x32_bf16 v[124:127], v[60:63], v[164:167], v[124:127]\n"
    "s_waitcnt lgkmcnt(6)\n"
    "v_mfma_f32_16x16x32_bf16 v[64:67], v[32:35], v[168:171], v[64:67]\n"
    "ds_read_b64_tr_b16 v[52:53], v23 offset:60832\n"
    "ds_read_b64_tr_b16 v[54:55], v23 offset:61088\n"
    "v_mfma_f32_16x16x32_bf16 v[68:71], v[32:35], v[172:175], v[68:71]\n"
    "v_mfma_f32_16x16x32_bf16 v[72:75], v[36:39], v[168:171], v[72:75]\n"
    "ds_read_b64_tr_b16 v[56:57], v23 offset:60864\n"
    "ds_read_b64_tr_b16 v[58:59], v23 offset:61120\n"
    "v_mfma_f32_16x16x32_bf16 v[76:79], v[36:39], v[172:175], v[76:79]\n"
    "s_waitcnt lgkmcnt(6)\n"
    "v_mfma_f32_16x16x32_bf16 v[80:83], v[40:43], v[168:171], v[80:83]\n"
    "ds_read_b64_tr_b16 v[60:61], v23 offset:60896\n"
    "ds_read_b64_tr_b16 v[62:63], v23 offset:61152\n"
    "v_mfma_f32_16x16x32_bf16 v[84:87], v[40:43], v[172:175], v[84:87]\n"
    "s_barrier\n"
    "v_mfma_f32_16x16x32_bf16 v[88:91], v[44:47], v[168:171], v[88:91]\n"
    "ds_read_b128 v[176:179], v17 offset:65024\n"
    "ds_read_b64_tr_b16 v[32:33], v24 offset:15872\n"
    "ds_read_b64_tr_b16 v[34:35], v24 offset:16128\n"
    "v_mfma_f32_16x16x32_bf16 v[92:95], v[44:47], v[172:175], v[92:95]\n"
    "s_waitcnt lgkmcnt(7)\n"
    "v_mfma_f32_16x16x32_bf16 v[96:99], v[48:51], v[168:171], v[96:99]\n"
    "ds_read_b128 v[180:183], v18 offset:1536\n"
    "ds_read_b64_tr_b16 v[36:37], v24 offset:15904\n"
    "ds_read_b64_tr_b16 v[38:39], v24 offset:16160\n"
    "v_mfma_f32_16x16x32_bf16 v[100:103], v[48:51], v[172:175], v[100:103]\n"
    "v_mfma_f32_16x16x32_bf16 v[104:107], v[52:55], v[168:171], v[104:107]\n"
    "ds_read_b64_tr_b16 v[40:41], v24 offset:15936\n"
    "ds_read_b64_tr_b16 v[42:43], v24 offset:16192\n"
    "v_mfma_f32_16x16x32_bf16 v[108:111], v[52:55], v[172:175], v[108:111]\n"
    "s_waitcnt lgkmcnt(8)\n"
    "v_mfma_f32_16x16x32_bf16 v[112:115], v[56:59], v[168:171], v[112:115]\n"
    "ds_read_b64_tr_b16 v[44:45], v24 offset:15968\n"
    "ds_read_b64_tr_b16 v[46:47], v24 offset:16224\n"
    "v_mfma_f32_16x16x32_bf16 v[116:119], v[56:59], v[172:175], v[116:119]\n"
    "v_mfma_f32_16x16x32_bf16 v[120:123], v[60:63], v[168:171], v[120:123]\n"
    "ds_read_b64_tr_b16 v[48:49], v24 offset:20096\n"
    "ds_read_b64_tr_b16 v[50:51], v24 offset:20352\n"
    "v_mfma_f32_16x16x32_bf16 v[124:127], v[60:63], v[172:175], v[124:127]\n"
    "s_waitcnt lgkmcnt(6)\n"
    "v_mfma_f32_16x16x32_bf16 v[64:67], v[32:35], v[176:179], v[64:67]\n"
    "ds_read_b64_tr_b16 v[52:53], v24 offset:20128\n"
    "ds_read_b64_tr_b16 v[54:55], v24 offset:20384\n"
    "v_mfma_f32_16x16x32_bf16 v[68:71], v[32:35], v[180:183], v[68:71]\n"
    "v_mfma_f32_16x16x32_bf16 v[72:75], v[36:39], v[176:179], v[72:75]\n"
    "ds_read_b64_tr_b16 v[56:57], v24 offset:20160\n"
    "ds_read_b64_tr_b16 v[58:59], v24 offset:20416\n"
    "v_mfma_f32_16x16x32_bf16 v[76:79], v[36:39], v[180:183], v[76:79]\n"
    "s_waitcnt lgkmcnt(6)\n"
    "v_mfma_f32_16x16x32_bf16 v[80:83], v[40:43], v[176:179], v[80:83]\n"
    "ds_read_b64_tr_b16 v[60:61], v24 offset:20192\n"
    "ds_read_b64_tr_b16 v[62:63], v24 offset:20448\n"
    "v_mfma_f32_16x16x32_bf16 v[84:87], v[40:43], v[180:183], v[84:87]\n"
    "v_mfma_f32_16x16x32_bf16 v[88:91], v[44:47], v[176:179], v[88:91]\n"
    "ds_read_b128 v[184:187], v20 offset:65024\n"
    "ds_read_b64_tr_b16 v[32:33], v24 offset:24320\n"
    "ds_read_b64_tr_b16 v[34:35], v24 offset:24576\n"
    "v_mfma_f32_16x16x32_bf16 v[92:95], v[44:47], v[180:183], v[92:95]\n"
    "s_waitcnt lgkmcnt(7)\n"
    "v_mfma_f32_16x16x32_bf16 v[96:99], v[48:51], v[176:179], v[96:99]\n"
    "ds_read_b128 v[188:191], v21 offset:1536\n"
    "ds_read_b64_tr_b16 v[36:37], v24 offset:24352\n"
    "ds_read_b64_tr_b16 v[38:39], v24 offset:24608\n"
    "v_mfma_f32_16x16x32_bf16 v[100:103], v[48:51], v[180:183], v[100:103]\n"
    "v_mfma_f32_16x16x32_bf16 v[104:107], v[52:55], v[176:179], v[104:107]\n"
    "ds_read_b64_tr_b16 v[40:41], v24 offset:24384\n"
    "ds_read_b64_tr_b16 v[42:43], v24 offset:24640\n"
    "v_mfma_f32_16x16x32_bf16 v[108:111], v[52:55], v[180:183], v[108:111]\n"
    "s_waitcnt lgkmcnt(8)\n"
    "v_mfma_f32_16x16x32_bf16 v[112:115], v[56:59], v[176:179], v[112:115]\n"
    "ds_read_b64_tr_b16 v[44:45], v24 offset:24416\n"
    "ds_read_b64_tr_b16 v[46:47], v24 offset:24672\n"
    "v_mfma_f32_16x16x32_bf16 v[116:119], v[56:59], v[180:183], v[116:119]\n"
    "v_mfma_f32_16x16x32_bf16 v[120:123], v[60:63], v[176:179], v[120:123]\n"
    "ds_read_b64_tr_b16 v[48:49], v24 offset:28544\n"
    "ds_read_b64_tr_b16 v[50:51], v24 offset:28800\n"
    "v_mfma_f32_16x16x32_bf16 v[124:127], v[60:63], v[180:183], v[124:127]\n"
    "s_waitcnt lgkmcnt(6)\n"
    "v_mfma_f32_16x16x32_bf16 v[64:67], v[32:35], v[184:187], v[64:67]\n"
    "ds_read_b64_tr_b16 v[52:53], v24 offset:28576\n"
    "ds_read_b64_tr_b16 v[54:55], v24 offset:28832\n"
    "v_mfma_f32_16x16x32_bf16 v[68:71], v[32:35], v[188:191], v[68:71]\n"
    "v_mfma_f32_16x16x32_bf16 v[72:75], v[36:39], v[184:187], v[72:75]\n"
    "ds_read_b64_tr_b16 v[56:57], v24 offset:28608\n"
    "ds_read_b64_tr_b16 v[58:59], v24 offset:28864\n"
    "v_mfma_f32_16x16x32_bf16 v[76:79], v[36:39], v[188:191], v[76:79]\n"
    "s_waitcnt lgkmcnt(6)\n"
    "v_mfma_f32_16x16x32_bf16 v[80:83], v[40:43], v[184:187], v[80:83]\n"
    "ds_read_b64_tr_b16 v[60:61], v24 offset:28640\n"
    "ds_read_b64_tr_b16 v[62:63], v24 offset:28896\n"
    "v_mfma_f32_16x16x32_bf16 v[84:87], v[40:43], v[188:191], v[84:87]\n"
    "s_barrier\n"
    "v_mfma_f32_16x16x32_bf16 v[88:91], v[44:47], v[184:187], v[88:91]\n"
    "ds_read_b128 v[192:195], v16 offset:30720\n"
    "ds_read_b64_tr_b16 v[32:33], v22 offset:47104\n"
    "ds_read_b64_tr_b16 v[34:35], v22 offset:47360\n"
    "v_mfma_f32_16x16x32_bf16 v[92:95], v[44:47], v[188:191], v[92:95]\n"
    "s_waitcnt lgkmcnt(7)\n"
    "v_mfma_f32_16x16x32_bf16 v[96:99], v[48:51], v[184:187], v[96:99]\n"
    "ds_read_b128 v[196:199], v16 offset:32768\n"
    "ds_read_b64_tr_b16 v[36:37], v22 offset:47136\n"
    "ds_read_b64_tr_b16 v[38:39], v22 offset:47392\n"
    "v_mfma_f32_16x16x32_bf16 v[100:103], v[48:51], v[188:191], v[100:103]\n"
    "v_mfma_f32_16x16x32_bf16 v[104:107], v[52:55], v[184:187], v[104:107]\n"
    "ds_read_b64_tr_b16 v[40:41], v22 offset:47168\n"
    "ds_read_b64_tr_b16 v[42:43], v22 offset:47424\n"
    "v_mfma_f32_16x16x32_bf16 v[108:111], v[52:55], v[188:191], v[108:111]\n"
    "s_waitcnt lgkmcnt(8)\n"
    "v_mfma_f32_16x16x32_bf16 v[112:115], v[56:59], v[184:187], v[112:115]\n"
    "ds_read_b64_tr_b16 v[44:45], v22 offset:47200\n"
    "ds_read_b64_tr_b16 v[46:47], v22 offset:47456\n"
    "v_mfma_f32_16x16x32_bf16 v[116:119], v[56:59], v[188:191], v[116:119]\n"
    "v_mfma_f32_16x16x32_bf16 v[120:123], v[60:63], v[184:187], v[120:123]\n"
    "ds_read_b64_tr_b16 v[48:49], v22 offset:51328\n"
    "ds_read_b64_tr_b16 v[50:51], v22 offset:51584\n"
    "v_mfma_f32_16x16x32_bf16 v[124:127], v[60:63], v[188:191], v[124:127]\n"
    "s_waitcnt lgkmcnt(6)\n"
    "v_mfma_f32_16x16x32_bf16 v[64:67], v[32:35], v[192:195], v[64:67]\n"
    "ds_read_b64_tr_b16 v[52:53], v22 offset:51360\n"
    "ds_read_b64_tr_b16 v[54:55], v22 offset:51616\n"
    "v_mfma_f32_16x16x32_bf16 v[68:71], v[32:35], v[196:199], v[68:71]\n"
    "v_mfma_f32_16x16x32_bf16 v[72:75], v[36:39], v[192:195], v[72:75]\n"
    "ds_read_b64_tr_b16 v[56:57], v22 offset:51392\n"
    "ds_read_b64_tr_b16 v[58:59], v22 offset:51648\n"
    "v_mfma_f32_16x16x32_bf16 v[76:79], v[36:39], v[196:199], v[76:79]\n"
    "s_waitcnt lgkmcnt(6)\n"
    "v_mfma_f32_16x16x32_bf16 v[80:83], v[40:43], v[192:195], v[80:83]\n"
    "ds_read_b64_tr_b16 v[60:61], v22 offset:51424\n"
    "ds_read_b64_tr_b16 v[62:63], v22 offset:51680\n"
    "v_mfma_f32_16x16x32_bf16 v[84:87], v[40:43], v[196:199], v[84:87]\n"
    "v_mfma_f32_16x16x32_bf16 v[88:91], v[44:47], v[192:195], v[88:91]\n"
    "ds_read_b128 v[200:203], v19 offset:30720\n"
    "ds_read_b64_tr_b16 v[32:33], v22 offset:55552\n"
    "ds_read_b64_tr_b16 v[34:35], v22 offset:55808\n"
    "v_mfma_f32_16x16x32_bf16 v[92:95], v[44:47], v[196:199], v[92:95]\n"
    "s_waitcnt lgkmcnt(7)\n"
    "v_mfma_f32_16x16x32_bf16 v[96:99], v[48:51], v[192:195], v[96:99]\n"
    "ds_read_b128 v[204:207], v19 offset:32768\n"
    "ds_read_b64_tr_b16 v[36:37], v22 offset:55584\n"
    "ds_read_b64_tr_b16 v[38:39], v22 offset:55840\n"
    "v_mfma_f32_16x16x32_bf16 v[100:103], v[48:51], v[196:199], v[100:103]\n"
    "v_mfma_f32_16x16x32_bf16 v[104:107], v[52:55], v[192:195], v[104:107]\n"
    "ds_read_b64_tr_b16 v[40:41], v22 offset:55616\n"
    "ds_read_b64_tr_b16 v[42:43], v22 offset:55872\n"
    "v_mfma_f32_16x16x32_bf16 v[108:111], v[52:55], v[196:199], v[108:111]\n"
    "s_waitcnt lgkmcnt(8)\n"
    "v_mfma_f32_16x16x32_bf16 v[112:115], v[56:59], v[192:195], v[112:115]\n"
    "ds_read_b64_tr_b16 v[44:45], v22 offset:55648\n"
    "ds_read_b64_tr_b16 v[46:47], v22 offset:55904\n"
    "v_mfma_f32_16x16x32_bf16 v[116:119], v[56:59], v[196:199], v[116:119]\n"
    "v_mfma_f32_16x16x32_bf16 v[120:123], v[60:63], v[192:195], v[120:123]\n"
    "ds_read_b64_tr_b16 v[48:49], v22 offset:59776\n"
    "ds_read_b64_tr_b16 v[50:51], v22 offset:60032\n"
    "v_mfma_f32_16x16x32_bf16 v[124:127], v[60:63], v[196:199], v[124:127]\n"
    "s_waitcnt lgkmcnt(6)\n"
    "v_mfma_f32_16x16x32_bf16 v[64:67], v[32:35], v[200:203], v[64:67]\n"
    "ds_read_b64_tr_b16 v[52:53], v22 offset:59808\n"
    "ds_read_b64_tr_b16 v[54:55], v22 offset:60064\n"
    "v_mfma_f32_16x16x32_bf16 v[68:71], v[32:35], v[204:207], v[68:71]\n"
    "v_mfma_f32_16x16x32_bf16 v[72:75], v[36:39], v[200:203], v[72:75]\n"
    "ds_read_b64_tr_b16 v[56:57], v22 offset:59840\n"
    "ds_read_b64_tr_b16 v[58:59], v22 offset:60096\n"
    "v_mfma_f32_16x16x32_bf16 v[76:79], v[36:39], v[204:207], v[76:79]\n"
    "s_waitcnt lgkmcnt(6)\n"
    "v_mfma_f32_16x16x32_bf16 v[80:83], v[40:43], v[200:203], v[80:83]\n"
    "ds_read_b64_tr_b16 v[60:61], v22 offset:59872\n"
    "ds_read_b64_tr_b16 v[62:63], v22 offset:60128\n"
    "v_mfma_f32_16x16x32_bf16 v[84:87], v[40:43], v[204:207], v[84:87]\n"
    "s_barrier\n"
    "v_mfma_f32_16x16x32_bf16 v[88:91], v[44:47], v[200:203], v[88:91]\n"
    "ds_read_b128 v[208:211], v16 offset:64000\n"
    "ds_read_b64_tr_b16 v[32:33], v23 offset:14848\n"
    "ds_read_b64_tr_b16 v[34:35], v23 offset:15104\n"
    "v_mfma_f32_16x16x32_bf16 v[92:95], v[44:47], v[204:207], v[92:95]\n"
    "s_waitcnt lgkmcnt(7)\n"
    "v_mfma_f32_16x16x32_bf16 v[96:99], v[48:51], v[200:203], v[96:99]\n"
    "ds_read_b128 v[212:215], v17 offset:512\n"
    "ds_read_b64_tr_b16 v[36:37], v23 offset:14880\n"
    "ds_read_b64_tr_b16 v[38:39], v23 offset:15136\n"
    "v_mfma_f32_16x16x32_bf16 v[100:103], v[48:51], v[204:207], v[100:103]\n"
    "v_mfma_f32_16x16x32_bf16 v[104:107], v[52:55], v[200:203], v[104:107]\n"
    "ds_read_b64_tr_b16 v[40:41], v23 offset:14912\n"
    "ds_read_b64_tr_b16 v[42:43], v23 offset:15168\n"
    "v_mfma_f32_16x16x32_bf16 v[108:111], v[52:55], v[204:207], v[108:111]\n"
    "s_waitcnt lgkmcnt(8)\n"
    "v_mfma_f32_16x16x32_bf16 v[112:115], v[56:59], v[200:203], v[112:115]\n"
    "ds_read_b64_tr_b16 v[44:45], v23 offset:14944\n"
    "ds_read_b64_tr_b16 v[46:47], v23 offset:15200\n"
    "v_mfma_f32_16x16x32_bf16 v[116:119], v[56:59], v[204:207], v[116:119]\n"
    "v_mfma_f32_16x16x32_bf16 v[120:123], v[60:63], v[200:203], v[120:123]\n"
    "ds_read_b64_tr_b16 v[48:49], v23 offset:19072\n"
    "ds_read_b64_tr_b16 v[50:51], v23 offset:19328\n"
    "v_mfma_f32_16x16x32_bf16 v[124:127], v[60:63], v[204:207], v[124:127]\n"
    "s_waitcnt lgkmcnt(6)\n"
    "v_mfma_f32_16x16x32_bf16 v[64:67], v[32:35], v[208:211], v[64:67]\n"
    "ds_read_b64_tr_b16 v[52:53], v23 offset:19104\n"
    "ds_read_b64_tr_b16 v[54:55], v23 offset:19360\n"
    "v_mfma_f32_16x16x32_bf16 v[68:71], v[32:35], v[212:215], v[68:71]\n"
    "v_mfma_f32_16x16x32_bf16 v[72:75], v[36:39], v[208:211], v[72:75]\n"
    "ds_read_b64_tr_b16 v[56:57], v23 offset:19136\n"
    "ds_read_b64_tr_b16 v[58:59], v23 offset:19392\n"
    "v_mfma_f32_16x16x32_bf16 v[76:79], v[36:39], v[212:215], v[76:79]\n"
    "s_waitcnt lgkmcnt(6)\n"
    "v_mfma_f32_16x16x32_bf16 v[80:83], v[40:43], v[208:211], v[80:83]\n"
    "ds_read_b64_tr_b16 v[60:61], v23 offset:19168\n"
    "ds_read_b64_tr_b16 v[62:63], v23 offset:19424\n"
    "v_mfma_f32_16x16x32_bf16 v[84:87], v[40:43], v[212:215], v[84:87]\n"
    "v_mfma_f32_16x16x32_bf16 v[88:91], v[44:47], v[208:211], v[88:91]\n"
    "ds_read_b128 v[216:219], v19 offset:64000\n"
    "ds_read_b64_tr_b16 v[32:33], v23 offset:23296\n"
    "ds_read_b64_tr_b16 v[34:35], v23 offset:23552\n"
    "v_mfma_f32_16x16x32_bf16 v[92:95], v[44:47], v[212:215], v[92:95]\n"
    "s_waitcnt lgkmcnt(7)\n"
    "v_mfma_f32_16x16x32_bf16 v[96:99], v[48:51], v[208:211], v[96:99]\n"
    "ds_read_b128 v[220:223], v20 offset:512\n"
    "ds_read_b64_tr_b16 v[36:37], v23 offset:23328\n"
    "ds_read_b64_tr_b16 v[38:39], v23 offset:23584\n"
    "v_mfma_f32_16x16x32_bf16 v[100:103], v[48:51], v[212:215], v[100:103]\n"
    "v_mfma_f32_16x16x32_bf16 v[104:107], v[52:55], v[208:211], v[104:107]\n"
    "ds_read_b64_tr_b16 v[40:41], v23 offset:23360\n"
    "ds_read_b64_tr_b16 v[42:43], v23 offset:23616\n"
    "v_mfma_f32_16x16x32_bf16 v[108:111], v[52:55], v[212:215], v[108:111]\n"
    "s_waitcnt lgkmcnt(8)\n"
    "v_mfma_f32_16x16x32_bf16 v[112:115], v[56:59], v[208:211], v[112:115]\n"
    "ds_read_b64_tr_b16 v[44:45], v23 offset:23392\n"
    "ds_read_b64_tr_b16 v[46:47], v23 offset:23648\n"
    "v_mfma_f32_16x16x32_bf16 v[116:119], v[56:59], v[212:215], v[116:119]\n"
    "v_mfma_f32_16x16x32_bf16 v[120:123], v[60:63], v[208:211], v[120:123]\n"
    "ds_read_b64_tr_b16 v[48:49], v23 offset:27520\n"
    "ds_read_b64_tr_b16 v[50:51], v23 offset:27776\n"
    "v_mfma_f32_16x16x32_bf16 v[124:127], v[60:63], v[212:215], v[124:127]\n"
    "s_waitcnt lgkmcnt(6)\n"
    "v_mfma_f32_16x16x32_bf16 v[64:67], v[32:35], v[216:219], v[64:67]\n"
    "ds_read_b64_tr_b16 v[52:53], v23 offset:27552\n"
    "ds_read_b64_tr_b16 v[54:55], v23 offset:27808\n"
    "v_mfma_f32_16x16x32_bf16 v[68:71], v[32:35], v[220:223], v[68:71]\n"
    "v_mfma_f32_16x16x32_bf16 v[72:75], v[36:39], v[216:219], v[72:75]\n"
    "ds_read_b64_tr_b16 v[56:57], v23 offset:27584\n"
    "ds_read_b64_tr_b16 v[58:59], v23 offset:27840\n"
    "v_mfma_f32_16x16x32_bf16 v[76:79], v[36:39], v[220:223], v[76:79]\n"
    "s_waitcnt lgkmcnt(6)\n"
    "v_mfma_f32_16x16x32_bf16 v[80:83], v[40:43], v[216:219], v[80:83]\n"
    "ds_read_b64_tr_b16 v[60:61], v23 offset:27616\n"
    "ds_read_b64_tr_b16 v[62:63], v23 offset:27872\n"
    "v_mfma_f32_16x16x32_bf16 v[84:87], v[40:43], v[220:223], v[84:87]\n"
    "s_barrier\n"
    "v_mfma_f32_16x16x32_bf16 v[88:91], v[44:47], v[216:219], v[88:91]\n"
    "ds_read_b128 v[224:227], v17 offset:31744\n"
    "ds_read_b64_tr_b16 v[32:33], v23 offset:48128\n"
    "ds_read_b64_tr_b16 v[34:35], v23 offset:48384\n"
    "v_mfma_f32_16x16x32_bf16 v[92:95], v[44:47], v[220:223], v[92:95]\n"
    "s_waitcnt lgkmcnt(7)\n"
    "v_mfma_f32_16x16x32_bf16 v[96:99], v[48:51], v[216:219], v[96:99]\n"
    "ds_read_b128 v[228:231], v17 offset:33792\n"
    "ds_read_b64_tr_b16 v[36:37], v23 offset:48160\n"
    "ds_read_b64_tr_b16 v[38:39], v23 offset:48416\n"
    "v_mfma_f32_16x16x32_bf16 v[100:103], v[48:51], v[220:223], v[100:103]\n"
    "v_mfma_f32_16x16x32_bf16 v[104:107], v[52:55], v[216:219], v[104:107]\n"
    "ds_read_b64_tr_b16 v[40:41], v23 offset:48192\n"
    "ds_read_b64_tr_b16 v[42:43], v23 offset:48448\n"
    "v_mfma_f32_16x16x32_bf16 v[108:111], v[52:55], v[220:223], v[108:111]\n"
    "s_waitcnt lgkmcnt(8)\n"
    "v_mfma_f32_16x16x32_bf16 v[112:115], v[56:59], v[216:219], v[112:115]\n"
    "ds_read_b64_tr_b16 v[44:45], v23 offset:48224\n"
    "ds_read_b64_tr_b16 v[46:47], v23 offset:48480\n"
    "v_mfma_f32_16x16x32_bf16 v[116:119], v[56:59], v[220:223], v[116:119]\n"
    "v_mfma_f32_16x16x32_bf16 v[120:123], v[60:63], v[216:219], v[120:123]\n"
    "ds_read_b64_tr_b16 v[48:49], v23 offset:52352\n"
    "ds_read_b64_tr_b16 v[50:51], v23 offset:52608\n"
    "v_mfma_f32_16x16x32_bf16 v[124:127], v[60:63], v[220:223], v[124:127]\n"
    "s_waitcnt lgkmcnt(6)\n"
    "v_mfma_f32_16x16x32_bf16 v[64:67], v[32:35], v[224:227], v[64:67]\n"
    "ds_read_b64_tr_b16 v[52:53], v23 offset:52384\n"
    "ds_read_b64_tr_b16 v[54:55], v23 offset:52640\n"
    "v_mfma_f32_16x16x32_bf16 v[68:71], v[32:35], v[228:231], v[68:71]\n"
    "v_mfma_f32_16x16x32_bf16 v[72:75], v[36:39], v[224:227], v[72:75]\n"
    "ds_read_b64_tr_b16 v[56:57], v23 offset:52416\n"
    "ds_read_b64_tr_b16 v[58:59], v23 offset:52672\n"
    "v_mfma_f32_16x16x32_bf16 v[76:79], v[36:39], v[228:231], v[76:79]\n"
    "s_waitcnt lgkmcnt(6)\n"
    "v_mfma_f32_16x16x32_bf16 v[80:83], v[40:43], v[224:227], v[80:83]\n"
    "ds_read_b64_tr_b16 v[60:61], v23 offset:52448\n"
    "ds_read_b64_tr_b16 v[62:63], v23 offset:52704\n"
    "v_mfma_f32_16x16x32_bf16 v[84:87], v[40:43], v[228:231], v[84:87]\n"
    "v_mfma_f32_16x16x32_bf16 v[88:91], v[44:47], v[224:227], v[88:91]\n"
    "ds_read_b128 v[232:235], v20 offset:31744\n"
    "ds_read_b64_tr_b16 v[32:33], v23 offset:56576\n"
    "ds_read_b64_tr_b16 v[34:35], v23 offset:56832\n"
    "v_mfma_f32_16x16x32_bf16 v[92:95], v[44:47], v[228:231], v[92:95]\n"
    "s_waitcnt lgkmcnt(7)\n"
    "v_mfma_f32_16x16x32_bf16 v[96:99], v[48:51], v[224:227], v[96:99]\n"
    "ds_read_b128 v[236:239], v20 offset:33792\n"
    "ds_read_b64_tr_b16 v[36:37], v23 offset:56608\n"
    "ds_read_b64_tr_b16 v[38:39], v23 offset:56864\n"
    "v_mfma_f32_16x16x32_bf16 v[100:103], v[48:51], v[228:231], v[100:103]\n"
    "v_mfma_f32_16x16x32_bf16 v[104:107], v[52:55], v[224:227], v[104:107]\n"
    "ds_read_b64_tr_b16 v[40:41], v23 offset:56640\n"
    "ds_read_b64_tr_b16 v[42:43], v23 offset:56896\n"
    "v_mfma_f32_16x16x32_bf16 v[108:111], v[52:55], v[228:231], v[108:111]\n"
    "s_waitcnt lgkmcnt(8)\n"
    "v_mfma_f32_16x16x32_bf16 v[112:115], v[56:59], v[224:227], v[112:115]\n"
    "ds_read_b64_tr_b16 v[44:45], v23 offset:56672\n"
    "ds_read_b64_tr_b16 v[46:47], v23 offset:56928\n"
    "v_mfma_f32_16x16x32_bf16 v[116:119], v[56:59], v[228:231], v[116:119]\n"
    "v_mfma_f32_16x16x32_bf16 v[120:123], v[60:63], v[224:227], v[120:123]\n"
    "ds_read_b64_tr_b16 v[48:49], v23 offset:60800\n"
    "ds_read_b64_tr_b16 v[50:51], v23 offset:61056\n"
    "v_mfma_f32_16x16x32_bf16 v[124:127], v[60:63], v[228:231], v[124:127]\n"
    "s_waitcnt lgkmcnt(6)\n"
    "v_mfma_f32_16x16x32_bf16 v[64:67], v[32:35], v[232:235], v[64:67]\n"
    "ds_read_b64_tr_b16 v[52:53], v23 offset:60832\n"
    "ds_read_b64_tr_b16 v[54:55], v23 offset:61088\n"
    "v_mfma_f32_16x16x32_bf16 v[68:71], v[32:35], v[236:239], v[68:71]\n"
    "v_mfma_f32_16x16x32_bf16 v[72:75], v[36:39], v[232:235], v[72:75]\n"
    "ds_read_b64_tr_b16 v[56:57], v23 offset:60864\n"
    "ds_read_b64_tr_b16 v[58:59], v23 offset:61120\n"
    "v_mfma_f32_16x16x32_bf16 v[76:79], v[36:39], v[236:239], v[76:79]\n"
    "s_waitcnt lgkmcnt(6)\n"
    "v_mfma_f32_16x16x32_bf16 v[80:83], v[40:43], v[232:235], v[80:83]\n"
    "ds_read_b64_tr_b16 v[60:61], v23 offset:60896\n"
    "ds_read_b64_tr_b16 v[62:63], v23 offset:61152\n"
    "v_mfma_f32_16x16x32_bf16 v[84:87], v[40:43], v[236:239], v[84:87]\n"
    "s_barrier\n"
    "v_mfma_f32_16x16x32_bf16 v[88:91], v[44:47], v[232:235], v[88:91]\n"
    "ds_read_b128 v[240:243], v17 offset:65024\n"
    "ds_read_b64_tr_b16 v[32:33], v24 offset:15872\n"
    "ds_read_b64_tr_b16 v[34:35], v24 offset:16128\n"
    "v_mfma_f32_16x16x32_bf16 v[92:95], v[44:47], v[236:239], v[92:95]\n"
    "s_waitcnt lgkmcnt(7)\n"
    "v_mfma_f32_16x16x32_bf16 v[96:99], v[48:51], v[232:235], v[96:99]\n"
    "ds_read_b128 v[244:247], v18 offset:1536\n"
    "ds_read_b64_tr_b16 v[36:37], v24 offset:15904\n"
    "ds_read_b64_tr_b16 v[38:39], v24 offset:16160\n"
    "v_mfma_f32_16x16x32_bf16 v[100:103], v[48:51], v[236:239], v[100:103]\n"
    "v_mfma_f32_16x16x32_bf16 v[104:107], v[52:55], v[232:235], v[104:107]\n"
    "ds_read_b64_tr_b16 v[40:41], v24 offset:15936\n"
    "ds_read_b64_tr_b16 v[42:43], v24 offset:16192\n"
    "v_mfma_f32_16x16x32_bf16 v[108:111], v[52:55], v[236:239], v[108:111]\n"
    "s_waitcnt lgkmcnt(8)\n"
    "v_mfma_f32_16x16x32_bf16 v[112:115], v[56:59], v[232:235], v[112:115]\n"
    "ds_read_b64_tr_b16 v[44:45], v24 offset:15968\n"
    "ds_read_b64_tr_b16 v[46:47], v24 offset:16224\n"
    "v_mfma_f32_16x16x32_bf16 v[116:119], v[56:59], v[236:239], v[116:119]\n"
    "v_mfma_f32_16x16x32_bf16 v[120:123], v[60:63], v[232:235], v[120:123]\n"
    "ds_read_b64_tr_b16 v[48:49], v24 offset:20096\n"
    "ds_read_b64_tr_b16 v[50:51], v24 offset:20352\n"
    "v_mfma_f32_16x16x32_bf16 v[124:127], v[60:63], v[236:239], v[124:127]\n"
    "s_waitcnt lgkmcnt(6)\n"
    "v_mfma_f32_16x16x32_bf16 v[64:67], v[32:35], v[240:243], v[64:67]\n"
    "ds_read_b64_tr_b16 v[52:53], v24 offset:20128\n"
    "ds_read_b64_tr_b16 v[54:55], v24 offset:20384\n"
    "v_mfma_f32_16x16x32_bf16 v[68:71], v[32:35], v[244:247], v[68:71]\n"
    "v_mfma_f32_16x16x32_bf16 v[72:75], v[36:39], v[240:243], v[72:75]\n"
    "ds_read_b64_tr_b16 v[56:57], v24 offset:20160\n"
    "ds_read_b64_tr_b16 v[58:59], v24 offset:20416\n"
    "v_mfma_f32_16x16x32_bf16 v[76:79], v[36:39], v[244:247], v[76:79]\n"
    "s_waitcnt lgkmcnt(6)\n"
    "v_mfma_f32_16x16x32_bf16 v[80:83], v[40:43], v[240:243], v[80:83]\n"
    "ds_read_b64_tr_b16 v[60:61], v24 offset:20192\n"
    "ds_read_b64_tr_b16 v[62:63], v24 offset:20448\n"
    "v_mfma_f32_16x16x32_bf16 v[84:87], v[40:43], v[244:247], v[84:87]\n"
    "v_mfma_f32_16x16x32_bf16 v[88:91], v[44:47], v[240:243], v[88:91]\n"
    "ds_read_b128 v[248:251], v20 offset:65024\n"
    "ds_read_b64_tr_b16 v[32:33], v24 offset:24320\n"
    "ds_read_b64_tr_b16 v[34:35], v24 offset:24576\n"
    "v_mfma_f32_16x16x32_bf16 v[92:95], v[44:47], v[244:247], v[92:95]\n"
    "s_waitcnt lgkmcnt(7)\n"
    "v_mfma_f32_16x16x32_bf16 v[96:99], v[48:51], v[240:243], v[96:99]\n"
    "ds_read_b128 v[252:255], v21 offset:1536\n"
    "ds_read_b64_tr_b16 v[36:37], v24 offset:24352\n"
    "ds_read_b64_tr_b16 v[38:39], v24 offset:24608\n"
    "v_mfma_f32_16x16x32_bf16 v[100:103], v[48:51], v[244:247], v[100:103]\n"
    "v_mfma_f32_16x16x32_bf16 v[104:107], v[52:55], v[240:243], v[104:107]\n"
    "ds_read_b64_tr_b16 v[40:41], v24 offset:24384\n"
    "ds_read_b64_tr_b16 v[42:43], v24 offset:24640\n"
    "v_mfma_f32_16x16x32_bf16 v[108:111], v[52:55], v[244:247], v[108:111]\n"
    "s_waitcnt lgkmcnt(8)\n"
    "v_mfma_f32_16x16x32_bf16 v[112:115], v[56:59], v[240:243], v[112:115]\n"
    "ds_read_b64_tr_b16 v[44:45], v24 offset:24416\n"
    "ds_read_b64_tr_b16 v[46:47], v24 offset:24672\n"
    "v_mfma_f32_16x16x32_bf16 v[116:119], v[56:59], v[244:247], v[116:119]\n"
    "v_mfma_f32_16x16x32_bf16 v[120:123], v[60:63], v[240:243], v[120:123]\n"
    "ds_read_b64_tr_b16 v[48:49], v24 offset:28544\n"
    "ds_read_b64_tr_b16 v[50:51], v24 offset:28800\n"
    "v_mfma_f32_16x16x32_bf16 v[124:127], v[60:63], v[244:247], v[124:127]\n"
    "s_waitcnt lgkmcnt(6)\n"
    "v_mfma_f32_16x16x32_bf16 v[64:67], v[32:35], v[248:251], v[64:67]\n"
    "ds_read_b64_tr_b16 v[52:53], v24 offset:28576\n"
    "ds_read_b64_tr_b16 v[54:55], v24 offset:28832\n"
    "v_mfma_f32_16x16x32_bf16 v[68:71], v[32:35], v[252:255], v[68:71]\n"
    "v_mfma_f32_16x16x32_bf16 v[72:75], v[36:39], v[248:251], v[72:75]\n"
    "ds_read_b64_tr_b16 v[56:57], v24 offset:28608\n"
    "ds_read_b64_tr_b16 v[58:59], v24 offset:28864\n"
    "v_mfma_f32_16x16x32_bf16 v[76:79], v[36:39], v[252:255], v[76:79]\n"
    "s_waitcnt lgkmcnt(6)\n"
    "v_mfma_f32_16x16x32_bf16 v[80:83], v[40:43], v[248:251], v[80:83]\n"
    "ds_read_b64_tr_b16 v[60:61], v24 offset:28640\n"
    "ds_read_b64_tr_b16 v[62:63], v24 offset:28896\n"
    "v_mfma_f32_16x16x32_bf16 v[84:87], v[40:43], v[252:255], v[84:87]\n"
    "s_barrier\n"
    "v_mfma_f32_16x16x32_bf16 v[88:91], v[44:47], v[248:251], v[88:91]\n"
    "v_mfma_f32_16x16x32_bf16 v[92:95], v[44:47], v[252:255], v[92:95]\n"
    "s_waitcnt lgkmcnt(4)\n"
    "v_mfma_f32_16x16x32_bf16 v[96:99], v[48:51], v[248:251], v[96:99]\n"
    "v_mfma_f32_16x16x32_bf16 v[100:103], v[48:51], v[252:255], v[100:103]\n"
    "v_mfma_f32_16x16x32_bf16 v[104:107], v[52:55], v[248:251], v[104:107]\n"
    "v_mfma_f32_16x16x32_bf16 v[108:111], v[52:55], v[252:255], v[108:111]\n"
    "s_waitcnt lgkmcnt(0)\n"
    "v_mfma_f32_16x16x32_bf16 v[112:115], v[56:59], v[248:251], v[112:115]\n"
    "v_mfma_f32_16x16x32_bf16 v[116:119], v[56:59], v[252:255], v[116:119]\n"
    "v_mfma_f32_16x16x32_bf16 v[120:123], v[60:63], v[248:251], v[120:123]\n"
    "v_mfma_f32_16x16x32_bf16 v[124:127], v[60:63], v[252:255], v[124:127]\n"
    "s_cmp_lt_u32 s50, 12\n"
    "s_cbranch_scc1 .Lhb\n"
    "s_lshl_b32 s94, s50, 12\n"
    "s_add_u32 s94, s94, s81\n"
    "s_add_u32 s75, s94, 12288\n"
    "s_add_u32 m0, s66, 0x19d00\n"
    "s_nop 0\n"
    "buffer_load_dwordx4 v28, s[12:15], s69 offen offset:0 lds\n"
    "buffer_load_dwordx4 v29, s[12:15], s69 offen offset:1024 lds\n"
    "buffer_load_dwordx4 v30, s[12:15], s69 offen offset:2048 lds\n"
    "s_add_u32 m0, s66, 0x1cd80\n"
    "s_nop 0\n"
    "buffer_load_dwordx4 v28, s[16:19], s69 offen offset:0 lds\n"
    "buffer_load_dwordx4 v29, s[16:19], s69 offen offset:1024 lds\n"
    "buffer_load_dwordx4 v30, s[16:19], s69 offen offset:2048 lds\n"
    "v_mov_b32 v40, s45\n"
    "v_pk_mul_f32 v[32:33], v[64:65], v[64:65]\n"
    "v_pk_mul_f32 v[34:35], v[66:67], v[66:67]\n"
    "v_pk_mul_f32 v[36:37], v[68:69], v[68:69]\n"
    "v_pk_mul_f32 v[38:39], v[70:71], v[70:71]\n"
    "v_pk_fma_f32 v[32:33], v[32:33], s[44:45], v[40:41] op_sel_hi:[1,0,0]\n"
    "v_pk_fma_f32 v[34:35], v[34:35], s[44:45], v[40:41] op_sel_hi:[1,0,0]\n"
    "v_pk_fma_f32 v[36:37], v[36:37], s[44:45], v[40:41] op_sel_hi:[1,0,0]\n"
    "v_pk_fma_f32 v[38:39], v[38:39], s[44:45], v[40:41] op_sel_hi:[1,0,0]\n"
    "v_pk_mul_f32 v[32:33], v[32:33], v[64:65]\n"
    "v_pk_mul_f32 v[34:35], v[34:35], v[66:67]\n"
    "v_pk_mul_f32 v[36:37], v[36:37], v[68:69]\n"
    "v_pk_mul_f32 v[38:39], v[38:39], v[70:71]\n"
    "v_exp_f32 v32, v32\n"
    "v_exp_f32 v33, v33\n"
    "v_exp_f32 v34, v34\n"
    "v_exp_f32 v35, v35\n"
    "v_exp_f32 v36, v36\n"
    "v_exp_f32 v37, v37\n"
    "v_exp_f32 v38, v38\n"
    "v_exp_f32 v39, v39\n"
    "v_pk_add_f32 v[32:33], v[32:33], 1.0 op_sel_hi:[1,0]\n"
    "v_pk_add_f32 v[34:35], v[34:35], 1.0 op_sel_hi:[1,0]\n"
    "v_pk_add_f32 v[36:37], v[36:37], 1.0 op_sel_hi:[1,0]\n"
    "v_pk_add_f32 v[38:39], v[38:39], 1.0 op_sel_hi:[1,0]\n"
    "v_rcp_f32 v32, v32\n"
    "v_rcp_f32 v33, v33\n"
    "v_rcp_f32 v34, v34\n"
    "v_rcp_f32 v35, v35\n"
    "v_rcp_f32 v36, v36\n"
    "v_rcp_f32 v37, v37\n"
    "v_rcp_f32 v38, v38\n"
    "v_rcp_f32 v39, v39\n"
    "v_pk_mul_f32 v[96:97], v[96:97], v[64:65]\n"
    "v_pk_mul_f32 v[98:99], v[98:99], v[66:67]\n"
    "v_pk_mul_f32 v[100:101], v[100:101], v[68:69]\n"
    "v_pk_mul_f32 v[102:103], v[102:103], v[70:71]\n"
    "v_pk_mul_f32 v[32:33], v[32:33], v[96:97]\n"
    "v_pk_mul_f32 v[34:35], v[34:35], v[98:99]\n"
    "v_pk_mul_f32 v[36:37], v[36:37], v[100:101]\n"
    "v_pk_mul_f32 v[38:39], v[38:39], v[102:103]\n"
    "v_cvt_pk_bf16_f32 v8, v32, v33\n"
    "v_cvt_pk_bf16_f32 v10, v36, v37\n"
    "v_cvt_pk_bf16_f32 v9, v34, v35\n"
    "v_cvt_pk_bf16_f32 v11, v38, v39\n"
    "v_permlane16_swap_b32 v8, v10\n"
    "s_nop 0\n"
    "v_permlane16_swap_b32 v9, v11\n"
    "buffer_store_dwordx4 v[8:11], v1, s[28:31], s75 offen offset:0 sc1\n"
    "s_barrier\n"
    "s_add_u32 m0, s66, 0x21f00\n"
    "s_nop 0\n"
    "buffer_load_dwordx4 v28, s[12:15], s70 offen offset:0 lds\n"
    "buffer_load_dwordx4 v29, s[12:15], s70 offen offset:1024 lds\n"
    "buffer_load_dwordx4 v30, s[12:15], s70 offen offset:2048 lds\n"
    "s_add_u32 m0, s66, 0x24f80\n"
    "s_nop 0\n"
    "buffer_load_dwordx4 v28, s[16:19], s70 offen offset:0 lds\n"
    "buffer_load_dwordx4 v29, s[16:19], s70 offen offset:1024 lds\n"
    "buffer_load_dwordx4 v30, s[16:19], s70 offen offset:2048 lds\n"
    "v_pk_mul_f32 v[32:33], v[72:73], v[72:73]\n"
    "v_pk_mul_f32 v[34:35], v[74:75], v[74:75]\n"
    "v_pk_mul_f32 v[36:37], v[76:77], v[76:77]\n"
    "v_pk_mul_f32 v[38:39], v[78:79], v[78:79]\n"
    "v_pk_fma_f32 v[32:33], v[32:33], s[44:45], v[40:41] op_sel_hi:[1,0,0]\n"
    "v_pk_fma_f32 v[34:35], v[34:35], s[44:45], v[40:41] op_sel_hi:[1,0,0]\n"
    "v_pk_fma_f32 v[36:37], v[36:37], s[44:45], v[40:41] op_sel_hi:[1,0,0]\n"
    "v_pk_fma_f32 v[38:39], v[38:39], s[44:45], v[40:41] op_sel_hi:[1,0,0]\n"
    "v_pk_mul_f32 v[32:33], v[32:33], v[72:73]\n"
    "v_pk_mul_f32 v[34:35], v[34:35], v[74:75]\n"
    "v_pk_mul_f32 v[36:37], v[36:37], v[76:77]\n"
    "v_pk_mul_f32 v[38:39], v[38:39], v[78:79]\n"
    "v_exp_f32 v32, v32\n"
    "v_exp_f32 v33, v33\n"
    "v_exp_f32 v34, v34\n"
    "v_exp_f32 v35, v35\n"
    "v_exp_f32 v36, v36\n"
    "v_exp_f32 v37, v37\n"
    "v_exp_f32 v38, v38\n"
    "v_exp_f32 v39, v39\n"
    "v_pk_add_f32 v[32:33], v[32:33], 1.0 op_sel_hi:[1,0]\n"
    "v_pk_add_f32 v[34:35], v[34:35], 1.0 op_sel_hi:[1,0]\n"
    "v_pk_add_f32 v[36:37], v[36:37], 1.0 op_sel_hi:[1,0]\n"
    "v_pk_add_f32 v[38:39], v[38:39], 1.0 op_sel_hi:[1,0]\n"
    "v_rcp_f32 v32, v32\n"
    "v_rcp_f32 v33, v33\n"
    "v_rcp_f32 v34, v34\n"
    "v_rcp_f32 v35, v35\n"
    "v_rcp_f32 v36, v36\n"
    "v_rcp_f32 v37, v37\n"
    "v_rcp_f32 v38, v38\n"
    "v_rcp_f32 v39, v39\n"
    "v_pk_mul_f32 v[104:105], v[104:105], v[72:73]\n"
    "v_pk_mul_f32 v[106:107], v[106:107], v[74:75]\n"
    "v_pk_mul_f32 v[108:109], v[108:109], v[76:77]\n"
    "v_pk_mul_f32 v[110:111], v[110:111], v[78:79]\n"
    "v_pk_mul_f32 v[32:33], v[32:33], v[104:105]\n"
    "v_pk_mul_f32 v[34:35], v[34:35], v[106:107]\n"
    "v_pk_mul_f32 v[36:37], v[36:37], v[108:109]\n"
    "v_pk_mul_f32 v[38:39], v[38:39], v[110:111]\n"
    "v_cvt_pk_bf16_f32 v12, v32, v33\n"
    "v_cvt_pk_bf16_f32 v14, v36, v37\n"
    "v_cvt_pk_bf16_f32 v13, v34, v35\n"
    "v_cvt_pk_bf16_f32 v15, v38, v39\n"
    "v_permlane16_swap_b32 v12, v14\n"
    "s_nop 0\n"
    "v_permlane16_swap_b32 v13, v15\n"
    "buffer_store_dwordx4 v[12:15], v1, s[28:31], s75 offen offset:1024 sc1\n"
    "s_waitcnt vmcnt(8)\n"
    "s_barrier\n"
    "s_add_u32 m0, s66, 0x9900\n"
    "s_nop 0\n"
    "buffer_load_dwordx4 v28, s[12:15], s71 offen offset:0 lds\n"
    "buffer_load_dwordx4 v29, s[12:15], s71 offen offset:1024 lds\n"
    "buffer_load_dwordx4 v30, s[12:15], s71 offen offset:2048 lds\n"
    "s_add_u32 m0, s66, 0xc980\n"
    "s_nop 0\n"
    "buffer_load_dwordx4 v28, s[16:19], s71 offen offset:0 lds\n"
    "buffer_load_dwordx4 v29, s[16:19], s71 offen offset:1024 lds\n"
    "buffer_load_dwordx4 v30, s[16:19], s71 offen offset:2048 lds\n"
    "v_pk_mul_f32 v[32:33], v[80:81], v[80:81]\n"
    "v_pk_mul_f32 v[34:35], v[82:83], v[82:83]\n"
    "v_pk_mul_f32 v[36:37], v[84:85], v[84:85]\n"
    "v_pk_mul_f32 v[38:39], v[86:87], v[86:87]\n"
    "v_pk_fma_f32 v[32:33], v[32:33], s[44:45], v[40:41] op_sel_hi:[1,0,0]\n"
    "v_pk_fma_f32 v[34:35], v[34:35], s[44:45], v[40:41] op_sel_hi:[1,0,0]\n"
    "v_pk_fma_f32 v[36:37], v[36:37], s[44:45], v[40:41] op_sel_hi:[1,0,0]\n"
    "v_pk_fma_f32 v[38:39], v[38:39], s[44:45], v[40:41] op_sel_hi:[1,0,0]\n"
    "v_pk_mul_f32 v[32:33], v[32:33], v[80:81]\n"
    "v_pk_mul_f32 v[34:35], v[34:35], v[82:83]\n"
    "v_pk_mul_f32 v[36:37], v[36:37], v[84:85]\n"
    "v_pk_mul_f32 v[38:39], v[38:39], v[86:87]\n"
    "v_exp_f32 v32, v32\n"
    "v_exp_f32 v33, v33\n"
    "v_exp_f32 v34, v34\n"
    "v_exp_f32 v35, v35\n"
    "v_exp_f32 v36, v36\n"
    "v_exp_f32 v37, v37\n"
    "v_exp_f32 v38, v38\n"
    "v_exp_f32 v39, v39\n"
    "v_pk_add_f32 v[32:33], v[32:33], 1.0 op_sel_hi:[1,0]\n"
    "v_pk_add_f32 v[34:35], v[34:35], 1.0 op_sel_hi:[1,0]\n"
    "v_pk_add_f32 v[36:37], v[36:37], 1.0 op_sel_hi:[1,0]\n"
    "v_pk_add_f32 v[38:39], v[38:39], 1.0 op_sel_hi:[1,0]\n"
    "v_rcp_f32 v32, v32\n"
    "v_rcp_f32 v33, v33\n"
    "v_rcp_f32 v34, v34\n"
    "v_rcp_f32 v35, v35\n"
    "v_rcp_f32 v36, v36\n"
    "v_rcp_f32 v37, v37\n"
    "v_rcp_f32 v38, v38\n"
    "v_rcp_f32 v39, v39\n"
    "v_pk_mul_f32 v[112:113], v[112:113], v[80:81]\n"
    "v_pk_mul_f32 v[114:115], v[114:115], v[82:83]\n"
    "v_pk_mul_f32 v[116:117], v[116:117], v[84:85]\n"
    "v_pk_mul_f32 v[118:119], v[118:119], v[86:87]\n"
    "v_pk_mul_f32 v[32:33], v[32:33], v[112:113]\n"
    "v_pk_mul_f32 v[34:35], v[34:35], v[114:115]\n"
    "v_pk_mul_f32 v[36:37], v[36:37], v[116:117]\n"
    "v_pk_mul_f32 v[38:39], v[38:39], v[118:119]\n"
    "v_cvt_pk_bf16_f32 v8, v32, v33\n"
    "v_cvt_pk_bf16_f32 v10, v36, v37\n"
    "v_cvt_pk_bf16_f32 v9, v34, v35\n"
    "v_cvt_pk_bf16_f32 v11, v38, v39\n"
    "v_permlane16_swap_b32 v8, v10\n"
    "s_nop 0\n"
    "v_permlane16_swap_b32 v9, v11\n"
    "buffer_store_dwordx4 v[8:11], v1, s[28:31], s75 offen offset:2048 sc1\n"
    "s_waitcnt vmcnt(8)\n"
    "s_barrier\n"
    "s_add_u32 m0, s66, 0x11b00\n"
    "s_nop 0\n"
    "buffer_load_dwordx4 v28, s[12:15], s72 offen offset:0 lds\n"
    "buffer_load_dwordx4 v29, s[12:15], s72 offen offset:1024 lds\n"
    "buffer_load_dwordx4 v30, s[12:15], s72 offen offset:2048 lds\n"
    "s_add_u32 m0, s66, 0x14b80\n"
    "s_nop 0\n"
    "buffer_load_dwordx4 v28, s[16:19], s72 offen offset:0 lds\n"
    "buffer_load_dwordx4 v29, s[16:19], s72 offen offset:1024 lds\n"
    "buffer_load_dwordx4 v30, s[16:19], s72 offen offset:2048 lds\n"
    "v_pk_mul_f32 v[32:33], v[88:89], v[88:89]\n"
    "v_pk_mul_f32 v[34:35], v[90:91], v[90:91]\n"
    "v_pk_mul_f32 v[36:37], v[92:93], v[92:93]\n"
    "v_pk_mul_f32 v[38:39], v[94:95], v[94:95]\n"
    "v_pk_fma_f32 v[32:33], v[32:33], s[44:45], v[40:41] op_sel_hi:[1,0,0]\n"
    "v_pk_fma_f32 v[34:35], v[34:35], s[44:45], v[40:41] op_sel_hi:[1,0,0]\n"
    "v_pk_fma_f32 v[36:37], v[36:37], s[44:45], v[40:41] op_sel_hi:[1,0,0]\n"
    "v_pk_fma_f32 v[38:39], v[38:39], s[44:45], v[40:41] op_sel_hi:[1,0,0]\n"
    "v_pk_mul_f32 v[32:33], v[32:33], v[88:89]\n"
    "v_pk_mul_f32 v[34:35], v[34:35], v[90:91]\n"
    "v_pk_mul_f32 v[36:37], v[36:37], v[92:93]\n"
    "v_pk_mul_f32 v[38:39], v[38:39], v[94:95]\n"
    "v_exp_f32 v32, v32\n"
    "v_exp_f32 v33, v33\n"
    "v_exp_f32 v34, v34\n"
    "v_exp_f32 v35, v35\n"
    "v_exp_f32 v36, v36\n"
    "v_exp_f32 v37, v37\n"
    "v_exp_f32 v38, v38\n"
    "v_exp_f32 v39, v39\n"
    "v_pk_add_f32 v[32:33], v[32:33], 1.0 op_sel_hi:[1,0]\n"
    "v_pk_add_f32 v[34:35], v[34:35], 1.0 op_sel_hi:[1,0]\n"
    "v_pk_add_f32 v[36:37], v[36:37], 1.0 op_sel_hi:[1,0]\n"
    "v_pk_add_f32 v[38:39], v[38:39], 1.0 op_sel_hi:[1,0]\n"
    "v_rcp_f32 v32, v32\n"
    "v_rcp_f32 v33, v33\n"
    "v_rcp_f32 v34, v34\n"
    "v_rcp_f32 v35, v35\n"
    "v_rcp_f32 v36, v36\n"
    "v_rcp_f32 v37, v37\n"
    "v_rcp_f32 v38, v38\n"
    "v_rcp_f32 v39, v39\n"
    "v_pk_mul_f32 v[120:121], v[120:121], v[88:89]\n"
    "v_pk_mul_f32 v[122:123], v[122:123], v[90:91]\n"
    "v_pk_mul_f32 v[124:125], v[124:125], v[92:93]\n"
    "v_pk_mul_f32 v[126:127], v[126:127], v[94:95]\n"
    "v_pk_mul_f32 v[32:33], v[32:33], v[120:121]\n"
    "v_pk_mul_f32 v[34:35], v[34:35], v[122:123]\n"
    "v_pk_mul_f32 v[36:37], v[36:37], v[124:125]\n"
    "v_pk_mul_f32 v[38:39], v[38:39], v[126:127]\n"
    "v_cvt_pk_bf16_f32 v12, v32, v33\n"
    "v_cvt_pk_bf16_f32 v14, v36, v37\n"
    "v_cvt_pk_bf16_f32 v13, v34, v35\n"
    "v_cvt_pk_bf16_f32 v15, v38, v39\n"
    "v_permlane16_swap_b32 v12, v14\n"
    "s_nop 0\n"
    "v_permlane16_swap_b32 v13, v15\n"
    "buffer_store_dwordx4 v[12:15], v1, s[28:31], s75 offen offset:3072 sc1\n"
    "s_waitcnt vmcnt(8)\n"
    "s_barrier\n"
    "s_add_u32 m0, s66, 0x19d00\n"
    "s_nop 0\n"
    "buffer_load_dwordx4 v28, s[12:15], s73 offen offset:0 lds\n"
    "buffer_load_dwordx4 v29, s[12:15], s73 offen offset:1024 lds\n"
    "buffer_load_dwordx4 v30, s[12:15], s73 offen offset:2048 lds\n"
    "s_add_u32 m0, s66, 0x1cd80\n"
    "s_nop 0\n"
    "buffer_load_dwordx4 v28, s[16:19], s73 offen offset:0 lds\n"
    "buffer_load_dwordx4 v29, s[16:19], s73 offen offset:1024 lds\n"
    "buffer_load_dwordx4 v30, s[16:19], s73 offen offset:2048 lds\n"
    "s_cmp_eq_u32 s50, 15\n"
    "s_cbranch_scc0 .Lskip_7\n"
    "v_and_b32 v8, 48, v0\n"
    "s_lshl1_add_u32 s94, s52, 0\n"
    "s_and_b32 s94, s94, 15\n"
    "s_lshl_b32 s94, s94, 6\n"
    "buffer_load_dwordx4 v[64:67], v8, s[36:39], s94 offen\n"
    "s_lshl1_add_u32 s94, s52, 1\n"
    "s_and_b32 s94, s94, 15\n"
    "s_lshl_b32 s94, s94, 6\n"
    "buffer_load_dwordx4 v[68:71], v8, s[36:39], s94 offen\n"
    "s_lshl1_add_u32 s94, s52, 2\n"
    "s_and_b32 s94, s94, 15\n"
    "s_lshl_b32 s94, s94, 6\n"
    "buffer_load_dwordx4 v[72:75], v8, s[36:39], s94 offen\n"
    "s_lshl1_add_u32 s94, s52, 3\n"
    "s_and_b32 s94, s94, 15\n"
    "s_lshl_b32 s94, s94, 6\n"
    "buffer_load_dwordx4 v[76:79], v8, s[36:39], s94 offen\n"
    "s_lshl1_add_u32 s94, s52, 4\n"
    "s_and_b32 s94, s94, 15\n"
    "s_lshl_b32 s94, s94, 6\n"
    "buffer_load_dwordx4 v[80:83], v8, s[36:39], s94 offen\n"
    "s_lshl1_add_u32 s94, s52, 5\n"
    "s_and_b32 s94, s94, 15\n"
    "s_lshl_b32 s94, s94, 6\n"
    "buffer_load_dwordx4 v[84:87], v8, s[36:39], s94 offen\n"
    "s_lshl1_add_u32 s94, s52, 6\n"
    "s_and_b32 s94, s94, 15\n"
    "s_lshl_b32 s94, s94, 6\n"
    "buffer_load_dwordx4 v[88:91], v8, s[36:39], s94 offen\n"
    "s_lshl1_add_u32 s94, s52, 7\n"
    "s_and_b32 s94, s94, 15\n"
    "s_lshl_b32 s94, s94, 6\n"
    "buffer_load_dwordx4 v[92:95], v8, s[36:39], s94 offen\n"
    "s_lshl1_add_u32 s94, s52, 8\n"
    "s_and_b32 s94, s94, 15\n"
    "s_lshl_b32 s94, s94, 6\n"
    "buffer_load_dwordx4 v[96:99], v8, s[36:39], s94 offen\n"
    "s_lshl1_add_u32 s94, s52, 9\n"
    "s_and_b32 s94, s94, 15\n"
    "s_lshl_b32 s94, s94, 6\n"
    "buffer_load_dwordx4 v[100:103], v8, s[36:39], s94 offen\n"
    "s_lshl1_add_u32 s94, s52, 10\n"
    "s_and_b32 s94, s94, 15\n"
    "s_lshl_b32 s94, s94, 6\n"
    "buffer_load_dwordx4 v[104:107], v8, s[36:39], s94 offen\n"
    "s_lshl1_add_u32 s94, s52, 11\n"
    "s_and_b32 s94, s94, 15\n"
    "s_lshl_b32 s94, s94, 6\n"
    "buffer_load_dwordx4 v[108:111], v8, s[36:39], s94 offen\n"
    "s_lshl1_add_u32 s94, s52, 12\n"
    "s_and_b32 s94, s94, 15\n"
    "s_lshl_b32 s94, s94, 6\n"
    "buffer_load_dwordx4 v[112:115], v8, s[36:39], s94 offen\n"
    "s_lshl1_add_u32 s94, s52, 13\n"
    "s_and_b32 s94, s94, 15\n"
    "s_lshl_b32 s94, s94, 6\n"
    "buffer_load_dwordx4 v[116:119], v8, s[36:39], s94 offen\n"
    "s_lshl1_add_u32 s94, s52, 14\n"
    "s_and_b32 s94, s94, 15\n"
    "s_lshl_b32 s94, s94, 6\n"
    "buffer_load_dwordx4 v[120:123], v8, s[36:39], s94 offen\n"
    "s_lshl1_add_u32 s94, s52, 15\n"
    "s_and_b32 s94, s94, 15\n"
    "s_lshl_b32 s94, s94, 6\n"
    "buffer_load_dwordx4 v[124:127], v8, s[36:39], s94 offen\n"
    "s_waitcnt vmcnt(0)\n"
    "v_mfma_f32_16x16x32_bf16 v[32:35], v[64:67], v[128:131], 0\n"
    "s_waitcnt vmcnt(0)\n"
    "v_mfma_f32_16x16x32_bf16 v[36:39], v[64:67], v[132:135], 0\n"
    "s_waitcnt vmcnt(0)\n"
    "v_mfma_f32_16x16x32_bf16 v[32:35], v[68:71], v[136:139], v[32:35]\n"
    "s_waitcnt vmcnt(0)\n"
    "v_mfma_f32_16x16x32_bf16 v[36:39], v[68:71], v[140:143], v[36:39]\n"
    "s_waitcnt vmcnt(0)\n"
    "v_mfma_f32_16x16x32_bf16 v[32:35], v[72:75], v[144:147], v[32:35]\n"
    "s_waitcnt vmcnt(0)\n"
    "v_mfma_f32_16x16x32_bf16 v[36:39], v[72:75], v[148:151], v[36:39]\n"
    "s_waitcnt vmcnt(0)\n"
    "v_mfma_f32_16x16x32_bf16 v[32:35], v[76:79], v[152:155], v[32:35]\n"
    "s_waitcnt vmcnt(0)\n"
    "v_mfma_f32_16x16x32_bf16 v[36:39], v[76:79], v[156:159], v[36:39]\n"
    "s_waitcnt vmcnt(0)\n"
    "v_mfma_f32_16x16x32_bf16 v[32:35], v[80:83], v[160:163], v[32:35]\n"
    "s_waitcnt vmcnt(0)\n"
    "v_mfma_f32_16x16x32_bf16 v[36:39], v[80:83], v[164:167], v[36:39]\n"
    "s_waitcnt vmcnt(0)\n"
    "v_mfma_f32_16x16x32_bf16 v[32:35], v[84:87], v[168:171], v[32:35]\n"
    "s_waitcnt vmcnt(0)\n"
    "v_mfma_f32_16x16x32_bf16 v[36:39], v[84:87], v[172:175], v[36:39]\n"
    "s_waitcnt vmcnt(0)\n"
    "v_mfma_f32_16x16x32_bf16 v[32:35], v[88:91], v[176:179], v[32:35]\n"
    "s_waitcnt vmcnt(0)\n"
    "v_mfma_f32_16x16x32_bf16 v[36:39], v[88:91], v[180:183], v[36:39]\n"
    "s_waitcnt vmcnt(0)\n"
    "v_mfma_f32_16x16x32_bf16 v[32:35], v[92:95], v[184:187], v[32:35]\n"
    "s_waitcnt vmcnt(0)\n"
    "v_mfma_f32_16x16x32_bf16 v[36:39], v[92:95], v[188:191], v[36:39]\n"
    "s_waitcnt vmcnt(0)\n"
    "v_mfma_f32_16x16x32_bf16 v[32:35], v[96:99], v[192:195], v[32:35]\n"
    "s_waitcnt vmcnt(0)\n"
    "v_mfma_f32_16x16x32_bf16 v[36:39], v[96:99], v[196:199], v[36:39]\n"
    "s_waitcnt vmcnt(0)\n"
    "v_mfma_f32_16x16x32_bf16 v[32:35], v[100:103], v[200:203], v[32:35]\n"
    "s_waitcnt vmcnt(0)\n"
    "v_mfma_f32_16x16x32_bf16 v[36:39], v[100:103], v[204:207], v[36:39]\n"
    "s_waitcnt vmcnt(0)\n"
    "v_mfma_f32_16x16x32_bf16 v[32:35], v[104:107], v[208:211], v[32:35]\n"
    "s_waitcnt vmcnt(0)\n"
    "v_mfma_f32_16x16x32_bf16 v[36:39], v[104:107], v[212:215], v[36:39]\n"
    "s_waitcnt vmcnt(0)\n"
    "v_mfma_f32_16x16x32_bf16 v[32:35], v[108:111], v[216:219], v[32:35]\n"
    "s_waitcnt vmcnt(0)\n"
    "v_mfma_f32_16x16x32_bf16 v[36:39], v[108:111], v[220:223], v[36:39]\n"
    "s_waitcnt vmcnt(0)\n"
    "v_mfma_f32_16x16x32_bf16 v[32:35], v[112:115], v[224:227], v[32:35]\n"
    "s_waitcnt vmcnt(0)\n"
    "v_mfma_f32_16x16x32_bf16 v[36:39], v[112:115], v[228:231], v[36:39]\n"
    "s_waitcnt vmcnt(0)\n"
    "v_mfma_f32_16x16x32_bf16 v[32:35], v[116:119], v[232:235], v[32:35]\n"
    "s_waitcnt vmcnt(0)\n"
    "v_mfma_f32_16x16x32_bf16 v[36:39], v[116:119], v[236:239], v[36:39]\n"
    "s_waitcnt vmcnt(0)\n"
    "v_mfma_f32_16x16x32_bf16 v[32:35], v[120:123], v[240:243], v[32:35]\n"
    "s_waitcnt vmcnt(0)\n"
    "v_mfma_f32_16x16x32_bf16 v[36:39], v[120:123], v[244:247], v[36:39]\n"
    "s_waitcnt vmcnt(0)\n"
    "v_mfma_f32_16x16x32_bf16 v[32:35], v[124:127], v[248:251], v[32:35]\n"
    "s_waitcnt vmcnt(0)\n"
    "v_mfma_f32_16x16x32_bf16 v[36:39], v[124:127], v[252:255], v[36:39]\n"
    "s_nop 7\n"
    "v_cndmask_b32 v8, v36, v32, s[42:43]\n"
    "v_mul_f32 v8, 0xbfb8aa3b, v8\n"
    "v_exp_f32 v8, v8\n"
    "s_nop 0\n"
    "v_add_f32 v8, 1.0, v8\n"
    "v_rcp_f32 v8, v8\n"
    "v_lshlrev_b32 v9, 2, v0\n"
    "s_lshl_b32 s94, s58, 7\n"
    "s_add_u32 s94, s94, 0x80000\n"
    "s_mov_b32 exec_hi, 0\n"
    "buffer_store_dword v8, v9, s[28:31], s94 offen sc1\n"
    "s_mov_b64 exec, -1\n"
    ".Lskip_7:\n"
    "s_waitcnt vmcnt(7)\n"
    "s_barrier\n"
    "s_add_u32 m0, s66, 0x21f00\n"
    "s_nop 0\n"
    "buffer_load_dwordx4 v28, s[12:15], s74 offen offset:0 lds\n"
    "buffer_load_dwordx4 v29, s[12:15], s74 offen offset:1024 lds\n"
    "buffer_load_dwordx4 v30, s[12:15], s74 offen offset:2048 lds\n"
    "s_add_u32 m0, s66, 0x24f80\n"
    "s_nop 0\n"
    "buffer_load_dwordx4 v28, s[16:19], s74 offen offset:0 lds\n"
    "buffer_load_dwordx4 v29, s[16:19], s74 offen offset:1024 lds\n"
    "buffer_load_dwordx4 v30, s[16:19], s74 offen offset:2048 lds\n"
    "v_add_u32 v128, 0x10000, v1\n"
    "v_mov_b32 v129, 0xdddddddd\n"
    "v_and_b32 v6, 15, v0\n"
    "v_lshrrev_b32 v7, 4, v0\n"
    "v_lshlrev_b32 v134, 4, v6\n"
    "v_lshl_or_b32 v134, v7, 9, v134\n"
    "v_lshlrev_b32 v159, 4, v6\n"
    "v_lshl_add_u32 v159, v7, 2, v159\n"
    "v_add_u32 v159, 4, v159\n"
    "v_and_b32 v138, 1, v0\n"
    "v_lshl_add_u32 v138, s54, 1, v138\n"
    "v_lshlrev_b32 v138, 4, v138\n"
    "v_cmp_gt_u32 vcc, 32, v0\n"
    "s_lshr_b32 s94, s53, 3\n"
    "s_add_u32 s94, s94, 32\n"
    "v_cndmask_b32 v159, v138, v159, vcc\n"
    "v_add_lshl_u32 v159, v159, s94, 7\n"
    "s_lshr_b32 s94, s53, 1\n"
    "v_lshl_add_u32 v135, s94, 4, v6\n"
    "s_lshr_b32 s94, s48, 2\n"
    "s_lshl1_add_u32 s94, s54, s94\n"
    "s_lshl4_add_u32 s94, s94, 15\n"
    "v_mov_b32 v138, s94\n"
    "v_cndmask_b32 v135, v138, v135, s[42:43]\n"
    "v_and_b32 v142, 31, v0\n"
    "v_lshl_add_u32 v138, s48, 5, v142\n"
    "s_cmp_eq_u32 s2, 0\n"
    "s_cselect_b32 s94, 32, 64\n"
    "v_cmp_gt_u32 vcc, s94, v0\n"
    "s_movk_i32 s94, 288\n"
    "v_lshlrev_b32 v142, 2, v142\n"
    "v_cndmask_b32 v135, v138, v135, vcc\n"
    "v_add_lshl_u32 v135, v135, s94, 7\n"
    "v_lshlrev_b32 v140, 2, v6\n"
    "v_and_b32 v138, 7, v0\n"
    "s_lshr_b32 s94, s87, 10\n"
    "s_add_u32 s94, s94, 1024\n"
    "v_add_lshl_u32 v136, v138, s94, 7\n"
    "v_lshrrev_b32 v158, 3, v0\n"
    "v_lshlrev_b32 v157, 5, v138\n"
    "s_movk_i32 s95, 272\n"
    "v_mad_u32_u24 v157, v158, s95, v157\n"
    "s_mul_i32 s94, s48, 2176\n"
    "s_add_u32 s94, s94, 0x10000\n"
    "v_add_u32 v157, s94, v157\n"
    "v_lshlrev_b32 v156, 4, v7\n"
    "v_mad_u32_u24 v156, v6, s95, v156\n"
    "s_mul_i32 s94, s48, 8704\n"
    "s_add_u32 s94, s94, 0x10000\n"
    "v_add_u32 v156, s94, v156\n"
    "v_lshlrev_b32 v158, 10, v158\n"
    "v_lshl_or_b32 v158, v138, 4, v158\n"
    "v_and_b32 v130, 16, v0\n"
    "v_lshlrev_b32 v130, 1, v130\n"
    "v_xor_b32 v130, v130, v158\n"
    "v_xor_b32 v131, 64, v130\n"
    "v_add_u32 v132, 14336, v130\n"
    "v_add_u32 v133, 21504, v131\n"
    "v_add_u32 v131, 7168, v131\n"
    "v_bfe_u32 v138, v0, 2, 2\n"
    "v_lshl_or_b32 v144, v7, 3, v138\n"
    "v_lshlrev_b32 v144, 7, v144\n"
    "v_bfe_u32 v138, v0, 3, 2\n"
    "v_lshl_or_b32 v144, v138, 5, v144\n"
    "v_and_b32 v138, 3, v0\n"
    "v_lshl_or_b32 v144, v138, 3, v144\n"
    "s_lshl_b32 s94, s57, 13\n"
    "v_or_b32 v144, s94, v144\n"
    "v_xor_b32 v147, 32, v144\n"
    "v_xor_b32 v150, 64, v144\n"
    "v_xor_b32 v153, 96, v144\n"
    "v_add_u32 v145, 0x10000, v144\n"
    "v_add_u32 v146, 0x20000, v144\n"
    "v_add_u32 v148, 0x10000, v147\n"
    "v_add_u32 v149, 0x20000, v147\n"
    "v_add_u32 v151, 0x10000, v150\n"
    "v_add_u32 v152, 0x20000, v150\n"
    "v_add_u32 v154, 0x10000, v153\n"
    "v_add_u32 v155, 0x20000, v153\n"
    "v_and_b32 v138, 27, v0\n"
    "v_cmp_eq_u32 vcc, 16, v138\n"
    "v_mov_b32 v138, 0x3f803f80\n"
    "v_mov_b64 v[176:177], 0\n"
    "v_cndmask_b32 v168, 0, v138, vcc\n"
    "v_mov_b32 v169, v168\n"
    "v_mov_b64 v[170:171], v[168:169]\n"
    "v_mov_b64 v[172:173], v[168:169]\n"
    "v_mov_b64 v[174:175], v[168:169]\n"
    "v_mov_b64 v[178:179], 0\n"
    "v_mov_b64 v[180:181], 0\n"
    "v_mov_b64 v[182:183], 0\n"
    "v_mov_b64 v[184:185], 0\n"
    "v_mov_b64 v[186:187], 0\n"
    "v_mov_b64 v[188:189], 0\n"
    "v_mov_b64 v[190:191], 0\n"
    "s_waitcnt vmcnt(6)\n"
    "s_barrier\n"
    "s_waitcnt vmcnt(0)\n"
    "s_barrier\n"
    "buffer_load_dwordx4 v[160:163], v1, s[28:31], s80 offen sc1\n"
    "s_add_u32 m0, s79, 0x0\n"
    "s_nop 0\n"
    "buffer_load_dwordx4 v1, s[28:31], s78 offen offset:0 sc1 lds\n"
    "buffer_load_dwordx4 v1, s[28:31], s78 offen offset:1024 sc1 lds\n"
    "buffer_load_dwordx4 v1, s[28:31], s78 offen offset:2048 sc1 lds\n"
    "buffer_load_dwordx4 v1, s[28:31], s78 offen offset:3072 sc1 lds\n"
    "s_add_u32 m0, s79, 0x8000\n"
    "s_add_u32 s78, s78, 0x8000\n"
    "buffer_load_dwordx4 v1, s[28:31], s78 offen offset:0 sc1 lds\n"
    "buffer_load_dwordx4 v1, s[28:31], s78 offen offset:1024 sc1 lds\n"
    "buffer_load_dwordx4 v1, s[28:31], s78 offen offset:2048 sc1 lds\n"
    "buffer_load_dwordx4 v1, s[28:31], s78 offen offset:3072 sc1 lds\n"
    "s_waitcnt vmcnt(4)\n"
    "s_branch .Ljoin\n"
    ".Lhv:\n"
    "s_waitcnt lgkmcnt(0)\n"
    "s_barrier\n"
    "ds_read_b128 v[128:131], v16 offset:30720\n"
    "ds_read_b128 v[132:135], v16 offset:32768\n"
    "ds_read_b64_tr_b16 v[32:33], v22 offset:47104\n"
    "ds_read_b64_tr_b16 v[34:35], v22 offset:47360\n"
    "ds_read_b64_tr_b16 v[36:37], v22 offset:47136\n"
    "ds_read_b64_tr_b16 v[38:39], v22 offset:47392\n"
    "ds_read_b64_tr_b16 v[40:41], v22 offset:47168\n"
    "ds_read_b64_tr_b16 v[42:43], v22 offset:47424\n"
    "ds_read_b64_tr_b16 v[44:45], v22 offset:47200\n"
    "ds_read_b64_tr_b16 v[46:47], v22 offset:47456\n"
    "ds_read_b64_tr_b16 v[48:49], v22 offset:51328\n"
    "ds_read_b64_tr_b16 v[50:51], v22 offset:51584\n"
    "s_waitcnt lgkmcnt(6)\n"
    "v_mfma_f32_16x16x32_bf16 v[64:67], v[128:131], v[32:35], 0\n"
    "ds_read_b64_tr_b16 v[52:53], v22 offset:51360\n"
    "ds_read_b64_tr_b16 v[54:55], v22 offset:51616\n"
    "v_mfma_f32_16x16x32_bf16 v[68:71], v[132:135], v[32:35], 0\n"
    "v_mfma_f32_16x16x32_bf16 v[72:75], v[128:131], v[36:39], 0\n"
    "ds_read_b64_tr_b16 v[56:57], v22 offset:51392\n"
    "ds_read_b64_tr_b16 v[58:59], v22 offset:51648\n"
    "v_mfma_f32_16x16x32_bf16 v[76:79], v[132:135], v[36:39], 0\n"
    "s_waitcnt lgkmcnt(6)\n"
    "v_mfma_f32_16x16x32_bf16 v[80:83], v[128:131], v[40:43], 0\n"
    "ds_read_b64_tr_b16 v[60:61], v22 offset:51424\n"
    "ds_read_b64_tr_b16 v[62:63], v22 offset:51680\n"
    "v_mfma_f32_16x16x32_bf16 v[84:87], v[132:135], v[40:43], 0\n"
    "v_mfma_f32_16x16x32_bf16 v[88:91], v[128:131], v[44:47], 0\n"
    "ds_read_b128 v[136:139], v19 offset:30720\n"
    "ds_read_b64_tr_b16 v[32:33], v22 offset:55552\n"
    "ds_read_b64_tr_b16 v[34:35], v22 offset:55808\n"
    "v_mfma_f32_16x16x32_bf16 v[92:95], v[132:135], v[44:47], 0\n"
    "s_waitcnt lgkmcnt(7)\n"
    "v_mfma_f32_16x16x32_bf16 v[96:99], v[128:131], v[48:51], 0\n"
    "ds_read_b128 v[140:143], v19 offset:32768\n"
    "ds_read_b64_tr_b16 v[36:37], v22 offset:55584\n"
    "ds_read_b64_tr_b16 v[38:39], v22 offset:55840\n"
    "v_mfma_f32_16x16x32_bf16 v[100:103], v[132:135], v[48:51], 0\n"
    "v_mfma_f32_16x16x32_bf16 v[104:107], v[128:131], v[52:55], 0\n"
    "ds_read_b64_tr_b16 v[40:41], v22 offset:55616\n"
    "ds_read_b64_tr_b16 v[42:43], v22 offset:55872\n"
    "v_mfma_f32_16x16x32_bf16 v[108:111], v[132:135], v[52:55], 0\n"
    "s_waitcnt lgkmcnt(8)\n"
    "v_mfma_f32_16x16x32_bf16 v[112:115], v[128:131], v[56:59], 0\n"
    "ds_read_b64_tr_b16 v[44:45], v22 offset:55648\n"
    "ds_read_b64_tr_b16 v[46:47], v22 offset:55904\n"
    "v_mfma_f32_16x16x32_bf16 v[116:119], v[132:135], v[56:59], 0\n"
    "v_mfma_f32_16x16x32_bf16 v[120:123], v[128:131], v[60:63], 0\n"
    "ds_read_b64_tr_b16 v[48:49], v22 offset:59776\n"
    "ds_read_b64_tr_b16 v[50:51], v22 offset:60032\n"
    "v_mfma_f32_16x16x32_bf16 v[124:127], v[132:135], v[60:63], 0\n"
    "s_waitcnt lgkmcnt(6)\n"
    "v_mfma_f32_16x16x32_bf16 v[64:67], v[136:139], v[32:35], v[64:67]\n"
    "ds_read_b64_tr_b16 v[52:53], v22 offset:59808\n"
    "ds_read_b64_tr_b16 v[54:55], v22 offset:60064\n"
    "v_mfma_f32_16x16x32_bf16 v[68:71], v[140:143], v[32:35], v[68:71]\n"
    "v_mfma_f32_16x16x32_bf16 v[72:75], v[136:139], v[36:39], v[72:75]\n"
    "ds_read_b64_tr_b16 v[56:57], v22 offset:59840\n"
    "ds_read_b64_tr_b16 v[58:59], v22 offset:60096\n"
    "v_mfma_f32_16x16x32_bf16 v[76:79], v[140:143], v[36:39], v[76:79]\n"
    "s_waitcnt lgkmcnt(6)\n"
    "v_mfma_f32_16x16x32_bf16 v[80:83], v[136:139], v[40:43], v[80:83]\n"
    "ds_read_b64_tr_b16 v[60:61], v22 offset:59872\n"
    "ds_read_b64_tr_b16 v[62:63], v22 offset:60128\n"
    "v_mfma_f32_16x16x32_bf16 v[84:87], v[140:143], v[40:43], v[84:87]\n"
    "s_barrier\n"
    "v_mfma_f32_16x16x32_bf16 v[88:91], v[136:139], v[44:47], v[88:91]\n"
    "ds_read_b128 v[144:147], v16 offset:64000\n"
    "ds_read_b64_tr_b16 v[32:33], v23 offset:14848\n"
    "ds_read_b64_tr_b16 v[34:35], v23 offset:15104\n"
    "v_mfma_f32_16x16x32_bf16 v[92:95], v[140:143], v[44:47], v[92:95]\n"
    "s_waitcnt lgkmcnt(7)\n"
    "v_mfma_f32_16x16x32_bf16 v[96:99], v[136:139], v[48:51], v[96:99]\n"
    "ds_read_b128 v[148:151], v17 offset:512\n"
    "ds_read_b64_tr_b16 v[36:37], v23 offset:14880\n"
    "ds_read_b64_tr_b16 v[38:39], v23 offset:15136\n"
    "v_mfma_f32_16x16x32_bf16 v[100:103], v[140:143], v[48:51], v[100:103]\n"
    "v_mfma_f32_16x16x32_bf16 v[104:107], v[136:139], v[52:55], v[104:107]\n"
    "ds_read_b64_tr_b16 v[40:41], v23 offset:14912\n"
    "ds_read_b64_tr_b16 v[42:43], v23 offset:15168\n"
    "v_mfma_f32_16x16x32_bf16 v[108:111], v[140:143], v[52:55], v[108:111]\n"
    "s_waitcnt lgkmcnt(8)\n"
    "v_mfma_f32_16x16x32_bf16 v[112:115], v[136:139], v[56:59], v[112:115]\n"
    "ds_read_b64_tr_b16 v[44:45], v23 offset:14944\n"
    "ds_read_b64_tr_b16 v[46:47], v23 offset:15200\n"
    "v_mfma_f32_16x16x32_bf16 v[116:119], v[140:143], v[56:59], v[116:119]\n"
    "v_mfma_f32_16x16x32_bf16 v[120:123], v[136:139], v[60:63], v[120:123]\n"
    "ds_read_b64_tr_b16 v[48:49], v23 offset:19072\n"
    "ds_read_b64_tr_b16 v[50:51], v23 offset:19328\n"
    "v_mfma_f32_16x16x32_bf16 v[124:127], v[140:143], v[60:63], v[124:127]\n"
    "s_waitcnt lgkmcnt(6)\n"
    "v_mfma_f32_16x16x32_bf16 v[64:67], v[144:147], v[32:35], v[64:67]\n"
    "ds_read_b64_tr_b16 v[52:53], v23 offset:19104\n"
    "ds_read_b64_tr_b16 v[54:55], v23 offset:19360\n"
    "v_mfma_f32_16x16x32_bf16 v[68:71], v[148:151], v[32:35], v[68:71]\n"
    "v_mfma_f32_16x16x32_bf16 v[72:75], v[144:147], v[36:39], v[72:75]\n"
    "ds_read_b64_tr_b16 v[56:57], v23 offset:19136\n"
    "ds_read_b64_tr_b16 v[58:59], v23 offset:19392\n"
    "v_mfma_f32_16x16x32_bf16 v[76:79], v[148:151], v[36:39], v[76:79]\n"
    "s_waitcnt lgkmcnt(6)\n"
    "v_mfma_f32_16x16x32_bf16 v[80:83], v[144:147], v[40:43], v[80:83]\n"
    "ds_read_b64_tr_b16 v[60:61], v23 offset:19168\n"
    "ds_read_b64_tr_b16 v[62:63], v23 offset:19424\n"
    "v_mfma_f32_16x16x32_bf16 v[84:87], v[148:151], v[40:43], v[84:87]\n"
    "v_mfma_f32_16x16x32_bf16 v[88:91], v[144:147], v[44:47], v[88:91]\n"
    "ds_read_b128 v[152:155], v19 offset:64000\n"
    "ds_read_b64_tr_b16 v[32:33], v23 offset:23296\n"
    "ds_read_b64_tr_b16 v[34:35], v23 offset:23552\n"
    "v_mfma_f32_16x16x32_bf16 v[92:95], v[148:151], v[44:47], v[92:95]\n"
    "s_waitcnt lgkmcnt(7)\n"
    "v_mfma_f32_16x16x32_bf16 v[96:99], v[144:147], v[48:51], v[96:99]\n"
    "ds_read_b128 v[156:159], v20 offset:512\n"
    "ds_read_b64_tr_b16 v[36:37], v23 offset:23328\n"
    "ds_read_b64_tr_b16 v[38:39], v23 offset:23584\n"
    "v_mfma_f32_16x16x32_bf16 v[100:103], v[148:151], v[48:51], v[100:103]\n"
    "v_mfma_f32_16x16x32_bf16 v[104:107], v[144:147], v[52:55], v[104:107]\n"
    "ds_read_b64_tr_b16 v[40:41], v23 offset:23360\n"
    "ds_read_b64_tr_b16 v[42:43], v23 offset:23616\n"
    "v_mfma_f32_16x16x32_bf16 v[108:111], v[148:151], v[52:55], v[108:111]\n"
    "s_waitcnt lgkmcnt(8)\n"
    "v_mfma_f32_16x16x32_bf16 v[112:115], v[144:147], v[56:59], v[112:115]\n"
    "ds_read_b64_tr_b16 v[44:45], v23 offset:23392\n"
    "ds_read_b64_tr_b16 v[46:47], v23 offset:23648\n"
    "v_mfma_f32_16x16x32_bf16 v[116:119], v[148:151], v[56:59], v[116:119]\n"
    "v_mfma_f32_16x16x32_bf16 v[120:123], v[144:147], v[60:63], v[120:123]\n"
    "ds_read_b64_tr_b16 v[48:49], v23 offset:27520\n"
    "ds_read_b64_tr_b16 v[50:51], v23 offset:27776\n"
    "v_mfma_f32_16x16x32_bf16 v[124:127], v[148:151], v[60:63], v[124:127]\n"
    "s_waitcnt lgkmcnt(6)\n"
    "v_mfma_f32_16x16x32_bf16 v[64:67], v[152:155], v[32:35], v[64:67]\n"
    "ds_read_b64_tr_b16 v[52:53], v23 offset:27552\n"
    "ds_read_b64_tr_b16 v[54:55], v23 offset:27808\n"
    "v_mfma_f32_16x16x32_bf16 v[68:71], v[156:159], v[32:35], v[68:71]\n"
    "v_mfma_f32_16x16x32_bf16 v[72:75], v[152:155], v[36:39], v[72:75]\n"
    "ds_read_b64_tr_b16 v[56:57], v23 offset:27584\n"
    "ds_read_b64_tr_b16 v[58:59], v23 offset:27840\n"
    "v_mfma_f32_16x16x32_bf16 v[76:79], v[156:159], v[36:39], v[76:79]\n"
    "s_waitcnt lgkmcnt(6)\n"
    "v_mfma_f32_16x16x32_bf16 v[80:83], v[152:155], v[40:43], v[80:83]\n"
    "ds_read_b64_tr_b16 v[60:61], v23 offset:27616\n"
    "ds_read_b64_tr_b16 v[62:63], v23 offset:27872\n"
    "v_mfma_f32_16x16x32_bf16 v[84:87], v[156:159], v[40:43], v[84:87]\n"
    "s_barrier\n"
    "v_mfma_f32_16x16x32_bf16 v[88:91], v[152:155], v[44:47], v[88:91]\n"
    "ds_read_b128 v[160:163], v17 offset:31744\n"
    "ds_read_b64_tr_b16 v[32:33], v23 offset:48128\n"
    "ds_read_b64_tr_b16 v[34:35], v23 offset:48384\n"
    "v_mfma_f32_16x16x32_bf16 v[92:95], v[156:159], v[44:47], v[92:95]\n"
    "s_waitcnt lgkmcnt(7)\n"
    "v_mfma_f32_16x16x32_bf16 v[96:99], v[152:155], v[48:51], v[96:99]\n"
    "ds_read_b128 v[164:167], v17 offset:33792\n"
    "ds_read_b64_tr_b16 v[36:37], v23 offset:48160\n"
    "ds_read_b64_tr_b16 v[38:39], v23 offset:48416\n"
    "v_mfma_f32_16x16x32_bf16 v[100:103], v[156:159], v[48:51], v[100:103]\n"
    "v_mfma_f32_16x16x32_bf16 v[104:107], v[152:155], v[52:55], v[104:107]\n"
    "ds_read_b64_tr_b16 v[40:41], v23 offset:48192\n"
    "ds_read_b64_tr_b16 v[42:43], v23 offset:48448\n"
    "v_mfma_f32_16x16x32_bf16 v[108:111], v[156:159], v[52:55], v[108:111]\n"
    "s_waitcnt lgkmcnt(8)\n"
    "v_mfma_f32_16x16x32_bf16 v[112:115], v[152:155], v[56:59], v[112:115]\n"
    "ds_read_b64_tr_b16 v[44:45], v23 offset:48224\n"
    "ds_read_b64_tr_b16 v[46:47], v23 offset:48480\n"
    "v_mfma_f32_16x16x32_bf16 v[116:119], v[156:159], v[56:59], v[116:119]\n"
    "v_mfma_f32_16x16x32_bf16 v[120:123], v[152:155], v[60:63], v[120:123]\n"
    "ds_read_b64_tr_b16 v[48:49], v23 offset:52352\n"
    "ds_read_b64_tr_b16 v[50:51], v23 offset:52608\n"
    "v_mfma_f32_16x16x32_bf16 v[124:127], v[156:159], v[60:63], v[124:127]\n"
    "s_waitcnt lgkmcnt(6)\n"
    "v_mfma_f32_16x16x32_bf16 v[64:67], v[160:163], v[32:35], v[64:67]\n"
    "ds_read_b64_tr_b16 v[52:53], v23 offset:52384\n"
    "ds_read_b64_tr_b16 v[54:55], v23 offset:52640\n"
    "v_mfma_f32_16x16x32_bf16 v[68:71], v[164:167], v[32:35], v[68:71]\n"
    "v_mfma_f32_16x16x32_bf16 v[72:75], v[160:163], v[36:39], v[72:75]\n"
    "ds_read_b64_tr_b16 v[56:57], v23 offset:52416\n"
    "ds_read_b64_tr_b16 v[58:59], v23 offset:52672\n"
    "v_mfma_f32_16x16x32_bf16 v[76:79], v[164:167], v[36:39], v[76:79]\n"
    "s_waitcnt lgkmcnt(6)\n"
    "v_mfma_f32_16x16x32_bf16 v[80:83], v[160:163], v[40:43], v[80:83]\n"
    "ds_read_b64_tr_b16 v[60:61], v23 offset:52448\n"
    "ds_read_b64_tr_b16 v[62:63], v23 offset:52704\n"
    "v_mfma_f32_16x16x32_bf16 v[84:87], v[164:167], v[40:43], v[84:87]\n"
    "v_mfma_f32_16x16x32_bf16 v[88:91], v[160:163], v[44:47], v[88:91]\n"
    "ds_read_b128 v[168:171], v20 offset:31744\n"
    "ds_read_b64_tr_b16 v[32:33], v23 offset:56576\n"
    "ds_read_b64_tr_b16 v[34:35], v23 offset:56832\n"
    "v_mfma_f32_16x16x32_bf16 v[92:95], v[164:167], v[44:47], v[92:95]\n"
    "s_waitcnt lgkmcnt(7)\n"
    "v_mfma_f32_16x16x32_bf16 v[96:99], v[160:163], v[48:51], v[96:99]\n"
    "ds_read_b128 v[172:175], v20 offset:33792\n"
    "ds_read_b64_tr_b16 v[36:37], v23 offset:56608\n"
    "ds_read_b64_tr_b16 v[38:39], v23 offset:56864\n"
    "v_mfma_f32_16x16x32_bf16 v[100:103], v[164:167], v[48:51], v[100:103]\n"
    "v_mfma_f32_16x16x32_bf16 v[104:107], v[160:163], v[52:55], v[104:107]\n"
    "ds_read_b64_tr_b16 v[40:41], v23 offset:56640\n"
    "ds_read_b64_tr_b16 v[42:43], v23 offset:56896\n"
    "v_mfma_f32_16x16x32_bf16 v[108:111], v[164:167], v[52:55], v[108:111]\n"
    "s_waitcnt lgkmcnt(8)\n"
    "v_mfma_f32_16x16x32_bf16 v[112:115], v[160:163], v[56:59], v[112:115]\n"
    "ds_read_b64_tr_b16 v[44:45], v23 offset:56672\n"
    "ds_read_b64_tr_b16 v[46:47], v23 offset:56928\n"
    "v_mfma_f32_16x16x32_bf16 v[116:119], v[164:167], v[56:59], v[116:119]\n"
    "v_mfma_f32_16x16x32_bf16 v[120:123], v[160:163], v[60:63], v[120:123]\n"
    "ds_read_b64_tr_b16 v[48:49], v23 offset:60800\n"
    "ds_read_b64_tr_b16 v[50:51], v23 offset:61056\n"
    "v_mfma_f32_16x16x32_bf16 v[124:127], v[164:167], v[60:63], v[124:127]\n"
    "s_waitcnt lgkmcnt(6)\n"
    "v_mfma_f32_16x16x32_bf16 v[64:67], v[168:171], v[32:35], v[64:67]\n"
    "ds_read_b64_tr_b16 v[52:53], v23 offset:60832\n"
    "ds_read_b64_tr_b16 v[54:55], v23 offset:61088\n"
    "v_mfma_f32_16x16x32_bf16 v[68:71], v[172:175], v[32:35], v[68:71]\n"
    "v_mfma_f32_16x16x32_bf16 v[72:75], v[168:171], v[36:39], v[72:75]\n"
    "ds_read_b64_tr_b16 v[56:57], v23 offset:60864\n"
    "ds_read_b64_tr_b16 v[58:59], v23 offset:61120\n"
    "v_mfma_f32_16x16x32_bf16 v[76:79], v[172:175], v[36:39], v[76:79]\n"
    "s_waitcnt lgkmcnt(6)\n"
    "v_mfma_f32_16x16x32_bf16 v[80:83], v[168:171], v[40:43], v[80:83]\n"
    "ds_read_b64_tr_b16 v[60:61], v23 offset:60896\n"
    "ds_read_b64_tr_b16 v[62:63], v23 offset:61152\n"
    "v_mfma_f32_16x16x32_bf16 v[84:87], v[172:175], v[40:43], v[84:87]\n"
    "s_barrier\n"
    "v_mfma_f32_16x16x32_bf16 v[88:91], v[168:171], v[44:47], v[88:91]\n"
    "ds_read_b128 v[176:179], v17 offset:65024\n"
    "ds_read_b64_tr_b16 v[32:33], v24 offset:15872\n"
    "ds_read_b64_tr_b16 v[34:35], v24 offset:16128\n"
    "v_mfma_f32_16x16x32_bf16 v[92:95], v[172:175], v[44:47], v[92:95]\n"
    "s_waitcnt lgkmcnt(7)\n"
    "v_mfma_f32_16x16x32_bf16 v[96:99], v[168:171], v[48:51], v[96:99]\n"
    "ds_read_b128 v[180:183], v18 offset:1536\n"
    "ds_read_b64_tr_b16 v[36:37], v24 offset:15904\n"
    "ds_read_b64_tr_b16 v[38:39], v24 offset:16160\n"
    "v_mfma_f32_16x16x32_bf16 v[100:103], v[172:175], v[48:51], v[100:103]\n"
    "v_mfma_f32_16x16x32_bf16 v[104:107], v[168:171], v[52:55], v[104:107]\n"
    "ds_read_b64_tr_b16 v[40:41], v24 offset:15936\n"
    "ds_read_b64_tr_b16 v[42:43], v24 offset:16192\n"
    "v_mfma_f32_16x16x32_bf16 v[108:111], v[172:175], v[52:55], v[108:111]\n"
    "s_waitcnt lgkmcnt(8)\n"
    "v_mfma_f32_16x16x32_bf16 v[112:115], v[168:171], v[56:59], v[112:115]\n"
    "ds_read_b64_tr_b16 v[44:45], v24 offset:15968\n"
    "ds_read_b64_tr_b16 v[46:47], v24 offset:16224\n"
    "v_mfma_f32_16x16x32_bf16 v[116:119], v[172:175], v[56:59], v[116:119]\n"
    "v_mfma_f32_16x16x32_bf16 v[120:123], v[168:171], v[60:63], v[120:123]\n"
    "ds_read_b64_tr_b16 v[48:49], v24 offset:20096\n"
    "ds_read_b64_tr_b16 v[50:51], v24 offset:20352\n"
    "v_mfma_f32_16x16x32_bf16 v[124:127], v[172:175], v[60:63], v[124:127]\n"
    "s_waitcnt lgkmcnt(6)\n"
    "v_mfma_f32_16x16x32_bf16 v[64:67], v[176:179], v[32:35], v[64:67]\n"
    "ds_read_b64_tr_b16 v[52:53], v24 offset:20128\n"
    "ds_read_b64_tr_b16 v[54:55], v24 offset:20384\n"
    "v_mfma_f32_16x16x32_bf16 v[68:71], v[180:183], v[32:35], v[68:71]\n"
    "v_mfma_f32_16x16x32_bf16 v[72:75], v[176:179], v[36:39], v[72:75]\n"
    "ds_read_b64_tr_b16 v[56:57], v24 offset:20160\n"
    "ds_read_b64_tr_b16 v[58:59], v24 offset:20416\n"
    "v_mfma_f32_16x16x32_bf16 v[76:79], v[180:183], v[36:39], v[76:79]\n"
    "s_waitcnt lgkmcnt(6)\n"
    "v_mfma_f32_16x16x32_bf16 v[80:83], v[176:179], v[40:43], v[80:83]\n"
    "ds_read_b64_tr_b16 v[60:61], v24 offset:20192\n"
    "ds_read_b64_tr_b16 v[62:63], v24 offset:20448\n"
    "v_mfma_f32_16x16x32_bf16 v[84:87], v[180:183], v[40:43], v[84:87]\n"
    "v_mfma_f32_16x16x32_bf16 v[88:91], v[176:179], v[44:47], v[88:91]\n"
    "ds_read_b128 v[184:187], v20 offset:65024\n"
    "ds_read_b64_tr_b16 v[32:33], v24 offset:24320\n"
    "ds_read_b64_tr_b16 v[34:35], v24 offset:24576\n"
    "v_mfma_f32_16x16x32_bf16 v[92:95], v[180:183], v[44:47], v[92:95]\n"
    "s_waitcnt lgkmcnt(7)\n"
    "v_mfma_f32_16x16x32_bf16 v[96:99], v[176:179], v[48:51], v[96:99]\n"
    "ds_read_b128 v[188:191], v21 offset:1536\n"
    "ds_read_b64_tr_b16 v[36:37], v24 offset:24352\n"
    "ds_read_b64_tr_b16 v[38:39], v24 offset:24608\n"
    "v_mfma_f32_16x16x32_bf16 v[100:103], v[180:183], v[48:51], v[100:103]\n"
    "v_mfma_f32_16x16x32_bf16 v[104:107], v[176:179], v[52:55], v[104:107]\n"
    "ds_read_b64_tr_b16 v[40:41], v24 offset:24384\n"
    "ds_read_b64_tr_b16 v[42:43], v24 offset:24640\n"
    "v_mfma_f32_16x16x32_bf16 v[108:111], v[180:183], v[52:55], v[108:111]\n"
    "s_waitcnt lgkmcnt(8)\n"
    "v_mfma_f32_16x16x32_bf16 v[112:115], v[176:179], v[56:59], v[112:115]\n"
    "ds_read_b64_tr_b16 v[44:45], v24 offset:24416\n"
    "ds_read_b64_tr_b16 v[46:47], v24 offset:24672\n"
    "v_mfma_f32_16x16x32_bf16 v[116:119], v[180:183], v[56:59], v[116:119]\n"
    "v_mfma_f32_16x16x32_bf16 v[120:123], v[176:179], v[60:63], v[120:123]\n"
    "ds_read_b64_tr_b16 v[48:49], v24 offset:28544\n"
    "ds_read_b64_tr_b16 v[50:51], v24 offset:28800\n"
    "v_mfma_f32_16x16x32_bf16 v[124:127], v[180:183], v[60:63], v[124:127]\n"
    "s_waitcnt lgkmcnt(6)\n"
    "v_mfma_f32_16x16x32_bf16 v[64:67], v[184:187], v[32:35], v[64:67]\n"
    "ds_read_b64_tr_b16 v[52:53], v24 offset:28576\n"
    "ds_read_b64_tr_b16 v[54:55], v24 offset:28832\n"
    "v_mfma_f32_16x16x32_bf16 v[68:71], v[188:191], v[32:35], v[68:71]\n"
    "v_mfma_f32_16x16x32_bf16 v[72:75], v[184:187], v[36:39], v[72:75]\n"
    "ds_read_b64_tr_b16 v[56:57], v24 offset:28608\n"
    "ds_read_b64_tr_b16 v[58:59], v24 offset:28864\n"
    "v_mfma_f32_16x16x32_bf16 v[76:79], v[188:191], v[36:39], v[76:79]\n"
    "s_waitcnt lgkmcnt(6)\n"
    "v_mfma_f32_16x16x32_bf16 v[80:83], v[184:187], v[40:43], v[80:83]\n"
    "ds_read_b64_tr_b16 v[60:61], v24 offset:28640\n"
    "ds_read_b64_tr_b16 v[62:63], v24 offset:28896\n"
    "v_mfma_f32_16x16x32_bf16 v[84:87], v[188:191], v[40:43], v[84:87]\n"
    "s_barrier\n"
    "v_mfma_f32_16x16x32_bf16 v[88:91], v[184:187], v[44:47], v[88:91]\n"
    "ds_read_b128 v[192:195], v16 offset:30720\n"
    "ds_read_b64_tr_b16 v[32:33], v22 offset:47104\n"
    "ds_read_b64_tr_b16 v[34:35], v22 offset:47360\n"
    "v_mfma_f32_16x16x32_bf16 v[92:95], v[188:191], v[44:47], v[92:95]\n"
    "s_waitcnt lgkmcnt(7)\n"
    "v_mfma_f32_16x16x32_bf16 v[96:99], v[184:187], v[48:51], v[96:99]\n"
    "ds_read_b128 v[196:199], v16 offset:32768\n"
    "ds_read_b64_tr_b16 v[36:37], v22 offset:47136\n"
    "ds_read_b64_tr_b16 v[38:39], v22 offset:47392\n"
    "v_mfma_f32_16x16x32_bf16 v[100:103], v[188:191], v[48:51], v[100:103]\n"
    "v_mfma_f32_16x16x32_bf16 v[104:107], v[184:187], v[52:55], v[104:107]\n"
    "ds_read_b64_tr_b16 v[40:41], v22 offset:47168\n"
    "ds_read_b64_tr_b16 v[42:43], v22 offset:47424\n"
    "v_mfma_f32_16x16x32_bf16 v[108:111], v[188:191], v[52:55], v[108:111]\n"
    "s_waitcnt lgkmcnt(8)\n"
    "v_mfma_f32_16x16x32_bf16 v[112:115], v[184:187], v[56:59], v[112:115]\n"
    "ds_read_b64_tr_b16 v[44:45], v22 offset:47200\n"
    "ds_read_b64_tr_b16 v[46:47], v22 offset:47456\n"
    "v_mfma_f32_16x16x32_bf16 v[116:119], v[188:191], v[56:59], v[116:119]\n"
    "v_mfma_f32_16x16x32_bf16 v[120:123], v[184:187], v[60:63], v[120:123]\n"
    "ds_read_b64_tr_b16 v[48:49], v22 offset:51328\n"
    "ds_read_b64_tr_b16 v[50:51], v22 offset:51584\n"
    "v_mfma_f32_16x16x32_bf16 v[124:127], v[188:191], v[60:63], v[124:127]\n"
    "s_waitcnt lgkmcnt(6)\n"
    "v_mfma_f32_16x16x32_bf16 v[64:67], v[192:195], v[32:35], v[64:67]\n"
    "ds_read_b64_tr_b16 v[52:53], v22 offset:51360\n"
    "ds_read_b64_tr_b16 v[54:55], v22 offset:51616\n"
    "v_mfma_f32_16x16x32_bf16 v[68:71], v[196:199], v[32:35], v[68:71]\n"
    "v_mfma_f32_16x16x32_bf16 v[72:75], v[192:195], v[36:39], v[72:75]\n"
    "ds_read_b64_tr_b16 v[56:57], v22 offset:51392\n"
    "ds_read_b64_tr_b16 v[58:59], v22 offset:51648\n"
    "v_mfma_f32_16x16x32_bf16 v[76:79], v[196:199], v[36:39], v[76:79]\n"
    "s_waitcnt lgkmcnt(6)\n"
    "v_mfma_f32_16x16x32_bf16 v[80:83], v[192:195], v[40:43], v[80:83]\n"
    "ds_read_b64_tr_b16 v[60:61], v22 offset:51424\n"
    "ds_read_b64_tr_b16 v[62:63], v22 offset:51680\n"
    "v_mfma_f32_16x16x32_bf16 v[84:87], v[196:199], v[40:43], v[84:87]\n"
    "v_mfma_f32_16x16x32_bf16 v[88:91], v[192:195], v[44:47], v[88:91]\n"
    "ds_read_b128 v[200:203], v19 offset:30720\n"
    "ds_read_b64_tr_b16 v[32:33], v22 offset:55552\n"
    "ds_read_b64_tr_b16 v[34:35], v22 offset:55808\n"
    "v_mfma_f32_16x16x32_bf16 v[92:95], v[196:199], v[44:47], v[92:95]\n"
    "s_waitcnt lgkmcnt(7)\n"
    "v_mfma_f32_16x16x32_bf16 v[96:99], v[192:195], v[48:51], v[96:99]\n"
    "ds_read_b128 v[204:207], v19 offset:32768\n"
    "ds_read_b64_tr_b16 v[36:37], v22 offset:55584\n"
    "ds_read_b64_tr_b16 v[38:39], v22 offset:55840\n"
    "v_mfma_f32_16x16x32_bf16 v[100:103], v[196:199], v[48:51], v[100:103]\n"
    "v_mfma_f32_16x16x32_bf16 v[104:107], v[192:195], v[52:55], v[104:107]\n"
    "ds_read_b64_tr_b16 v[40:41], v22 offset:55616\n"
    "ds_read_b64_tr_b16 v[42:43], v22 offset:55872\n"
    "v_mfma_f32_16x16x32_bf16 v[108:111], v[196:199], v[52:55], v[108:111]\n"
    "s_waitcnt lgkmcnt(8)\n"
    "v_mfma_f32_16x16x32_bf16 v[112:115], v[192:195], v[56:59], v[112:115]\n"
    "ds_read_b64_tr_b16 v[44:45], v22 offset:55648\n"
    "ds_read_b64_tr_b16 v[46:47], v22 offset:55904\n"
    "v_mfma_f32_16x16x32_bf16 v[116:119], v[196:199], v[56:59], v[116:119]\n"
    "v_mfma_f32_16x16x32_bf16 v[120:123], v[192:195], v[60:63], v[120:123]\n"
    "ds_read_b64_tr_b16 v[48:49], v22 offset:59776\n"
    "ds_read_b64_tr_b16 v[50:51], v22 offset:60032\n"
    "v_mfma_f32_16x16x32_bf16 v[124:127], v[196:199], v[60:63], v[124:127]\n"
    "s_waitcnt lgkmcnt(6)\n"
    "v_mfma_f32_16x16x32_bf16 v[64:67], v[200:203], v[32:35], v[64:67]\n"
    "ds_read_b64_tr_b16 v[52:53], v22 offset:59808\n"
    "ds_read_b64_tr_b16 v[54:55], v22 offset:60064\n"
    "v_mfma_f32_16x16x32_bf16 v[68:71], v[204:207], v[32:35], v[68:71]\n"
    "v_mfma_f32_16x16x32_bf16 v[72:75], v[200:203], v[36:39], v[72:75]\n"
    "ds_read_b64_tr_b16 v[56:57], v22 offset:59840\n"
    "ds_read_b64_tr_b16 v[58:59], v22 offset:60096\n"
    "v_mfma_f32_16x16x32_bf16 v[76:79], v[204:207], v[36:39], v[76:79]\n"
    "s_waitcnt lgkmcnt(6)\n"
    "v_mfma_f32_16x16x32_bf16 v[80:83], v[200:203], v[40:43], v[80:83]\n"
    "ds_read_b64_tr_b16 v[60:61], v22 offset:59872\n"
    "ds_read_b64_tr_b16 v[62:63], v22 offset:60128\n"
    "v_mfma_f32_16x16x32_bf16 v[84:87], v[204:207], v[40:43], v[84:87]\n"
    "s_barrier\n"
    "v_mfma_f32_16x16x32_bf16 v[88:91], v[200:203], v[44:47], v[88:91]\n"
    "ds_read_b128 v[208:211], v16 offset:64000\n"
    "ds_read_b64_tr_b16 v[32:33], v23 offset:14848\n"
    "ds_read_b64_tr_b16 v[34:35], v23 offset:15104\n"
    "v_mfma_f32_16x16x32_bf16 v[92:95], v[204:207], v[44:47], v[92:95]\n"
    "s_waitcnt lgkmcnt(7)\n"
    "v_mfma_f32_16x16x32_bf16 v[96:99], v[200:203], v[48:51], v[96:99]\n"
    "ds_read_b128 v[212:215], v17 offset:512\n"
    "ds_read_b64_tr_b16 v[36:37], v23 offset:14880\n"
    "ds_read_b64_tr_b16 v[38:39], v23 offset:15136\n"
    "v_mfma_f32_16x16x32_bf16 v[100:103], v[204:207], v[48:51], v[100:103]\n"
    "v_mfma_f32_16x16x32_bf16 v[104:107], v[200:203], v[52:55], v[104:107]\n"
    "ds_read_b64_tr_b16 v[40:41], v23 offset:14912\n"
    "ds_read_b64_tr_b16 v[42:43], v23 offset:15168\n"
    "v_mfma_f32_16x16x32_bf16 v[108:111], v[204:207], v[52:55], v[108:111]\n"
    "s_waitcnt lgkmcnt(8)\n"
    "v_mfma_f32_16x16x32_bf16 v[112:115], v[200:203], v[56:59], v[112:115]\n"
    "ds_read_b64_tr_b16 v[44:45], v23 offset:14944\n"
    "ds_read_b64_tr_b16 v[46:47], v23 offset:15200\n"
    "v_mfma_f32_16x16x32_bf16 v[116:119], v[204:207], v[56:59], v[116:119]\n"
    "v_mfma_f32_16x16x32_bf16 v[120:123], v[200:203], v[60:63], v[120:123]\n"
    "ds_read_b64_tr_b16 v[48:49], v23 offset:19072\n"
    "ds_read_b64_tr_b16 v[50:51], v23 offset:19328\n"
    "v_mfma_f32_16x16x32_bf16 v[124:127], v[204:207], v[60:63], v[124:127]\n"
    "s_waitcnt lgkmcnt(6)\n"
    "v_mfma_f32_16x16x32_bf16 v[64:67], v[208:211], v[32:35], v[64:67]\n"
    "ds_read_b64_tr_b16 v[52:53], v23 offset:19104\n"
    "ds_read_b64_tr_b16 v[54:55], v23 offset:19360\n"
    "v_mfma_f32_16x16x32_bf16 v[68:71], v[212:215], v[32:35], v[68:71]\n"
    "v_mfma_f32_16x16x32_bf16 v[72:75], v[208:211], v[36:39], v[72:75]\n"
    "ds_read_b64_tr_b16 v[56:57], v23 offset:19136\n"
    "ds_read_b64_tr_b16 v[58:59], v23 offset:19392\n"
    "v_mfma_f32_16x16x32_bf16 v[76:79], v[212:215], v[36:39], v[76:79]\n"
    "s_waitcnt lgkmcnt(6)\n"
    "v_mfma_f32_16x16x32_bf16 v[80:83], v[208:211], v[40:43], v[80:83]\n"
    "ds_read_b64_tr_b16 v[60:61], v23 offset:19168\n"
    "ds_read_b64_tr_b16 v[62:63], v23 offset:19424\n"
    "v_mfma_f32_16x16x32_bf16 v[84:87], v[212:215], v[40:43], v[84:87]\n"
    "v_mfma_f32_16x16x32_bf16 v[88:91], v[208:211], v[44:47], v[88:91]\n"
    "ds_read_b128 v[216:219], v19 offset:64000\n"
    "ds_read_b64_tr_b16 v[32:33], v23 offset:23296\n"
    "ds_read_b64_tr_b16 v[34:35], v23 offset:23552\n"
    "v_mfma_f32_16x16x32_bf16 v[92:95], v[212:215], v[44:47], v[92:95]\n"
    "s_waitcnt lgkmcnt(7)\n"
    "v_mfma_f32_16x16x32_bf16 v[96:99], v[208:211], v[48:51], v[96:99]\n"
    "ds_read_b128 v[220:223], v20 offset:512\n"
    "ds_read_b64_tr_b16 v[36:37], v23 offset:23328\n"
    "ds_read_b64_tr_b16 v[38:39], v23 offset:23584\n"
    "v_mfma_f32_16x16x32_bf16 v[100:103], v[212:215], v[48:51], v[100:103]\n"
    "v_mfma_f32_16x16x32_bf16 v[104:107], v[208:211], v[52:55], v[104:107]\n"
    "ds_read_b64_tr_b16 v[40:41], v23 offset:23360\n"
    "ds_read_b64_tr_b16 v[42:43], v23 offset:23616\n"
    "v_mfma_f32_16x16x32_bf16 v[108:111], v[212:215], v[52:55], v[108:111]\n"
    "s_waitcnt lgkmcnt(8)\n"
    "v_mfma_f32_16x16x32_bf16 v[112:115], v[208:211], v[56:59], v[112:115]\n"
    "ds_read_b64_tr_b16 v[44:45], v23 offset:23392\n"
    "ds_read_b64_tr_b16 v[46:47], v23 offset:23648\n"
    "v_mfma_f32_16x16x32_bf16 v[116:119], v[212:215], v[56:59], v[116:119]\n"
    "v_mfma_f32_16x16x32_bf16 v[120:123], v[208:211], v[60:63], v[120:123]\n"
    "ds_read_b64_tr_b16 v[48:49], v23 offset:27520\n"
    "ds_read_b64_tr_b16 v[50:51], v23 offset:27776\n"
    "v_mfma_f32_16x16x32_bf16 v[124:127], v[212:215], v[60:63], v[124:127]\n"
    "s_waitcnt lgkmcnt(6)\n"
    "v_mfma_f32_16x16x32_bf16 v[64:67], v[216:219], v[32:35], v[64:67]\n"
    "ds_read_b64_tr_b16 v[52:53], v23 offset:27552\n"
    "ds_read_b64_tr_b16 v[54:55], v23 offset:27808\n"
    "v_mfma_f32_16x16x32_bf16 v[68:71], v[220:223], v[32:35], v[68:71]\n"
    "v_mfma_f32_16x16x32_bf16 v[72:75], v[216:219], v[36:39], v[72:75]\n"
    "ds_read_b64_tr_b16 v[56:57], v23 offset:27584\n"
    "ds_read_b64_tr_b16 v[58:59], v23 offset:27840\n"
    "v_mfma_f32_16x16x32_bf16 v[76:79], v[220:223], v[36:39], v[76:79]\n"
    "s_waitcnt lgkmcnt(6)\n"
    "v_mfma_f32_16x16x32_bf16 v[80:83], v[216:219], v[40:43], v[80:83]\n"
    "ds_read_b64_tr_b16 v[60:61], v23 offset:27616\n"
    "ds_read_b64_tr_b16 v[62:63], v23 offset:27872\n"
    "v_mfma_f32_16x16x32_bf16 v[84:87], v[220:223], v[40:43], v[84:87]\n"
    "s_barrier\n"
    "v_mfma_f32_16x16x32_bf16 v[88:91], v[216:219], v[44:47], v[88:91]\n"
    "ds_read_b128 v[224:227], v17 offset:31744\n"
    "ds_read_b64_tr_b16 v[32:33], v23 offset:48128\n"
    "ds_read_b64_tr_b16 v[34:35], v23 offset:48384\n"
    "v_mfma_f32_16x16x32_bf16 v[92:95], v[220:223], v[44:47], v[92:95]\n"
    "s_waitcnt lgkmcnt(7)\n"
    "v_mfma_f32_16x16x32_bf16 v[96:99], v[216:219], v[48:51], v[96:99]\n"
    "ds_read_b128 v[228:231], v17 offset:33792\n"
    "ds_read_b64_tr_b16 v[36:37], v23 offset:48160\n"
    "ds_read_b64_tr_b16 v[38:39], v23 offset:48416\n"
    "v_mfma_f32_16x16x32_bf16 v[100:103], v[220:223], v[48:51], v[100:103]\n"
    "v_mfma_f32_16x16x32_bf16 v[104:107], v[216:219], v[52:55], v[104:107]\n"
    "ds_read_b64_tr_b16 v[40:41], v23 offset:48192\n"
    "ds_read_b64_tr_b16 v[42:43], v23 offset:48448\n"
    "v_mfma_f32_16x16x32_bf16 v[108:111], v[220:223], v[52:55], v[108:111]\n"
    "s_waitcnt lgkmcnt(8)\n"
    "v_mfma_f32_16x16x32_bf16 v[112:115], v[216:219], v[56:59], v[112:115]\n"
    "ds_read_b64_tr_b16 v[44:45], v23 offset:48224\n"
    "ds_read_b64_tr_b16 v[46:47], v23 offset:48480\n"
    "v_mfma_f32_16x16x32_bf16 v[116:119], v[220:223], v[56:59], v[116:119]\n"
    "v_mfma_f32_16x16x32_bf16 v[120:123], v[216:219], v[60:63], v[120:123]\n"
    "ds_read_b64_tr_b16 v[48:49], v23 offset:52352\n"
    "ds_read_b64_tr_b16 v[50:51], v23 offset:52608\n"
    "v_mfma_f32_16x16x32_bf16 v[124:127], v[220:223], v[60:63], v[124:127]\n"
    "s_waitcnt lgkmcnt(6)\n"
    "v_mfma_f32_16x16x32_bf16 v[64:67], v[224:227], v[32:35], v[64:67]\n"
    "ds_read_b64_tr_b16 v[52:53], v23 offset:52384\n"
    "ds_read_b64_tr_b16 v[54:55], v23 offset:52640\n"
    "v_mfma_f32_16x16x32_bf16 v[68:71], v[228:231], v[32:35], v[68:71]\n"
    "v_mfma_f32_16x16x32_bf16 v[72:75], v[224:227], v[36:39], v[72:75]\n"
    "ds_read_b64_tr_b16 v[56:57], v23 offset:52416\n"
    "ds_read_b64_tr_b16 v[58:59], v23 offset:52672\n"
    "v_mfma_f32_16x16x32_bf16 v[76:79], v[228:231], v[36:39], v[76:79]\n"
    "s_waitcnt lgkmcnt(6)\n"
    "v_mfma_f32_16x16x32_bf16 v[80:83], v[224:227], v[40:43], v[80:83]\n"
    "ds_read_b64_tr_b16 v[60:61], v23 offset:52448\n"
    "ds_read_b64_tr_b16 v[62:63], v23 offset:52704\n"
    "v_mfma_f32_16x16x32_bf16 v[84:87], v[228:231], v[40:43], v[84:87]\n"
    "v_mfma_f32_16x16x32_bf16 v[88:91], v[224:227], v[44:47], v[88:91]\n"
    "ds_read_b128 v[232:235], v20 offset:31744\n"
    "ds_read_b64_tr_b16 v[32:33], v23 offset:56576\n"
    "ds_read_b64_tr_b16 v[34:35], v23 offset:56832\n"
    "v_mfma_f32_16x16x32_bf16 v[92:95], v[228:231], v[44:47], v[92:95]\n"
    "s_waitcnt lgkmcnt(7)\n"
    "v_mfma_f32_16x16x32_bf16 v[96:99], v[224:227], v[48:51], v[96:99]\n"
    "ds_read_b128 v[236:239], v20 offset:33792\n"
    "ds_read_b64_tr_b16 v[36:37], v23 offset:56608\n"
    "ds_read_b64_tr_b16 v[38:39], v23 offset:56864\n"
    "v_mfma_f32_16x16x32_bf16 v[100:103], v[228:231], v[48:51], v[100:103]\n"
    "v_mfma_f32_16x16x32_bf16 v[104:107], v[224:227], v[52:55], v[104:107]\n"
    "ds_read_b64_tr_b16 v[40:41], v23 offset:56640\n"
    "ds_read_b64_tr_b16 v[42:43], v23 offset:56896\n"
    "v_mfma_f32_16x16x32_bf16 v[108:111], v[228:231], v[52:55], v[108:111]\n"
    "s_waitcnt lgkmcnt(8)\n"
    "v_mfma_f32_16x16x32_bf16 v[112:115], v[224:227], v[56:59], v[112:115]\n"
    "ds_read_b64_tr_b16 v[44:45], v23 offset:56672\n"
    "ds_read_b64_tr_b16 v[46:47], v23 offset:56928\n"
    "v_mfma_f32_16x16x32_bf16 v[116:119], v[228:231], v[56:59], v[116:119]\n"
    "v_mfma_f32_16x16x32_bf16 v[120:123], v[224:227], v[60:63], v[120:123]\n"
    "ds_read_b64_tr_b16 v[48:49], v23 offset:60800\n"
    "ds_read_b64_tr_b16 v[50:51], v23 offset:61056\n"
    "v_mfma_f32_16x16x32_bf16 v[124:127], v[228:231], v[60:63], v[124:127]\n"
    "s_waitcnt lgkmcnt(6)\n"
    "v_mfma_f32_16x16x32_bf16 v[64:67], v[232:235], v[32:35], v[64:67]\n"
    "ds_read_b64_tr_b16 v[52:53], v23 offset:60832\n"
    "ds_read_b64_tr_b16 v[54:55], v23 offset:61088\n"
    "v_mfma_f32_16x16x32_bf16 v[68:71], v[236:239], v[32:35], v[68:71]\n"
    "v_mfma_f32_16x16x32_bf16 v[72:75], v[232:235], v[36:39], v[72:75]\n"
    "ds_read_b64_tr_b16 v[56:57], v23 offset:60864\n"
    "ds_read_b64_tr_b16 v[58:59], v23 offset:61120\n"
    "v_mfma_f32_16x16x32_bf16 v[76:79], v[236:239], v[36:39], v[76:79]\n"
    "s_waitcnt lgkmcnt(6)\n"
    "v_mfma_f32_16x16x32_bf16 v[80:83], v[232:235], v[40:43], v[80:83]\n"
    "ds_read_b64_tr_b16 v[60:61], v23 offset:60896\n"
    "ds_read_b64_tr_b16 v[62:63], v23 offset:61152\n"
    "v_mfma_f32_16x16x32_bf16 v[84:87], v[236:239], v[40:43], v[84:87]\n"
    "s_barrier\n"
    "v_mfma_f32_16x16x32_bf16 v[88:91], v[232:235], v[44:47], v[88:91]\n"
    "ds_read_b128 v[240:243], v17 offset:65024\n"
    "ds_read_b64_tr_b16 v[32:33], v24 offset:15872\n"
    "ds_read_b64_tr_b16 v[34:35], v24 offset:16128\n"
    "v_mfma_f32_16x16x32_bf16 v[92:95], v[236:239], v[44:47], v[92:95]\n"
    "s_waitcnt lgkmcnt(7)\n"
    "v_mfma_f32_16x16x32_bf16 v[96:99], v[232:235], v[48:51], v[96:99]\n"
    "ds_read_b128 v[244:247], v18 offset:1536\n"
    "ds_read_b64_tr_b16 v[36:37], v24 offset:15904\n"
    "ds_read_b64_tr_b16 v[38:39], v24 offset:16160\n"
    "v_mfma_f32_16x16x32_bf16 v[100:103], v[236:239], v[48:51], v[100:103]\n"
    "v_mfma_f32_16x16x32_bf16 v[104:107], v[232:235], v[52:55], v[104:107]\n"
    "ds_read_b64_tr_b16 v[40:41], v24 offset:15936\n"
    "ds_read_b64_tr_b16 v[42:43], v24 offset:16192\n"
    "v_mfma_f32_16x16x32_bf16 v[108:111], v[236:239], v[52:55], v[108:111]\n"
    "s_waitcnt lgkmcnt(8)\n"
    "v_mfma_f32_16x16x32_bf16 v[112:115], v[232:235], v[56:59], v[112:115]\n"
    "ds_read_b64_tr_b16 v[44:45], v24 offset:15968\n"
    "ds_read_b64_tr_b16 v[46:47], v24 offset:16224\n"
    "v_mfma_f32_16x16x32_bf16 v[116:119], v[236:239], v[56:59], v[116:119]\n"
    "v_mfma_f32_16x16x32_bf16 v[120:123], v[232:235], v[60:63], v[120:123]\n"
    "ds_read_b64_tr_b16 v[48:49], v24 offset:20096\n"
    "ds_read_b64_tr_b16 v[50:51], v24 offset:20352\n"
    "v_mfma_f32_16x16x32_bf16 v[124:127], v[236:239], v[60:63], v[124:127]\n"
    "s_waitcnt lgkmcnt(6)\n"
    "v_mfma_f32_16x16x32_bf16 v[64:67], v[240:243], v[32:35], v[64:67]\n"
    "ds_read_b64_tr_b16 v[52:53], v24 offset:20128\n"
    "ds_read_b64_tr_b16 v[54:55], v24 offset:20384\n"
    "v_mfma_f32_16x16x32_bf16 v[68:71], v[244:247], v[32:35], v[68:71]\n"
    "v_mfma_f32_16x16x32_bf16 v[72:75], v[240:243], v[36:39], v[72:75]\n"
    "ds_read_b64_tr_b16 v[56:57], v24 offset:20160\n"
    "ds_read_b64_tr_b16 v[58:59], v24 offset:20416\n"
    "v_mfma_f32_16x16x32_bf16 v[76:79], v[244:247], v[36:39], v[76:79]\n"
    "s_waitcnt lgkmcnt(6)\n"
    "v_mfma_f32_16x16x32_bf16 v[80:83], v[240:243], v[40:43], v[80:83]\n"
    "ds_read_b64_tr_b16 v[60:61], v24 offset:20192\n"
    "ds_read_b64_tr_b16 v[62:63], v24 offset:20448\n"
    "v_mfma_f32_16x16x32_bf16 v[84:87], v[244:247], v[40:43], v[84:87]\n"
    "v_mfma_f32_16x16x32_bf16 v[88:91], v[240:243], v[44:47], v[88:91]\n"
    "ds_read_b128 v[248:251], v20 offset:65024\n"
    "ds_read_b64_tr_b16 v[32:33], v24 offset:24320\n"
    "ds_read_b64_tr_b16 v[34:35], v24 offset:24576\n"
    "v_mfma_f32_16x16x32_bf16 v[92:95], v[244:247], v[44:47], v[92:95]\n"
    "s_waitcnt lgkmcnt(7)\n"
    "v_mfma_f32_16x16x32_bf16 v[96:99], v[240:243], v[48:51], v[96:99]\n"
    "ds_read_b128 v[252:255], v21 offset:1536\n"
    "ds_read_b64_tr_b16 v[36:37], v24 offset:24352\n"
    "ds_read_b64_tr_b16 v[38:39], v24 offset:24608\n"
    "v_mfma_f32_16x16x32_bf16 v[100:103], v[244:247], v[48:51], v[100:103]\n"
    "v_mfma_f32_16x16x32_bf16 v[104:107], v[240:243], v[52:55], v[104:107]\n"
    "ds_read_b64_tr_b16 v[40:41], v24 offset:24384\n"
    "ds_read_b64_tr_b16 v[42:43], v24 offset:24640\n"
    "v_mfma_f32_16x16x32_bf16 v[108:111], v[244:247], v[52:55], v[108:111]\n"
    "s_waitcnt lgkmcnt(8)\n"
    "v_mfma_f32_16x16x32_bf16 v[112:115], v[240:243], v[56:59], v[112:115]\n"
    "ds_read_b64_tr_b16 v[44:45], v24 offset:24416\n"
    "ds_read_b64_tr_b16 v[46:47], v24 offset:24672\n"
    "v_mfma_f32_16x16x32_bf16 v[116:119], v[244:247], v[56:59], v[116:119]\n"
    "v_mfma_f32_16x16x32_bf16 v[120:123], v[240:243], v[60:63], v[120:123]\n"
    "ds_read_b64_tr_b16 v[48:49], v24 offset:28544\n"
    "ds_read_b64_tr_b16 v[50:51], v24 offset:28800\n"
    "v_mfma_f32_16x16x32_bf16 v[124:127], v[244:247], v[60:63], v[124:127]\n"
    "s_waitcnt lgkmcnt(6)\n"
    "v_mfma_f32_16x16x32_bf16 v[64:67], v[248:251], v[32:35], v[64:67]\n"
    "ds_read_b64_tr_b16 v[52:53], v24 offset:28576\n"
    "ds_read_b64_tr_b16 v[54:55], v24 offset:28832\n"
    "v_mfma_f32_16x16x32_bf16 v[68:71], v[252:255], v[32:35], v[68:71]\n"
    "v_mfma_f32_16x16x32_bf16 v[72:75], v[248:251], v[36:39], v[72:75]\n"
    "ds_read_b64_tr_b16 v[56:57], v24 offset:28608\n"
    "ds_read_b64_tr_b16 v[58:59], v24 offset:28864\n"
    "v_mfma_f32_16x16x32_bf16 v[76:79], v[252:255], v[36:39], v[76:79]\n"
    "s_waitcnt lgkmcnt(6)\n"
    "v_mfma_f32_16x16x32_bf16 v[80:83], v[248:251], v[40:43], v[80:83]\n"
    "ds_read_b64_tr_b16 v[60:61], v24 offset:28640\n"
    "ds_read_b64_tr_b16 v[62:63], v24 offset:28896\n"
    "v_mfma_f32_16x16x32_bf16 v[84:87], v[252:255], v[40:43], v[84:87]\n"
    "s_barrier\n"
    "v_mfma_f32_16x16x32_bf16 v[88:91], v[248:251], v[44:47], v[88:91]\n"
    "v_mfma_f32_16x16x32_bf16 v[92:95], v[252:255], v[44:47], v[92:95]\n"
    "s_waitcnt lgkmcnt(4)\n"
    "v_mfma_f32_16x16x32_bf16 v[96:99], v[248:251], v[48:51], v[96:99]\n"
    "v_mfma_f32_16x16x32_bf16 v[100:103], v[252:255], v[48:51], v[100:103]\n"
    "v_mfma_f32_16x16x32_bf16 v[104:107], v[248:251], v[52:55], v[104:107]\n"
    "v_mfma_f32_16x16x32_bf16 v[108:111], v[252:255], v[52:55], v[108:111]\n"
    "s_waitcnt lgkmcnt(0)\n"
    "v_mfma_f32_16x16x32_bf16 v[112:115], v[248:251], v[56:59], v[112:115]\n"
    "v_mfma_f32_16x16x32_bf16 v[116:119], v[252:255], v[56:59], v[116:119]\n"
    "v_mfma_f32_16x16x32_bf16 v[120:123], v[248:251], v[60:63], v[120:123]\n"
    "v_mfma_f32_16x16x32_bf16 v[124:127], v[252:255], v[60:63], v[124:127]\n"
    ".Lhb:\n"
    "s_add_u32 m0, s66, 0x19d00\n"
    "s_nop 0\n"
    "buffer_load_dwordx4 v28, s[12:15], s69 offen offset:0 lds\n"
    "buffer_load_dwordx4 v29, s[12:15], s69 offen offset:1024 lds\n"
    "buffer_load_dwordx4 v30, s[12:15], s69 offen offset:2048 lds\n"
    "v_pk_mul_f32 v[64:65], v[64:65], s[46:47] op_sel_hi:[1,0]\n"
    "v_pk_mul_f32 v[66:67], v[66:67], s[46:47] op_sel_hi:[1,0]\n"
    "v_pk_mul_f32 v[68:69], v[68:69], s[46:47] op_sel_hi:[1,0]\n"
    "v_pk_mul_f32 v[70:71], v[70:71], s[46:47] op_sel_hi:[1,0]\n"
    "v_cvt_pk_bf16_f32 v8, v64, v65\n"
    "v_cvt_pk_bf16_f32 v10, v68, v69\n"
    "v_cvt_pk_bf16_f32 v9, v66, v67\n"
    "v_cvt_pk_bf16_f32 v11, v70, v71\n"
    "s_cmp_lt_u32 s50, 8\n"
    "s_cbranch_scc0 .Lskip_8\n"
    "v_permlane16_swap_b32 v8, v10\n"
    "v_permlane16_swap_b32 v9, v11\n"
    ".Lskip_8:\n"
    "buffer_store_dwordx4 v[8:11], v31, s[28:31], s75 offen offset:0 sc1\n"
    "s_add_u32 s75, s75, s76\n"
    "v_pk_mul_f32 v[72:73], v[72:73], s[46:47] op_sel_hi:[1,0]\n"
    "v_pk_mul_f32 v[74:75], v[74:75], s[46:47] op_sel_hi:[1,0]\n"
    "v_pk_mul_f32 v[76:77], v[76:77], s[46:47] op_sel_hi:[1,0]\n"
    "v_pk_mul_f32 v[78:79], v[78:79], s[46:47] op_sel_hi:[1,0]\n"
    "v_cvt_pk_bf16_f32 v12, v72, v73\n"
    "v_cvt_pk_bf16_f32 v14, v76, v77\n"
    "v_cvt_pk_bf16_f32 v13, v74, v75\n"
    "v_cvt_pk_bf16_f32 v15, v78, v79\n"
    "s_cmp_lt_u32 s50, 8\n"
    "s_cbranch_scc0 .Lskip_9\n"
    "v_permlane16_swap_b32 v12, v14\n"
    "v_permlane16_swap_b32 v13, v15\n"
    ".Lskip_9:\n"
    "buffer_store_dwordx4 v[12:15], v31, s[28:31], s75 offen offset:0 sc1\n"
    "s_add_u32 s75, s75, s76\n"
    "s_add_u32 m0, s66, 0x1cd80\n"
    "s_nop 0\n"
    "buffer_load_dwordx4 v28, s[16:19], s69 offen offset:0 lds\n"
    "buffer_load_dwordx4 v29, s[16:19], s69 offen offset:1024 lds\n"
    "buffer_load_dwordx4 v30, s[16:19], s69 offen offset:2048 lds\n"
    "v_pk_mul_f32 v[80:81], v[80:81], s[46:47] op_sel_hi:[1,0]\n"
    "v_pk_mul_f32 v[82:83], v[82:83], s[46:47] op_sel_hi:[1,0]\n"
    "v_pk_mul_f32 v[84:85], v[84:85], s[46:47] op_sel_hi:[1,0]\n"
    "v_pk_mul_f32 v[86:87], v[86:87], s[46:47] op_sel_hi:[1,0]\n"
    "v_cvt_pk_bf16_f32 v8, v80, v81\n"
    "v_cvt_pk_bf16_f32 v10, v84, v85\n"
    "v_cvt_pk_bf16_f32 v9, v82, v83\n"
    "v_cvt_pk_bf16_f32 v11, v86, v87\n"
    "s_cmp_lt_u32 s50, 8\n"
    "s_cbranch_scc0 .Lskip_10\n"
    "v_permlane16_swap_b32 v8, v10\n"
    "v_permlane16_swap_b32 v9, v11\n"
    ".Lskip_10:\n"
    "buffer_store_dwordx4 v[8:11], v31, s[28:31], s75 offen offset:0 sc1\n"
    "s_add_u32 s75, s75, s76\n"
    "v_pk_mul_f32 v[88:89], v[88:89], s[46:47] op_sel_hi:[1,0]\n"
    "v_pk_mul_f32 v[90:91], v[90:91], s[46:47] op_sel_hi:[1,0]\n"
    "v_pk_mul_f32 v[92:93], v[92:93], s[46:47] op_sel_hi:[1,0]\n"
    "v_pk_mul_f32 v[94:95], v[94:95], s[46:47] op_sel_hi:[1,0]\n"
    "v_cvt_pk_bf16_f32 v12, v88, v89\n"
    "v_cvt_pk_bf16_f32 v14, v92, v93\n"
    "v_cvt_pk_bf16_f32 v13, v90, v91\n"
    "v_cvt_pk_bf16_f32 v15, v94, v95\n"
    "s_cmp_lt_u32 s50, 8\n"
    "s_cbranch_scc0 .Lskip_11\n"
    "v_permlane16_swap_b32 v12, v14\n"
    "v_permlane16_swap_b32 v13, v15\n"
    ".Lskip_11:\n"
    "buffer_store_dwordx4 v[12:15], v31, s[28:31], s75 offen offset:0 sc1\n"
    "s_add_u32 s75, s75, s76\n"
    "ds_read_b64_tr_b16 v[32:33], v25 offset:39168\n"
    "ds_read_b64_tr_b16 v[34:35], v25 offset:39936\n"
    "ds_read_b64_tr_b16 v[36:37], v25 offset:51584\n"
    "ds_read_b64_tr_b16 v[38:39], v25 offset:52352\n"
    "ds_read_b64_tr_b16 v[40:41], v25 offset:39200\n"
    "ds_read_b64_tr_b16 v[42:43], v25 offset:39968\n"
    "ds_read_b64_tr_b16 v[44:45], v25 offset:51616\n"
    "ds_read_b64_tr_b16 v[46:47], v25 offset:52384\n"
    "s_barrier\n"
    "s_waitcnt lgkmcnt(4)\n"
    "v_mfma_f32_16x16x32_bf16 v[64:67], v[32:35], v[128:131], 0\n"
    "ds_read_b64_tr_b16 v[48:49], v25 offset:45376\n"
    "ds_read_b64_tr_b16 v[50:51], v25 offset:46144\n"
    "v_mfma_f32_16x16x32_bf16 v[68:71], v[32:35], v[132:135], 0\n"
    "v_pk_mul_f32 v[96:97], v[96:97], s[46:47] op_sel_hi:[1,0]\n"
    "v_pk_mul_f32 v[98:99], v[98:99], s[46:47] op_sel_hi:[1,0]\n"
    "v_pk_mul_f32 v[100:101], v[100:101], s[46:47] op_sel_hi:[1,0]\n"
    "v_pk_mul_f32 v[102:103], v[102:103], s[46:47] op_sel_hi:[1,0]\n"
    "v_cvt_pk_bf16_f32 v8, v96, v97\n"
    "v_cvt_pk_bf16_f32 v10, v100, v101\n"
    "v_cvt_pk_bf16_f32 v9, v98, v99\n"
    "v_cvt_pk_bf16_f32 v11, v102, v103\n"
    "s_cmp_lt_u32 s50, 8\n"
    "s_cbranch_scc0 .Lskip_12\n"
    "v_permlane16_swap_b32 v8, v10\n"
    "v_permlane16_swap_b32 v9, v11\n"
    ".Lskip_12:\n"
    "buffer_store_dwordx4 v[8:11], v31, s[28:31], s75 offen offset:0 sc1\n"
    "s_add_u32 s75, s75, s76\n"
    "v_mfma_f32_16x16x32_bf16 v[72:75], v[36:39], v[128:131], 0\n"
    "ds_read_b64_tr_b16 v[52:53], v25 offset:57792\n"
    "ds_read_b64_tr_b16 v[54:55], v25 offset:58560\n"
    "v_mfma_f32_16x16x32_bf16 v[76:79], v[36:39], v[132:135], 0\n"
    "v_pk_mul_f32 v[104:105], v[104:105], s[46:47] op_sel_hi:[1,0]\n"
    "v_pk_mul_f32 v[106:107], v[106:107], s[46:47] op_sel_hi:[1,0]\n"
    "v_pk_mul_f32 v[108:109], v[108:109], s[46:47] op_sel_hi:[1,0]\n"
    "v_pk_mul_f32 v[110:111], v[110:111], s[46:47] op_sel_hi:[1,0]\n"
    "v_cvt_pk_bf16_f32 v12, v104, v105\n"
    "v_cvt_pk_bf16_f32 v14, v108, v109\n"
    "v_cvt_pk_bf16_f32 v13, v106, v107\n"
    "v_cvt_pk_bf16_f32 v15, v110, v111\n"
    "s_cmp_lt_u32 s50, 8\n"
    "s_cbranch_scc0 .Lskip_13\n"
    "v_permlane16_swap_b32 v12, v14\n"
    "v_permlane16_swap_b32 v13, v15\n"
    ".Lskip_13:\n"
    "buffer_store_dwordx4 v[12:15], v31, s[28:31], s75 offen offset:0 sc1\n"
    "s_add_u32 s75, s75, s76\n"
    "s_waitcnt lgkmcnt(4)\n"
    "v_mfma_f32_16x16x32_bf16 v[80:83], v[40:43], v[128:131], 0\n"
    "ds_read_b64_tr_b16 v[56:57], v25 offset:45408\n"
    "ds_read_b64_tr_b16 v[58:59], v25 offset:46176\n"
    "v_mfma_f32_16x16x32_bf16 v[84:87], v[40:43], v[132:135], 0\n"
    "s_add_u32 m0, s66, 0x21f00\n"
    "s_nop 0\n"
    "buffer_load_dwordx4 v28, s[12:15], s70 offen offset:0 lds\n"
    "buffer_load_dwordx4 v29, s[12:15], s70 offen offset:1024 lds\n"
    "buffer_load_dwordx4 v30, s[12:15], s70 offen offset:2048 lds\n"
    "v_mfma_f32_16x16x32_bf16 v[88:91], v[44:47], v[128:131], 0\n"
    "ds_read_b64_tr_b16 v[60:61], v25 offset:57824\n"
    "ds_read_b64_tr_b16 v[62:63], v25 offset:58592\n"
    "v_mfma_f32_16x16x32_bf16 v[92:95], v[44:47], v[132:135], 0\n"
    "v_pk_mul_f32 v[112:113], v[112:113], s[46:47] op_sel_hi:[1,0]\n"
    "v_pk_mul_f32 v[114:115], v[114:115], s[46:47] op_sel_hi:[1,0]\n"
    "v_pk_mul_f32 v[116:117], v[116:117], s[46:47] op_sel_hi:[1,0]\n"
    "v_pk_mul_f32 v[118:119], v[118:119], s[46:47] op_sel_hi:[1,0]\n"
    "v_cvt_pk_bf16_f32 v8, v112, v113\n"
    "v_cvt_pk_bf16_f32 v10, v116, v117\n"
    "v_cvt_pk_bf16_f32 v9, v114, v115\n"
    "v_cvt_pk_bf16_f32 v11, v118, v119\n"
    "s_cmp_lt_u32 s50, 8\n"
    "s_cbranch_scc0 .Lskip_14\n"
    "v_permlane16_swap_b32 v8, v10\n"
    "v_permlane16_swap_b32 v9, v11\n"
    ".Lskip_14:\n"
    "buffer_store_dwordx4 v[8:11], v31, s[28:31], s75 offen offset:0 sc1\n"
    "s_add_u32 s75, s75, s76\n"
    "s_waitcnt lgkmcnt(4)\n"
    "v_mfma_f32_16x16x32_bf16 v[64:67], v[48:51], v[136:139], v[64:67]\n"
    "ds_read_b64_tr_b16 v[32:33], v26 offset:6912\n"
    "ds_read_b64_tr_b16 v[34:35], v26 offset:7680\n"
    "v_mfma_f32_16x16x32_bf16 v[68:71], v[48:51], v[140:143], v[68:71]\n"
    "v_pk_mul_f32 v[120:121], v[120:121], s[46:47] op_sel_hi:[1,0]\n"
    "v_pk_mul_f32 v[122:123], v[122:123], s[46:47] op_sel_hi:[1,0]\n"
    "v_pk_mul_f32 v[124:125], v[124:125], s[46:47] op_sel_hi:[1,0]\n"
    "v_pk_mul_f32 v[126:127], v[126:127], s[46:47] op_sel_hi:[1,0]\n"
    "v_cvt_pk_bf16_f32 v12, v120, v121\n"
    "v_cvt_pk_bf16_f32 v14, v124, v125\n"
    "v_cvt_pk_bf16_f32 v13, v122, v123\n"
    "v_cvt_pk_bf16_f32 v15, v126, v127\n"
    "s_cmp_lt_u32 s50, 8\n"
    "s_cbranch_scc0 .Lskip_15\n"
    "v_permlane16_swap_b32 v12, v14\n"
    "v_permlane16_swap_b32 v13, v15\n"
    ".Lskip_15:\n"
    "buffer_store_dwordx4 v[12:15], v31, s[28:31], s75 offen offset:0 sc1\n"
    "v_mfma_f32_16x16x32_bf16 v[72:75], v[52:55], v[136:139], v[72:75]\n"
    "ds_read_b64_tr_b16 v[36:37], v26 offset:19328\n"
    "ds_read_b64_tr_b16 v[38:39], v26 offset:20096\n"
    "v_mfma_f32_16x16x32_bf16 v[76:79], v[52:55], v[140:143], v[76:79]\n"
    "s_add_u32 m0, s66, 0x24f80\n"
    "s_nop 0\n"
    "buffer_load_dwordx4 v28, s[16:19], s70 offen offset:0 lds\n"
    "buffer_load_dwordx4 v29, s[16:19], s70 offen offset:1024 lds\n"
    "buffer_load_dwordx4 v30, s[16:19], s70 offen offset:2048 lds\n"
    "s_waitcnt lgkmcnt(4)\n"
    "v_mfma_f32_16x16x32_bf16 v[80:83], v[56:59], v[136:139], v[80:83]\n"
    "ds_read_b64_tr_b16 v[40:41], v26 offset:6944\n"
    "ds_read_b64_tr_b16 v[42:43], v26 offset:7712\n"
    "v_mfma_f32_16x16x32_bf16 v[84:87], v[56:59], v[140:143], v[84:87]\n"
    "v_mfma_f32_16x16x32_bf16 v[88:91], v[60:63], v[136:139], v[88:91]\n"
    "ds_read_b64_tr_b16 v[44:45], v26 offset:19360\n"
    "ds_read_b64_tr_b16 v[46:47], v26 offset:20128\n"
    "v_mfma_f32_16x16x32_bf16 v[92:95], v[60:63], v[140:143], v[92:95]\n"
    "s_waitcnt vmcnt(12)\n"
    "s_barrier\n"
    "s_waitcnt lgkmcnt(4)\n"
    "v_mfma_f32_16x16x32_bf16 v[64:67], v[32:35], v[144:147], v[64:67]\n"
    "ds_read_b64_tr_b16 v[48:49], v26 offset:13120\n"
    "ds_read_b64_tr_b16 v[50:51], v26 offset:13888\n"
    "v_mfma_f32_16x16x32_bf16 v[68:71], v[32:35], v[148:151], v[68:71]\n"
    "v_mfma_f32_16x16x32_bf16 v[72:75], v[36:39], v[144:147], v[72:75]\n"
    "ds_read_b64_tr_b16 v[52:53], v26 offset:25536\n"
    "ds_read_b64_tr_b16 v[54:55], v26 offset:26304\n"
    "v_mfma_f32_16x16x32_bf16 v[76:79], v[36:39], v[148:151], v[76:79]\n"
    "s_waitcnt lgkmcnt(4)\n"
    "v_mfma_f32_16x16x32_bf16 v[80:83], v[40:43], v[144:147], v[80:83]\n"
    "ds_read_b64_tr_b16 v[56:57], v26 offset:13152\n"
    "ds_read_b64_tr_b16 v[58:59], v26 offset:13920\n"
    "v_mfma_f32_16x16x32_bf16 v[84:87], v[40:43], v[148:151], v[84:87]\n"
    "s_add_u32 m0, s66, 0x9900\n"
    "s_nop 0\n"
    "buffer_load_dwordx4 v28, s[12:15], s71 offen offset:0 lds\n"
    "buffer_load_dwordx4 v29, s[12:15], s71 offen offset:1024 lds\n"
    "buffer_load_dwordx4 v30, s[12:15], s71 offen offset:2048 lds\n"
    "v_mfma_f32_16x16x32_bf16 v[88:91], v[44:47], v[144:147], v[88:91]\n"
    "ds_read_b64_tr_b16 v[60:61], v26 offset:25568\n"
    "ds_read_b64_tr_b16 v[62:63], v26 offset:26336\n"
    "v_mfma_f32_16x16x32_bf16 v[92:95], v[44:47], v[148:151], v[92:95]\n"
    "s_waitcnt lgkmcnt(4)\n"
    "v_mfma_f32_16x16x32_bf16 v[64:67], v[48:51], v[152:155], v[64:67]\n"
    "ds_read_b64_tr_b16 v[32:33], v26 offset:40192\n"
    "ds_read_b64_tr_b16 v[34:35], v26 offset:40960\n"
    "v_mfma_f32_16x16x32_bf16 v[68:71], v[48:51], v[156:159], v[68:71]\n"
    "v_mfma_f32_16x16x32_bf16 v[72:75], v[52:55], v[152:155], v[72:75]\n"
    "ds_read_b64_tr_b16 v[36:37], v26 offset:52608\n"
    "ds_read_b64_tr_b16 v[38:39], v26 offset:53376\n"
    "v_mfma_f32_16x16x32_bf16 v[76:79], v[52:55], v[156:159], v[76:79]\n"
    "s_add_u32 m0, s66, 0xc980\n"
    "s_nop 0\n"
    "buffer_load_dwordx4 v28, s[16:19], s71 offen offset:0 lds\n"
    "buffer_load_dwordx4 v29, s[16:19], s71 offen offset:1024 lds\n"
    "buffer_load_dwordx4 v30, s[16:19], s71 offen offset:2048 lds\n"
    "s_waitcnt lgkmcnt(4)\n"
    "v_mfma_f32_16x16x32_bf16 v[80:83], v[56:59], v[152:155], v[80:83]\n"
    "ds_read_b64_tr_b16 v[40:41], v26 offset:40224\n"
    "ds_read_b64_tr_b16 v[42:43], v26 offset:40992\n"
    "v_mfma_f32_16x16x32_bf16 v[84:87], v[56:59], v[156:159], v[84:87]\n"
    "v_mfma_f32_16x16x32_bf16 v[88:91], v[60:63], v[152:155], v[88:91]\n"
    "ds_read_b64_tr_b16 v[44:45], v26 offset:52640\n"
    "ds_read_b64_tr_b16 v[46:47], v26 offset:53408\n"
    "v_mfma_f32_16x16x32_bf16 v[92:95], v[60:63], v[156:159], v[92:95]\n"
    "s_waitcnt vmcnt(6)\n"
    "s_barrier\n"
    "s_cmp_eq_u32 s48, 0\n"
    "s_cbranch_scc0 .Lskip_16\n"
    "s_mov_b64 exec, 1\n"
    "buffer_store_dwordx2 v[4:5], off, s[28:31], s77 sc1\n"
    "s_mov_b64 exec, -1\n"
    ".Lskip_16:\n"
    "s_waitcnt lgkmcnt(4)\n"
    "v_mfma_f32_16x16x32_bf16 v[64:67], v[32:35], v[160:163], v[64:67]\n"
    "ds_read_b64_tr_b16 v[48:49], v26 offset:46400\n"
    "ds_read_b64_tr_b16 v[50:51], v26 offset:47168\n"
    "v_mfma_f32_16x16x32_bf16 v[68:71], v[32:35], v[164:167], v[68:71]\n"
    "v_mfma_f32_16x16x32_bf16 v[72:75], v[36:39], v[160:163], v[72:75]\n"
    "ds_read_b64_tr_b16 v[52:53], v26 offset:58816\n"
    "ds_read_b64_tr_b16 v[54:55], v26 offset:59584\n"
    "v_mfma_f32_16x16x32_bf16 v[76:79], v[36:39], v[164:167], v[76:79]\n"
    "s_waitcnt lgkmcnt(4)\n"
    "v_mfma_f32_16x16x32_bf16 v[80:83], v[40:43], v[160:163], v[80:83]\n"
    "ds_read_b64_tr_b16 v[56:57], v26 offset:46432\n"
    "ds_read_b64_tr_b16 v[58:59], v26 offset:47200\n"
    "v_mfma_f32_16x16x32_bf16 v[84:87], v[40:43], v[164:167], v[84:87]\n"
    "s_add_u32 m0, s66, 0x11b00\n"
    "s_nop 0\n"
    "buffer_load_dwordx4 v28, s[12:15], s72 offen offset:0 lds\n"
    "buffer_load_dwordx4 v29, s[12:15], s72 offen offset:1024 lds\n"
    "buffer_load_dwordx4 v30, s[12:15], s72 offen offset:2048 lds\n"
    "v_mfma_f32_16x16x32_bf16 v[88:91], v[44:47], v[160:163], v[88:91]\n"
    "ds_read_b64_tr_b16 v[60:61], v26 offset:58848\n"
    "ds_read_b64_tr_b16 v[62:63], v26 offset:59616\n"
    "v_mfma_f32_16x16x32_bf16 v[92:95], v[44:47], v[164:167], v[92:95]\n"
    "s_waitcnt lgkmcnt(4)\n"
    "v_mfma_f32_16x16x32_bf16 v[64:67], v[48:51], v[168:171], v[64:67]\n"
    "ds_read_b64_tr_b16 v[32:33], v27 offset:7936\n"
    "ds_read_b64_tr_b16 v[34:35], v27 offset:8704\n"
    "v_mfma_f32_16x16x32_bf16 v[68:71], v[48:51], v[172:175], v[68:71]\n"
    "v_mfma_f32_16x16x32_bf16 v[72:75], v[52:55], v[168:171], v[72:75]\n"
    "ds_read_b64_tr_b16 v[36:37], v27 offset:20352\n"
    "ds_read_b64_tr_b16 v[38:39], v27 offset:21120\n"
    "v_mfma_f32_16x16x32_bf16 v[76:79], v[52:55], v[172:175], v[76:79]\n"
    "s_add_u32 m0, s66, 0x14b80\n"
    "s_nop 0\n"
    "buffer_load_dwordx4 v28, s[16:19], s72 offen offset:0 lds\n"
    "buffer_load_dwordx4 v29, s[16:19], s72 offen offset:1024 lds\n"
    "buffer_load_dwordx4 v30, s[16:19], s72 offen offset:2048 lds\n"
    "s_waitcnt lgkmcnt(4)\n"
    "v_mfma_f32_16x16x32_bf16 v[80:83], v[56:59], v[168:171], v[80:83]\n"
    "ds_read_b64_tr_b16 v[40:41], v27 offset:7968\n"
    "ds_read_b64_tr_b16 v[42:43], v27 offset:8736\n"
    "v_mfma_f32_16x16x32_bf16 v[84:87], v[56:59], v[172:175], v[84:87]\n"
    "v_mfma_f32_16x16x32_bf16 v[88:91], v[60:63], v[168:171], v[88:91]\n"
    "ds_read_b64_tr_b16 v[44:45], v27 offset:20384\n"
    "ds_read_b64_tr_b16 v[46:47], v27 offset:21152\n"
    "v_mfma_f32_16x16x32_bf16 v[92:95], v[60:63], v[172:175], v[92:95]\n"
    "s_waitcnt vmcnt(6)\n"
    "s_barrier\n"
    "s_waitcnt lgkmcnt(4)\n"
    "v_mfma_f32_16x16x32_bf16 v[64:67], v[32:35], v[176:179], v[64:67]\n"
    "ds_read_b64_tr_b16 v[48:49], v27 offset:14144\n"
    "ds_read_b64_tr_b16 v[50:51], v27 offset:14912\n"
    "v_mfma_f32_16x16x32_bf16 v[68:71], v[32:35], v[180:183], v[68:71]\n"
    "v_mfma_f32_16x16x32_bf16 v[72:75], v[36:39], v[176:179], v[72:75]\n"
    "ds_read_b64_tr_b16 v[52:53], v27 offset:26560\n"
    "ds_read_b64_tr_b16 v[54:55], v27 offset:27328\n"
    "v_mfma_f32_16x16x32_bf16 v[76:79], v[36:39], v[180:183], v[76:79]\n"
    "s_waitcnt lgkmcnt(4)\n"
    "v_mfma_f32_16x16x32_bf16 v[80:83], v[40:43], v[176:179], v[80:83]\n"
    "ds_read_b64_tr_b16 v[56:57], v27 offset:14176\n"
    "ds_read_b64_tr_b16 v[58:59], v27 offset:14944\n"
    "v_mfma_f32_16x16x32_bf16 v[84:87], v[40:43], v[180:183], v[84:87]\n"
    "s_add_u32 m0, s66, 0x19d00\n"
    "s_nop 0\n"
    "buffer_load_dwordx4 v28, s[12:15], s73 offen offset:0 lds\n"
    "buffer_load_dwordx4 v29, s[12:15], s73 offen offset:1024 lds\n"
    "buffer_load_dwordx4 v30, s[12:15], s73 offen offset:2048 lds\n"
    "v_mfma_f32_16x16x32_bf16 v[88:91], v[44:47], v[176:179], v[88:91]\n"
    "ds_read_b64_tr_b16 v[60:61], v27 offset:26592\n"
    "ds_read_b64_tr_b16 v[62:63], v27 offset:27360\n"
    "v_mfma_f32_16x16x32_bf16 v[92:95], v[44:47], v[180:183], v[92:95]\n"
    "s_waitcnt lgkmcnt(4)\n"
    "v_mfma_f32_16x16x32_bf16 v[64:67], v[48:51], v[184:187], v[64:67]\n"
    "ds_read_b64_tr_b16 v[32:33], v25 offset:39168\n"
    "ds_read_b64_tr_b16 v[34:35], v25 offset:39936\n"
    "v_mfma_f32_16x16x32_bf16 v[68:71], v[48:51], v[188:191], v[68:71]\n"
    "v_mfma_f32_16x16x32_bf16 v[72:75], v[52:55], v[184:187], v[72:75]\n"
    "ds_read_b64_tr_b16 v[36:37], v25 offset:51584\n"
    "ds_read_b64_tr_b16 v[38:39], v25 offset:52352\n"
    "v_mfma_f32_16x16x32_bf16 v[76:79], v[52:55], v[188:191], v[76:79]\n"
    "s_add_u32 m0, s66, 0x1cd80\n"
    "s_nop 0\n"
    "buffer_load_dwordx4 v28, s[16:19], s73 offen offset:0 lds\n"
    "buffer_load_dwordx4 v29, s[16:19], s73 offen offset:1024 lds\n"
    "buffer_load_dwordx4 v30, s[16:19], s73 offen offset:2048 lds\n"
    "s_waitcnt lgkmcnt(4)\n"
    "v_mfma_f32_16x16x32_bf16 v[80:83], v[56:59], v[184:187], v[80:83]\n"
    "ds_read_b64_tr_b16 v[40:41], v25 offset:39200\n"
    "ds_read_b64_tr_b16 v[42:43], v25 offset:39968\n"
    "v_mfma_f32_16x16x32_bf16 v[84:87], v[56:59], v[188:191], v[84:87]\n"
    "v_mfma_f32_16x16x32_bf16 v[88:91], v[60:63], v[184:187], v[88:91]\n"
    "ds_read_b64_tr_b16 v[44:45], v25 offset:51616\n"
    "ds_read_b64_tr_b16 v[46:47], v25 offset:52384\n"
    "v_mfma_f32_16x16x32_bf16 v[92:95], v[60:63], v[188:191], v[92:95]\n"
    "s_waitcnt vmcnt(6)\n"
    "s_barrier\n"
    "s_waitcnt lgkmcnt(4)\n"
    "v_mfma_f32_16x16x32_bf16 v[64:67], v[32:35], v[192:195], v[64:67]\n"
    "ds_read_b64_tr_b16 v[48:49], v25 offset:45376\n"
    "ds_read_b64_tr_b16 v[50:51], v25 offset:46144\n"
    "v_mfma_f32_16x16x32_bf16 v[68:71], v[32:35], v[196:199], v[68:71]\n"
    "v_add_u32 v128, 0x10000, v1\n"
    "v_mov_b32 v129, 0xdddddddd\n"
    "v_and_b32 v6, 15, v0\n"
    "v_lshrrev_b32 v7, 4, v0\n"
    "v_lshlrev_b32 v134, 4, v6\n"
    "v_mfma_f32_16x16x32_bf16 v[72:75], v[36:39], v[192:195], v[72:75]\n"
    "ds_read_b64_tr_b16 v[52:53], v25 offset:57792\n"
    "ds_read_b64_tr_b16 v[54:55], v25 offset:58560\n"
    "v_mfma_f32_16x16x32_bf16 v[76:79], v[36:39], v[196:199], v[76:79]\n"
    "v_lshl_or_b32 v134, v7, 9, v134\n"
    "v_lshlrev_b32 v159, 4, v6\n"
    "v_lshl_add_u32 v159, v7, 2, v159\n"
    "v_add_u32 v159, 4, v159\n"
    "v_and_b32 v138, 1, v0\n"
    "s_waitcnt lgkmcnt(4)\n"
    "v_mfma_f32_16x16x32_bf16 v[80:83], v[40:43], v[192:195], v[80:83]\n"
    "ds_read_b64_tr_b16 v[56:57], v25 offset:45408\n"
    "ds_read_b64_tr_b16 v[58:59], v25 offset:46176\n"
    "v_mfma_f32_16x16x32_bf16 v[84:87], v[40:43], v[196:199], v[84:87]\n"
    "s_add_u32 m0, s66, 0x21f00\n"
    "s_nop 0\n"
    "buffer_load_dwordx4 v28, s[12:15], s74 offen offset:0 lds\n"
    "buffer_load_dwordx4 v29, s[12:15], s74 offen offset:1024 lds\n"
    "buffer_load_dwordx4 v30, s[12:15], s74 offen offset:2048 lds\n"
    "v_lshl_add_u32 v138, s54, 1, v138\n"
    "v_lshlrev_b32 v138, 4, v138\n"
    "v_cmp_gt_u32 vcc, 32, v0\n"
    "s_lshr_b32 s94, s53, 3\n"
    "s_add_u32 s94, s94, 32\n"
    "v_mfma_f32_16x16x32_bf16 v[88:91], v[44:47], v[192:195], v[88:91]\n"
    "ds_read_b64_tr_b16 v[60:61], v25 offset:57824\n"
    "ds_read_b64_tr_b16 v[62:63], v25 offset:58592\n"
    "v_mfma_f32_16x16x32_bf16 v[92:95], v[44:47], v[196:199], v[92:95]\n"
    "v_cndmask_b32 v159, v138, v159, vcc\n"
    "v_add_lshl_u32 v159, v159, s94, 7\n"
    "s_lshr_b32 s94, s53, 1\n"
    "v_lshl_add_u32 v135, s94, 4, v6\n"
    "s_lshr_b32 s94, s48, 2\n"
    "s_waitcnt lgkmcnt(4)\n"
    "v_mfma_f32_16x16x32_bf16 v[64:67], v[48:51], v[200:203], v[64:67]\n"
    "ds_read_b64_tr_b16 v[32:33], v26 offset:6912\n"
    "ds_read_b64_tr_b16 v[34:35], v26 offset:7680\n"
    "v_mfma_f32_16x16x32_bf16 v[68:71], v[48:51], v[204:207], v[68:71]\n"
    "s_lshl1_add_u32 s94, s54, s94\n"
    "s_lshl4_add_u32 s94, s94, 15\n"
    "v_mov_b32 v138, s94\n"
    "v_cndmask_b32 v135, v138, v135, s[42:43]\n"
    "v_and_b32 v142, 31, v0\n"
    "v_mfma_f32_16x16x32_bf16 v[72:75], v[52:55], v[200:203], v[72:75]\n"
    "ds_read_b64_tr_b16 v[36:37], v26 offset:19328\n"
    "ds_read_b64_tr_b16 v[38:39], v26 offset:20096\n"
    "v_mfma_f32_16x16x32_bf16 v[76:79], v[52:55], v[204:207], v[76:79]\n"
    "s_add_u32 m0, s66, 0x24f80\n"
    "s_nop 0\n"
    "buffer_load_dwordx4 v28, s[16:19], s74 offen offset:0 lds\n"
    "buffer_load_dwordx4 v29, s[16:19], s74 offen offset:1024 lds\n"
    "buffer_load_dwordx4 v30, s[16:19], s74 offen offset:2048 lds\n"
    "v_lshl_add_u32 v138, s48, 5, v142\n"
    "s_cmp_eq_u32 s2, 0\n"
    "s_cselect_b32 s94, 32, 64\n"
    "v_cmp_gt_u32 vcc, s94, v0\n"
    "s_movk_i32 s94, 288\n"
    "v_lshlrev_b32 v142, 2, v142\n"
    "s_waitcnt lgkmcnt(4)\n"
    "v_mfma_f32_16x16x32_bf16 v[80:83], v[56:59], v[200:203], v[80:83]\n"
    "ds_read_b64_tr_b16 v[40:41], v26 offset:6944\n"
    "ds_read_b64_tr_b16 v[42:43], v26 offset:7712\n"
    "v_mfma_f32_16x16x32_bf16 v[84:87], v[56:59], v[204:207], v[84:87]\n"
    "v_cndmask_b32 v135, v138, v135, vcc\n"
    "v_add_lshl_u32 v135, v135, s94, 7\n"
    "v_lshlrev_b32 v140, 2, v6\n"
    "v_and_b32 v138, 7, v0\n"
    "s_lshr_b32 s94, s87, 10\n"
    "v_mfma_f32_16x16x32_bf16 v[88:91], v[60:63], v[200:203], v[88:91]\n"
    "ds_read_b64_tr_b16 v[44:45], v26 offset:19360\n"
    "ds_read_b64_tr_b16 v[46:47], v26 offset:20128\n"
    "v_mfma_f32_16x16x32_bf16 v[92:95], v[60:63], v[204:207], v[92:95]\n"
    "s_add_u32 s94, s94, 1024\n"
    "v_add_lshl_u32 v136, v138, s94, 7\n"
    "v_lshrrev_b32 v158, 3, v0\n"
    "v_lshlrev_b32 v157, 5, v138\n"
    "s_movk_i32 s95, 272\n"
    "s_waitcnt vmcnt(6)\n"
    "s_barrier\n"
    "s_waitcnt lgkmcnt(4)\n"
    "v_mfma_f32_16x16x32_bf16 v[64:67], v[32:35], v[208:211], v[64:67]\n"
    "ds_read_b64_tr_b16 v[48:49], v26 offset:13120\n"
    "ds_read_b64_tr_b16 v[50:51], v26 offset:13888\n"
    "v_mfma_f32_16x16x32_bf16 v[68:71], v[32:35], v[212:215], v[68:71]\n"
    "v_mad_u32_u24 v157, v158, s95, v157\n"
    "s_mul_i32 s94, s48, 2176\n"
    "s_add_u32 s94, s94, 0x10000\n"
    "v_add_u32 v157, s94, v157\n"
    "v_lshlrev_b32 v156, 4, v7\n"
    "v_mfma_f32_16x16x32_bf16 v[72:75], v[36:39], v[208:211], v[72:75]\n"
    "ds_read_b64_tr_b16 v[52:53], v26 offset:25536\n"
    "ds_read_b64_tr_b16 v[54:55], v26 offset:26304\n"
    "v_mfma_f32_16x16x32_bf16 v[76:79], v[36:39], v[212:215], v[76:79]\n"
    "v_mad_u32_u24 v156, v6, s95, v156\n"
    "s_mul_i32 s94, s48, 8704\n"
    "s_add_u32 s94, s94, 0x10000\n"
    "v_add_u32 v156, s94, v156\n"
    "v_lshlrev_b32 v158, 10, v158\n"
    "s_waitcnt lgkmcnt(4)\n"
    "v_mfma_f32_16x16x32_bf16 v[80:83], v[40:43], v[208:211], v[80:83]\n"
    "ds_read_b64_tr_b16 v[56:57], v26 offset:13152\n"
    "ds_read_b64_tr_b16 v[58:59], v26 offset:13920\n"
    "v_mfma_f32_16x16x32_bf16 v[84:87], v[40:43], v[212:215], v[84:87]\n"
    "v_lshl_or_b32 v158, v138, 4, v158\n"
    "v_and_b32 v130, 16, v0\n"
    "v_lshlrev_b32 v130, 1, v130\n"
    "v_xor_b32 v130, v130, v158\n"
    "v_xor_b32 v131, 64, v130\n"
    "v_mfma_f32_16x16x32_bf16 v[88:91], v[44:47], v[208:211], v[88:91]\n"
    "ds_read_b64_tr_b16 v[60:61], v26 offset:25568\n"
    "ds_read_b64_tr_b16 v[62:63], v26 offset:26336\n"
    "v_mfma_f32_16x16x32_bf16 v[92:95], v[44:47], v[212:215], v[92:95]\n"
    "v_add_u32 v132, 14336, v130\n"
    "v_add_u32 v133, 21504, v131\n"
    "v_add_u32 v131, 7168, v131\n"
    "v_bfe_u32 v138, v0, 2, 2\n"
    "v_lshl_or_b32 v144, v7, 3, v138\n"
    "s_waitcnt lgkmcnt(4)\n"
    "v_mfma_f32_16x16x32_bf16 v[64:67], v[48:51], v[216:219], v[64:67]\n"
    "ds_read_b64_tr_b16 v[32:33], v26 offset:40192\n"
    "ds_read_b64_tr_b16 v[34:35], v26 offset:40960\n"
    "v_mfma_f32_16x16x32_bf16 v[68:71], v[48:51], v[220:223], v[68:71]\n"
    "v_lshlrev_b32 v144, 7, v144\n"
    "v_bfe_u32 v138, v0, 3, 2\n"
    "v_lshl_or_b32 v144, v138, 5, v144\n"
    "v_and_b32 v138, 3, v0\n"
    "v_lshl_or_b32 v144, v138, 3, v144\n"
    "v_mfma_f32_16x16x32_bf16 v[72:75], v[52:55], v[216:219], v[72:75]\n"
    "ds_read_b64_tr_b16 v[36:37], v26 offset:52608\n"
    "ds_read_b64_tr_b16 v[38:39], v26 offset:53376\n"
    "v_mfma_f32_16x16x32_bf16 v[76:79], v[52:55], v[220:223], v[76:79]\n"
    "s_lshl_b32 s94, s57, 13\n"
    "v_or_b32 v144, s94, v144\n"
    "v_xor_b32 v147, 32, v144\n"
    "v_xor_b32 v150, 64, v144\n"
    "v_xor_b32 v153, 96, v144\n"
    "s_waitcnt lgkmcnt(4)\n"
    "v_mfma_f32_16x16x32_bf16 v[80:83], v[56:59], v[216:219], v[80:83]\n"
    "ds_read_b64_tr_b16 v[40:41], v26 offset:40224\n"
    "ds_read_b64_tr_b16 v[42:43], v26 offset:40992\n"
    "v_mfma_f32_16x16x32_bf16 v[84:87], v[56:59], v[220:223], v[84:87]\n"
    "v_add_u32 v145, 0x10000, v144\n"
    "v_add_u32 v146, 0x20000, v144\n"
    "v_add_u32 v148, 0x10000, v147\n"
    "v_add_u32 v149, 0x20000, v147\n"
    "v_add_u32 v151, 0x10000, v150\n"
    "v_mfma_f32_16x16x32_bf16 v[88:91], v[60:63], v[216:219], v[88:91]\n"
    "ds_read_b64_tr_b16 v[44:45], v26 offset:52640\n"
    "ds_read_b64_tr_b16 v[46:47], v26 offset:53408\n"
    "v_mfma_f32_16x16x32_bf16 v[92:95], v[60:63], v[220:223], v[92:95]\n"
    "v_add_u32 v152, 0x20000, v150\n"
    "v_add_u32 v154, 0x10000, v153\n"
    "v_add_u32 v155, 0x20000, v153\n"
    "v_and_b32 v138, 27, v0\n"
    "v_cmp_eq_u32 vcc, 16, v138\n"
    "s_waitcnt vmcnt(0)\n"
    "s_barrier\n"
    "buffer_load_dwordx4 v[160:163], v1, s[28:31], s80 offen sc1\n"
    "s_add_u32 m0, s79, 0x0\n"
    "s_nop 0\n"
    "buffer_load_dwordx4 v1, s[28:31], s78 offen offset:0 sc1 lds\n"
    "buffer_load_dwordx4 v1, s[28:31], s78 offen offset:1024 sc1 lds\n"
    "buffer_load_dwordx4 v1, s[28:31], s78 offen offset:2048 sc1 lds\n"
    "buffer_load_dwordx4 v1, s[28:31], s78 offen offset:3072 sc1 lds\n"
    "s_waitcnt lgkmcnt(4)\n"
    "v_mfma_f32_16x16x32_bf16 v[64:67], v[32:35], v[224:227], v[64:67]\n"
    "ds_read_b64_tr_b16 v[48:49], v26 offset:46400\n"
    "ds_read_b64_tr_b16 v[50:51], v26 offset:47168\n"
    "v_mfma_f32_16x16x32_bf16 v[68:71], v[32:35], v[228:231], v[68:71]\n"
    "v_mov_b32 v138, 0x3f803f80\n"
    "v_mov_b64 v[176:177], 0\n"
    "v_cndmask_b32 v168, 0, v138, vcc\n"
    "v_mov_b32 v169, v168\n"
    "v_mov_b64 v[170:171], v[168:169]\n"
    "v_mfma_f32_16x16x32_bf16 v[72:75], v[36:39], v[224:227], v[72:75]\n"
    "ds_read_b64_tr_b16 v[52:53], v26 offset:58816\n"
    "ds_read_b64_tr_b16 v[54:55], v26 offset:59584\n"
    "v_mfma_f32_16x16x32_bf16 v[76:79], v[36:39], v[228:231], v[76:79]\n"
    "v_mov_b64 v[172:173], v[168:169]\n"
    "v_mov_b64 v[174:175], v[168:169]\n"
    "v_mov_b64 v[178:179], 0\n"
    "v_mov_b64 v[180:181], 0\n"
    "v_mov_b64 v[182:183], 0\n"
    "s_waitcnt lgkmcnt(4)\n"
    "v_mfma_f32_16x16x32_bf16 v[80:83], v[40:43], v[224:227], v[80:83]\n"
    "ds_read_b64_tr_b16 v[56:57], v26 offset:46432\n"
    "ds_read_b64_tr_b16 v[58:59], v26 offset:47200\n"
    "v_mfma_f32_16x16x32_bf16 v[84:87], v[40:43], v[228:231], v[84:87]\n"
    "v_mov_b64 v[184:185], 0\n"
    "v_mov_b64 v[186:187], 0\n"
    "v_mov_b64 v[188:189], 0\n"
    "v_mov_b64 v[190:191], 0\n"
    "v_mfma_f32_16x16x32_bf16 v[88:91], v[44:47], v[224:227], v[88:91]\n"
    "ds_read_b64_tr_b16 v[60:61], v26 offset:58848\n"
    "ds_read_b64_tr_b16 v[62:63], v26 offset:59616\n"
    "v_mfma_f32_16x16x32_bf16 v[92:95], v[44:47], v[228:231], v[92:95]\n"
    "s_waitcnt lgkmcnt(4)\n"
    "v_mfma_f32_16x16x32_bf16 v[64:67], v[48:51], v[232:235], v[64:67]\n"
    "ds_read_b64_tr_b16 v[32:33], v27 offset:7936\n"
    "ds_read_b64_tr_b16 v[34:35], v27 offset:8704\n"
    "v_mfma_f32_16x16x32_bf16 v[68:71], v[48:51], v[236:239], v[68:71]\n"
    "v_mfma_f32_16x16x32_bf16 v[72:75], v[52:55], v[232:235], v[72:75]\n"
    "ds_read_b64_tr_b16 v[36:37], v27 offset:20352\n"
    "ds_read_b64_tr_b16 v[38:39], v27 offset:21120\n"
    "v_mfma_f32_16x16x32_bf16 v[76:79], v[52:55], v[236:239], v[76:79]\n"
    "s_waitcnt lgkmcnt(4)\n"
    "v_mfma_f32_16x16x32_bf16 v[80:83], v[56:59], v[232:235], v[80:83]\n"
    "ds_read_b64_tr_b16 v[40:41], v27 offset:7968\n"
    "ds_read_b64_tr_b16 v[42:43], v27 offset:8736\n"
    "v_mfma_f32_16x16x32_bf16 v[84:87], v[56:59], v[236:239], v[84:87]\n"
    "v_mfma_f32_16x16x32_bf16 v[88:91], v[60:63], v[232:235], v[88:91]\n"
    "ds_read_b64_tr_b16 v[44:45], v27 offset:20384\n"
    "ds_read_b64_tr_b16 v[46:47], v27 offset:21152\n"
    "v_mfma_f32_16x16x32_bf16 v[92:95], v[60:63], v[236:239], v[92:95]\n"
    "s_waitcnt lgkmcnt(4)\n"
    "v_mfma_f32_16x16x32_bf16 v[64:67], v[32:35], v[240:243], v[64:67]\n"
    "ds_read_b64_tr_b16 v[48:49], v27 offset:14144\n"
    "ds_read_b64_tr_b16 v[50:51], v27 offset:14912\n"
    "v_mfma_f32_16x16x32_bf16 v[68:71], v[32:35], v[244:247], v[68:71]\n"
    "v_mfma_f32_16x16x32_bf16 v[72:75], v[36:39], v[240:243], v[72:75]\n"
    "ds_read_b64_tr_b16 v[52:53], v27 offset:26560\n"
    "ds_read_b64_tr_b16 v[54:55], v27 offset:27328\n"
    "v_mfma_f32_16x16x32_bf16 v[76:79], v[36:39], v[244:247], v[76:79]\n"
    "s_waitcnt lgkmcnt(4)\n"
    "v_mfma_f32_16x16x32_bf16 v[80:83], v[40:43], v[240:243], v[80:83]\n"
    "ds_read_b64_tr_b16 v[56:57], v27 offset:14176\n"
    "ds_read_b64_tr_b16 v[58:59], v27 offset:14944\n"
    "v_mfma_f32_16x16x32_bf16 v[84:87], v[40:43], v[244:247], v[84:87]\n"
    "v_mfma_f32_16x16x32_bf16 v[88:91], v[44:47], v[240:243], v[88:91]\n"
    "ds_read_b64_tr_b16 v[60:61], v27 offset:26592\n"
    "ds_read_b64_tr_b16 v[62:63], v27 offset:27360\n"
    "v_mfma_f32_16x16x32_bf16 v[92:95], v[44:47], v[244:247], v[92:95]\n"
    "s_waitcnt lgkmcnt(4)\n"
    "v_mfma_f32_16x16x32_bf16 v[64:67], v[48:51], v[248:251], v[64:67]\n"
    "v_mfma_f32_16x16x32_bf16 v[68:71], v[48:51], v[252:255], v[68:71]\n"
    "v_mfma_f32_16x16x32_bf16 v[72:75], v[52:55], v[248:251], v[72:75]\n"
    "v_mfma_f32_16x16x32_bf16 v[76:79], v[52:55], v[252:255], v[76:79]\n"
    "s_waitcnt lgkmcnt(0)\n"
    "v_mfma_f32_16x16x32_bf16 v[80:83], v[56:59], v[248:251], v[80:83]\n"
    "v_mfma_f32_16x16x32_bf16 v[84:87], v[56:59], v[252:255], v[84:87]\n"
    "v_mfma_f32_16x16x32_bf16 v[88:91], v[60:63], v[248:251], v[88:91]\n"
    "v_mfma_f32_16x16x32_bf16 v[92:95], v[60:63], v[252:255], v[92:95]\n"
    "s_add_u32 m0, s79, 0x8000\n"
    "s_add_u32 s78, s78, 0x8000\n"
    "buffer_load_dwordx4 v1, s[28:31], s78 offen offset:0 sc1 lds\n"
    "buffer_load_dwordx4 v1, s[28:31], s78 offen offset:1024 sc1 lds\n"
    "buffer_load_dwordx4 v1, s[28:31], s78 offen offset:2048 sc1 lds\n"
    "buffer_load_dwordx4 v1, s[28:31], s78 offen offset:3072 sc1 lds\n"
    "s_lshl_b32 s94, s55, 10\n"
    "s_add_u32 s96, s81, s94\n"
    "s_add_u32 s96, s96, 3072\n"
    "v_mov_b32 v40, s45\n"
    "v_pk_mul_f32 v[32:33], v[64:65], v[64:65]\n"
    "v_pk_mul_f32 v[34:35], v[66:67], v[66:67]\n"
    "v_pk_mul_f32 v[36:37], v[68:69], v[68:69]\n"
    "v_pk_mul_f32 v[38:39], v[70:71], v[70:71]\n"
    "v_pk_fma_f32 v[32:33], v[32:33], s[44:45], v[40:41] op_sel_hi:[1,0,0]\n"
    "v_pk_fma_f32 v[34:35], v[34:35], s[44:45], v[40:41] op_sel_hi:[1,0,0]\n"
    "v_pk_fma_f32 v[36:37], v[36:37], s[44:45], v[40:41] op_sel_hi:[1,0,0]\n"
    "v_pk_fma_f32 v[38:39], v[38:39], s[44:45], v[40:41] op_sel_hi:[1,0,0]\n"
    "v_pk_mul_f32 v[32:33], v[32:33], v[64:65]\n"
    "v_pk_mul_f32 v[34:35], v[34:35], v[66:67]\n"
    "v_pk_mul_f32 v[36:37], v[36:37], v[68:69]\n"
    "v_pk_mul_f32 v[38:39], v[38:39], v[70:71]\n"
    "v_exp_f32 v32, v32\n"
    "v_exp_f32 v33, v33\n"
    "v_exp_f32 v34, v34\n"
    "v_exp_f32 v35, v35\n"
    "v_exp_f32 v36, v36\n"
    "v_exp_f32 v37, v37\n"
    "v_exp_f32 v38, v38\n"
    "v_exp_f32 v39, v39\n"
    "v_pk_add_f32 v[32:33], v[32:33], 1.0 op_sel_hi:[1,0]\n"
    "v_pk_add_f32 v[34:35], v[34:35], 1.0 op_sel_hi:[1,0]\n"
    "v_pk_add_f32 v[36:37], v[36:37], 1.0 op_sel_hi:[1,0]\n"
    "v_pk_add_f32 v[38:39], v[38:39], 1.0 op_sel_hi:[1,0]\n"
    "v_rcp_f32 v32, v32\n"
    "v_rcp_f32 v33, v33\n"
    "v_rcp_f32 v34, v34\n"
    "v_rcp_f32 v35, v35\n"
    "v_rcp_f32 v36, v36\n"
    "v_rcp_f32 v37, v37\n"
    "v_rcp_f32 v38, v38\n"
    "v_rcp_f32 v39, v39\n"
    "v_pk_mul_f32 v[72:73], v[72:73], v[64:65]\n"
    "v_pk_mul_f32 v[74:75], v[74:75], v[66:67]\n"
    "v_pk_mul_f32 v[76:77], v[76:77], v[68:69]\n"
    "v_pk_mul_f32 v[78:79], v[78:79], v[70:71]\n"
    "v_pk_mul_f32 v[32:33], v[32:33], v[72:73]\n"
    "v_pk_mul_f32 v[34:35], v[34:35], v[74:75]\n"
    "v_pk_mul_f32 v[36:37], v[36:37], v[76:77]\n"
    "v_pk_mul_f32 v[38:39], v[38:39], v[78:79]\n"
    "v_cvt_pk_bf16_f32 v8, v32, v33\n"
    "v_cvt_pk_bf16_f32 v10, v36, v37\n"
    "v_cvt_pk_bf16_f32 v9, v34, v35\n"
    "v_cvt_pk_bf16_f32 v11, v38, v39\n"
    "v_permlane16_swap_b32 v8, v10\n"
    "s_nop 0\n"
    "v_permlane16_swap_b32 v9, v11\n"
    "buffer_store_dwordx4 v[8:11], v1, s[28:31], s96 offen offset:0 sc1\n"
    "v_pk_mul_f32 v[32:33], v[80:81], v[80:81]\n"
    "v_pk_mul_f32 v[34:35], v[82:83], v[82:83]\n"
    "v_pk_mul_f32 v[36:37], v[84:85], v[84:85]\n"
    "v_pk_mul_f32 v[38:39], v[86:87], v[86:87]\n"
    "v_pk_fma_f32 v[32:33], v[32:33], s[44:45], v[40:41] op_sel_hi:[1,0,0]\n"
    "v_pk_fma_f32 v[34:35], v[34:35], s[44:45], v[40:41] op_sel_hi:[1,0,0]\n"
    "v_pk_fma_f32 v[36:37], v[36:37], s[44:45], v[40:41] op_sel_hi:[1,0,0]\n"
    "v_pk_fma_f32 v[38:39], v[38:39], s[44:45], v[40:41] op_sel_hi:[1,0,0]\n"
    "v_pk_mul_f32 v[32:33], v[32:33], v[80:81]\n"
    "v_pk_mul_f32 v[34:35], v[34:35], v[82:83]\n"
    "v_pk_mul_f32 v[36:37], v[36:37], v[84:85]\n"
    "v_pk_mul_f32 v[38:39], v[38:39], v[86:87]\n"
    "v_exp_f32 v32, v32\n"
    "v_exp_f32 v33, v33\n"
    "v_exp_f32 v34, v34\n"
    "v_exp_f32 v35, v35\n"
    "v_exp_f32 v36, v36\n"
    "v_exp_f32 v37, v37\n"
    "v_exp_f32 v38, v38\n"
    "v_exp_f32 v39, v39\n"
    "v_pk_add_f32 v[32:33], v[32:33], 1.0 op_sel_hi:[1,0]\n"
    "v_pk_add_f32 v[34:35], v[34:35], 1.0 op_sel_hi:[1,0]\n"
    "v_pk_add_f32 v[36:37], v[36:37], 1.0 op_sel_hi:[1,0]\n"
    "v_pk_add_f32 v[38:39], v[38:39], 1.0 op_sel_hi:[1,0]\n"
    "v_rcp_f32 v32, v32\n"
    "v_rcp_f32 v33, v33\n"
    "v_rcp_f32 v34, v34\n"
    "v_rcp_f32 v35, v35\n"
    "v_rcp_f32 v36, v36\n"
    "v_rcp_f32 v37, v37\n"
    "v_rcp_f32 v38, v38\n"
    "v_rcp_f32 v39, v39\n"
    "v_pk_mul_f32 v[88:89], v[88:89], v[80:81]\n"
    "v_pk_mul_f32 v[90:91], v[90:91], v[82:83]\n"
    "v_pk_mul_f32 v[92:93], v[92:93], v[84:85]\n"
    "v_pk_mul_f32 v[94:95], v[94:95], v[86:87]\n"
    "v_pk_mul_f32 v[32:33], v[32:33], v[88:89]\n"
    "v_pk_mul_f32 v[34:35], v[34:35], v[90:91]\n"
    "v_pk_mul_f32 v[36:37], v[36:37], v[92:93]\n"
    "v_pk_mul_f32 v[38:39], v[38:39], v[94:95]\n"
    "v_cvt_pk_bf16_f32 v12, v32, v33\n"
    "v_cvt_pk_bf16_f32 v14, v36, v37\n"
    "v_cvt_pk_bf16_f32 v13, v34, v35\n"
    "v_cvt_pk_bf16_f32 v15, v38, v39\n"
    "v_permlane16_swap_b32 v12, v14\n"
    "s_nop 0\n"
    "v_permlane16_swap_b32 v13, v15\n"
    "buffer_store_dwordx4 v[12:15], v1, s[28:31], s96 offen offset:1024 sc1\n"
    "s_waitcnt vmcnt(6)\n"
    ".Ljoin:\n"
    "s_waitcnt lgkmcnt(0)\n"
    "s_barrier\n"
    "s_setprio 3\n"
    "ds_read_b128 v[164:167], v1 offset:0\n"
    "s_add_u32 m0, s79, 0x10000\n"
    "s_add_u32 s78, s78, 0x8000\n"
    "buffer_load_dwordx4 v1, s[28:31], s78 offen offset:0 sc1 lds\n"
    "buffer_load_dwordx4 v1, s[28:31], s78 offen offset:1024 sc1 lds\n"
    "buffer_load_dwordx4 v1, s[28:31], s78 offen offset:2048 sc1 lds\n"
    "buffer_load_dwordx4 v1, s[28:31], s78 offen offset:3072 sc1 lds\n"
    "s_waitcnt lgkmcnt(0)\n"
    "v_mfma_f32_32x32x16_bf16 v[224:239], v[164:167], v[160:163], 0\n"
    "s_nop 11\n"
    "v_max3_f32 v6, v224, v225, v226\n"
    "v_max3_f32 v7, v227, v228, v229\n"
    "v_max3_f32 v6, v6, v230, v231\n"
    "v_max3_f32 v7, v7, v232, v233\n"
    "v_max3_f32 v6, v6, v234, v235\n"
    "v_max3_f32 v7, v7, v236, v237\n"
    "v_max3_f32 v6, v6, v238, v239\n"
    "v_max_f32 v6, v6, v7\n"
    "v_mov_b32 v7, v6\n"
    "s_nop 1\n"
    "v_permlane32_swap_b32 v6, v7\n"
    "v_min_f32 v192, -v6, -v7\n"
    "v_mov_b32 v193, v192\n"
    "v_mov_b64 v[194:195], v[192:193]\n"
    "v_mov_b64 v[196:197], v[192:193]\n"
    "v_mov_b64 v[198:199], v[192:193]\n"
    "v_mov_b64 v[200:201], v[192:193]\n"
    "v_mov_b64 v[202:203], v[192:193]\n"
    "v_mov_b64 v[204:205], v[192:193]\n"
    "v_mov_b64 v[206:207], v[192:193]\n"
    "s_nop 1\n"
    "v_mfma_f32_32x32x16_bf16 v[208:223], v[164:167], v[160:163], v[192:207]\n"
    "ds_read_b128 v[164:167], v1 offset:2048\n"
    "s_setprio 2\n"
    "s_nop 9\n"
    "v_exp_f32 v208, v208\n"
    "v_exp_f32 v209, v209\n"
    "v_exp_f32 v210, v210\n"
    "s_mov_b64 exec, s[42:43]\n"
    "ds_read_b128 v[168:171], v1 offset:1024\n"
    "ds_read_b128 v[172:175], v1 offset:1280\n"
    "s_mov_b64 exec, -1\n"
    "v_exp_f32 v211, v211\n"
    "v_exp_f32 v212, v212\n"
    "s_waitcnt lgkmcnt(2)\n"
    "v_mfma_f32_32x32x16_bf16 v[224:239], v[164:167], v[160:163], v[192:207]\n"
    "ds_read_b128 v[164:167], v1 offset:4096\n"
    "v_exp_f32 v213, v213\n"
    "v_exp_f32 v214, v214\n"
    "v_exp_f32 v215, v215\n"
    "v_exp_f32 v216, v216\n"
    "v_exp_f32 v217, v217\n"
    "v_exp_f32 v218, v218\n"
    "v_exp_f32 v219, v219\n"
    "v_exp_f32 v220, v220\n"
    "v_exp_f32 v221, v221\n"
    "v_exp_f32 v222, v222\n"
    "v_exp_f32 v223, v223\n"
    "s_waitcnt lgkmcnt(0)\n"
    "v_smfmac_f32_32x32x32_bf16 v[176:191], v[168:171], v[208:215], v129\n"
    "v_exp_f32 v224, v224\n"
    "v_exp_f32 v225, v225\n"
    "v_exp_f32 v226, v226\n"
    "v_smfmac_f32_32x32x32_bf16 v[176:191], v[172:175], v[216:223], v129\n"
    "s_mov_b64 exec, s[42:43]\n"
    "ds_read_b128 v[168:171], v1 offset:3072\n"
    "ds_read_b128 v[172:175], v1 offset:3328\n"
    "s_mov_b64 exec, -1\n"
    "v_exp_f32 v227, v227\n"
    "v_exp_f32 v228, v228\n"
    "v_mfma_f32_32x32x16_bf16 v[208:223], v[164:167], v[160:163], v[192:207]\n"
    "ds_read_b128 v[164:167], v1 offset:6144\n"
    "v_exp_f32 v229, v229\n"
    "v_exp_f32 v230, v230\n"
    "v_exp_f32 v231, v231\n"
    "v_exp_f32 v232, v232\n"
    "v_exp_f32 v233, v233\n"
    "v_exp_f32 v234, v234\n"
    "v_exp_f32 v235, v235\n"
    "v_exp_f32 v236, v236\n"
    "v_exp_f32 v237, v237\n"
    "v_exp_f32 v238, v238\n"
    "v_exp_f32 v239, v239\n"
    "s_waitcnt lgkmcnt(0)\n"
    "v_smfmac_f32_32x32x32_bf16 v[176:191], v[168:171], v[224:231], v129\n"
    "v_exp_f32 v208, v208\n"
    "v_exp_f32 v209, v209\n"
    "v_exp_f32 v210, v210\n"
    "v_smfmac_f32_32x32x32_bf16 v[176:191], v[172:175], v[232:239], v129\n"
    "s_mov_b64 exec, s[42:43]\n"
    "ds_read_b128 v[168:171], v1 offset:5120\n"
    "ds_read_b128 v[172:175], v1 offset:5376\n"
    "s_mov_b64 exec, -1\n"
    "v_exp_f32 v211, v211\n"
    "v_exp_f32 v212, v212\n"
    "v_mfma_f32_32x32x16_bf16 v[224:239], v[164:167], v[160:163], v[192:207]\n"
    "ds_read_b128 v[164:167], v1 offset:8192\n"
    "v_exp_f32 v213, v213\n"
    "v_exp_f32 v214, v214\n"
    "v_exp_f32 v215, v215\n"
    "v_exp_f32 v216, v216\n"
    "v_exp_f32 v217, v217\n"
    "v_exp_f32 v218, v218\n"
    "v_exp_f32 v219, v219\n"
    "v_exp_f32 v220, v220\n"
    "v_exp_f32 v221, v221\n"
    "v_exp_f32 v222, v222\n"
    "v_exp_f32 v223, v223\n"
    "s_waitcnt lgkmcnt(0)\n"
    "v_smfmac_f32_32x32x32_bf16 v[176:191], v[168:171], v[208:215], v129\n"
    "v_exp_f32 v224, v224\n"
    "v_exp_f32 v225, v225\n"
    "v_exp_f32 v226, v226\n"
    "v_smfmac_f32_32x32x32_bf16 v[176:191], v[172:175], v[216:223], v129\n"
    "s_mov_b64 exec, s[42:43]\n"
    "ds_read_b128 v[168:171], v1 offset:7168\n"
    "ds_read_b128 v[172:175], v1 offset:7424\n"
    "s_mov_b64 exec, -1\n"
    "v_exp_f32 v227, v227\n"
    "v_exp_f32 v228, v228\n"
    "v_mfma_f32_32x32x16_bf16 v[208:223], v[164:167], v[160:163], v[192:207]\n"
    "ds_read_b128 v[164:167], v1 offset:10240\n"
    "v_exp_f32 v229, v229\n"
    "v_exp_f32 v230, v230\n"
    "v_exp_f32 v231, v231\n"
    "v_exp_f32 v232, v232\n"
    "v_exp_f32 v233, v233\n"
    "v_exp_f32 v234, v234\n"
    "v_exp_f32 v235, v235\n"
    "v_exp_f32 v236, v236\n"
    "v_exp_f32 v237, v237\n"
    "v_exp_f32 v238, v238\n"
    "v_exp_f32 v239, v239\n"
    "s_setprio 1\n"
    "s_waitcnt lgkmcnt(0)\n"
    "v_smfmac_f32_32x32x32_bf16 v[176:191], v[168:171], v[224:231], v129\n"
    "v_exp_f32 v208, v208\n"
    "v_exp_f32 v209, v209\n"
    "v_exp_f32 v210, v210\n"
    "v_smfmac_f32_32x32x32_bf16 v[176:191], v[172:175], v[232:239], v129\n"
    "s_mov_b64 exec, s[42:43]\n"
    "ds_read_b128 v[168:171], v1 offset:9216\n"
    "ds_read_b128 v[172:175], v1 offset:9472\n"
    "s_mov_b64 exec, -1\n"
    "v_exp_f32 v211, v211\n"
    "v_exp_f32 v212, v212\n"
    "v_mfma_f32_32x32x16_bf16 v[224:239], v[164:167], v[160:163], v[192:207]\n"
    "ds_read_b128 v[164:167], v1 offset:12288\n"
    "v_exp_f32 v213, v213\n"
    "v_exp_f32 v214, v214\n"
    "v_exp_f32 v215, v215\n"
    "v_exp_f32 v216, v216\n"
    "v_exp_f32 v217, v217\n"
    "s_add_u32 m0, s79, 0x18000\n"
    "s_add_u32 s78, s78, 0x8000\n"
    "buffer_load_dwordx4 v1, s[28:31], s78 offen offset:0 sc1 lds\n"
    "buffer_load_dwordx4 v1, s[28:31], s78 offen offset:1024 sc1 lds\n"
    "buffer_load_dwordx4 v1, s[28:31], s78 offen offset:2048 sc1 lds\n"
    "buffer_load_dwordx4 v1, s[28:31], s78 offen offset:3072 sc1 lds\n"
    "v_exp_f32 v218, v218\n"
    "v_exp_f32 v219, v219\n"
    "v_exp_f32 v220, v220\n"
    "v_exp_f32 v221, v221\n"
    "v_exp_f32 v222, v222\n"
    "v_exp_f32 v223, v223\n"
    "s_waitcnt lgkmcnt(0)\n"
    "v_smfmac_f32_32x32x32_bf16 v[176:191], v[168:171], v[208:215], v129\n"
    "v_exp_f32 v224, v224\n"
    "v_exp_f32 v225, v225\n"
    "v_exp_f32 v226, v226\n"
    "v_smfmac_f32_32x32x32_bf16 v[176:191], v[172:175], v[216:223], v129\n"
    "s_mov_b64 exec, s[42:43]\n"
    "ds_read_b128 v[168:171], v1 offset:11264\n"
    "ds_read_b128 v[172:175], v1 offset:11520\n"
    "s_mov_b64 exec, -1\n"
    "v_exp_f32 v227, v227\n"
    "v_exp_f32 v228, v228\n"
    "v_mfma_f32_32x32x16_bf16 v[208:223], v[164:167], v[160:163], v[192:207]\n"
    "ds_read_b128 v[164:167], v1 offset:14336\n"
    "v_exp_f32 v229, v229\n"
    "v_exp_f32 v230, v230\n"
    "v_exp_f32 v231, v231\n"
    "v_exp_f32 v232, v232\n"
    "v_exp_f32 v233, v233\n"
    "v_exp_f32 v234, v234\n"
    "v_exp_f32 v235, v235\n"
    "v_exp_f32 v236, v236\n"
    "v_exp_f32 v237, v237\n"
    "v_exp_f32 v238, v238\n"
    "v_exp_f32 v239, v239\n"
    "s_waitcnt lgkmcnt(0)\n"
    "v_smfmac_f32_32x32x32_bf16 v[176:191], v[168:171], v[224:231], v129\n"
    "v_exp_f32 v208, v208\n"
    "v_exp_f32 v209, v209\n"
    "v_exp_f32 v210, v210\n"
    "v_smfmac_f32_32x32x32_bf16 v[176:191], v[172:175], v[232:239], v129\n"
    "s_mov_b64 exec, s[42:43]\n"
    "ds_read_b128 v[168:171], v1 offset:13312\n"
    "ds_read_b128 v[172:175], v1 offset:13568\n"
    "s_mov_b64 exec, -1\n"
    "v_exp_f32 v211, v211\n"
    "v_exp_f32 v212, v212\n"
    "v_mfma_f32_32x32x16_bf16 v[224:239], v[164:167], v[160:163], v[192:207]\n"
    "ds_read_b128 v[164:167], v1 offset:16384\n"
    "v_exp_f32 v213, v213\n"
    "v_exp_f32 v214, v214\n"
    "v_exp_f32 v215, v215\n"
    "v_exp_f32 v216, v216\n"
    "v_exp_f32 v217, v217\n"
    "v_exp_f32 v218, v218\n"
    "v_exp_f32 v219, v219\n"
    "v_exp_f32 v220, v220\n"
    "v_exp_f32 v221, v221\n"
    "v_exp_f32 v222, v222\n"
    "v_exp_f32 v223, v223\n"
    "s_waitcnt lgkmcnt(0)\n"
    "v_smfmac_f32_32x32x32_bf16 v[176:191], v[168:171], v[208:215], v129\n"
    "v_exp_f32 v224, v224\n"
    "v_exp_f32 v225, v225\n"
    "v_exp_f32 v226, v226\n"
    "v_smfmac_f32_32x32x32_bf16 v[176:191], v[172:175], v[216:223], v129\n"
    "s_mov_b64 exec, s[42:43]\n"
    "ds_read_b128 v[168:171], v1 offset:15360\n"
    "ds_read_b128 v[172:175], v1 offset:15616\n"
    "s_mov_b64 exec, -1\n"
    "v_exp_f32 v227, v227\n"
    "v_exp_f32 v228, v228\n"
    "v_mfma_f32_32x32x16_bf16 v[208:223], v[164:167], v[160:163], v[192:207]\n"
    "ds_read_b128 v[164:167], v1 offset:18432\n"
    "v_exp_f32 v229, v229\n"
    "v_exp_f32 v230, v230\n"
    "v_exp_f32 v231, v231\n"
    "v_exp_f32 v232, v232\n"
    "v_exp_f32 v233, v233\n"
    "v_exp_f32 v234, v234\n"
    "v_exp_f32 v235, v235\n"
    "v_exp_f32 v236, v236\n"
    "v_exp_f32 v237, v237\n"
    "v_exp_f32 v238, v238\n"
    "v_exp_f32 v239, v239\n"
    "s_setprio 0\n"
    "s_waitcnt lgkmcnt(0)\n"
    "v_smfmac_f32_32x32x32_bf16 v[176:191], v[168:171], v[224:231], v129\n"
    "v_exp_f32 v208, v208\n"
    "v_exp_f32 v209, v209\n"
    "v_exp_f32 v210, v210\n"
    "v_smfmac_f32_32x32x32_bf16 v[176:191], v[172:175], v[232:239], v129\n"
    "s_mov_b64 exec, s[42:43]\n"
    "ds_read_b128 v[168:171], v1 offset:17408\n"
    "ds_read_b128 v[172:175], v1 offset:17664\n"
    "s_mov_b64 exec, -1\n"
    "v_exp_f32 v211, v211\n"
    "v_exp_f32 v212, v212\n"
    "v_mfma_f32_32x32x16_bf16 v[224:239], v[164:167], v[160:163], v[192:207]\n"
    "ds_read_b128 v[164:167], v1 offset:20480\n"
    "v_exp_f32 v213, v213\n"
    "v_exp_f32 v214, v214\n"
    "v_exp_f32 v215, v215\n"
    "v_exp_f32 v216, v216\n"
    "v_exp_f32 v217, v217\n"
    "v_exp_f32 v218, v218\n"
    "v_exp_f32 v219, v219\n"
    "v_exp_f32 v220, v220\n"
    "v_exp_f32 v221, v221\n"
    "v_exp_f32 v222, v222\n"
    "v_exp_f32 v223, v223\n"
    "s_waitcnt lgkmcnt(0)\n"
    "v_smfmac_f32_32x32x32_bf16 v[176:191], v[168:171], v[208:215], v129\n"
    "v_exp_f32 v224, v224\n"
    "v_exp_f32 v225, v225\n"
    "v_exp_f32 v226, v226\n"
    "v_smfmac_f32_32x32x32_bf16 v[176:191], v[172:175], v[216:223], v129\n"
    "s_mov_b64 exec, s[42:43]\n"
    "ds_read_b128 v[168:171], v1 offset:19456\n"
    "ds_read_b128 v[172:175], v1 offset:19712\n"
    "s_mov_b64 exec, -1\n"
    "v_exp_f32 v227, v227\n"
    "v_exp_f32 v228, v228\n"
    "v_mfma_f32_32x32x16_bf16 v[208:223], v[164:167], v[160:163], v[192:207]\n"
    "ds_read_b128 v[164:167], v1 offset:22528\n"
    "v_exp_f32 v229, v229\n"
    "v_exp_f32 v230, v230\n"
    "v_exp_f32 v231, v231\n"
    "v_exp_f32 v232, v232\n"
    "v_exp_f32 v233, v233\n"
    "v_exp_f32 v234, v234\n"
    "v_exp_f32 v235, v235\n"
    "v_exp_f32 v236, v236\n"
    "v_exp_f32 v237, v237\n"
    "v_exp_f32 v238, v238\n"
    "v_exp_f32 v239, v239\n"
    "s_waitcnt lgkmcnt(0)\n"
    "v_smfmac_f32_32x32x32_bf16 v[176:191], v[168:171], v[224:231], v129\n"
    "v_exp_f32 v208, v208\n"
    "v_exp_f32 v209, v209\n"
    "v_exp_f32 v210, v210\n"
    "v_smfmac_f32_32x32x32_bf16 v[176:191], v[172:175], v[232:239], v129\n"
    "s_mov_b64 exec, s[42:43]\n"
    "ds_read_b128 v[168:171], v1 offset:21504\n"
    "ds_read_b128 v[172:175], v1 offset:21760\n"
    "s_mov_b64 exec, -1\n"
    "v_exp_f32 v211, v211\n"
    "v_exp_f32 v212, v212\n"
    "v_mfma_f32_32x32x16_bf16 v[224:239], v[164:167], v[160:163], v[192:207]\n"
    "ds_read_b128 v[164:167], v1 offset:24576\n"
    "v_exp_f32 v213, v213\n"
    "v_exp_f32 v214, v214\n"
    "v_exp_f32 v215, v215\n"
    "v_exp_f32 v216, v216\n"
    "v_exp_f32 v217, v217\n"
    "v_exp_f32 v218, v218\n"
    "v_exp_f32 v219, v219\n"
    "v_exp_f32 v220, v220\n"
    "v_exp_f32 v221, v221\n"
    "v_exp_f32 v222, v222\n"
    "v_exp_f32 v223, v223\n"
    "s_waitcnt lgkmcnt(0)\n"
    "v_smfmac_f32_32x32x32_bf16 v[176:191], v[168:171], v[208:215], v129\n"
    "v_exp_f32 v224, v224\n"
    "v_exp_f32 v225, v225\n"
    "v_exp_f32 v226, v226\n"
    "v_smfmac_f32_32x32x32_bf16 v[176:191], v[172:175], v[216:223], v129\n"
    "s_mov_b64 exec, s[42:43]\n"
    "ds_read_b128 v[168:171], v1 offset:23552\n"
    "ds_read_b128 v[172:175], v1 offset:23808\n"
    "s_mov_b64 exec, -1\n"
    "v_exp_f32 v227, v227\n"
    "v_exp_f32 v228, v228\n"
    "v_mfma_f32_32x32x16_bf16 v[208:223], v[164:167], v[160:163], v[192:207]\n"
    "ds_read_b128 v[164:167], v1 offset:26624\n"
    "v_exp_f32 v229, v229\n"
    "v_exp_f32 v230, v230\n"
    "v_exp_f32 v231, v231\n"
    "v_exp_f32 v232, v232\n"
    "v_exp_f32 v233, v233\n"
    "v_exp_f32 v234, v234\n"
    "v_exp_f32 v235, v235\n"
    "v_exp_f32 v236, v236\n"
    "v_exp_f32 v237, v237\n"
    "v_exp_f32 v238, v238\n"
    "v_exp_f32 v239, v239\n"
    "s_waitcnt vmcnt(0)\n"
    "s_barrier\n"
    "s_add_u32 s95, s77, 0x8000\n"
    "s_cmp_eq_u32 s48, 0\n"
    "s_cbranch_scc0 .Lskip_17\n"
    "s_mov_b64 exec, 1\n"
    "buffer_store_dwordx2 v[4:5], off, s[28:31], s95 sc1\n"
    "s_mov_b64 exec, -1\n"
    ".Lskip_17:\n"
    "s_setprio 3\n"
    "s_waitcnt lgkmcnt(0)\n"
    "v_smfmac_f32_32x32x32_bf16 v[176:191], v[168:171], v[224:231], v129\n"
    "v_exp_f32 v208, v208\n"
    "v_exp_f32 v209, v209\n"
    "v_exp_f32 v210, v210\n"
    "v_smfmac_f32_32x32x32_bf16 v[176:191], v[172:175], v[232:239], v129\n"
    "s_mov_b64 exec, s[42:43]\n"
    "ds_read_b128 v[168:171], v1 offset:25600\n"
    "ds_read_b128 v[172:175], v1 offset:25856\n"
    "s_mov_b64 exec, -1\n"
    "v_exp_f32 v211, v211\n"
    "v_exp_f32 v212, v212\n"
    "v_mfma_f32_32x32x16_bf16 v[224:239], v[164:167], v[160:163], v[192:207]\n"
    "ds_read_b128 v[164:167], v1 offset:28672\n"
    "v_exp_f32 v213, v213\n"
    "v_exp_f32 v214, v214\n"
    "v_exp_f32 v215, v215\n"
    "v_exp_f32 v216, v216\n"
    "v_exp_f32 v217, v217\n"
    "v_exp_f32 v218, v218\n"
    "v_exp_f32 v219, v219\n"
    "v_exp_f32 v220, v220\n"
    "v_exp_f32 v221, v221\n"
    "v_exp_f32 v222, v222\n"
    "v_exp_f32 v223, v223\n"
    "s_waitcnt lgkmcnt(0)\n"
    "v_smfmac_f32_32x32x32_bf16 v[176:191], v[168:171], v[208:215], v129\n"
    "v_exp_f32 v224, v224\n"
    "v_exp_f32 v225, v225\n"
    "v_exp_f32 v226, v226\n"
    "v_smfmac_f32_32x32x32_bf16 v[176:191], v[172:175], v[216:223], v129\n"
    "s_mov_b64 exec, s[42:43]\n"
    "ds_read_b128 v[168:171], v1 offset:27648\n"
    "ds_read_b128 v[172:175], v1 offset:27904\n"
    "s_mov_b64 exec, -1\n"
    "v_exp_f32 v227, v227\n"
    "v_exp_f32 v228, v228\n"
    "v_mfma_f32_32x32x16_bf16 v[208:223], v[164:167], v[160:163], v[192:207]\n"
    "ds_read_b128 v[164:167], v1 offset:30720\n"
    "v_exp_f32 v229, v229\n"
    "v_exp_f32 v230, v230\n"
    "v_exp_f32 v231, v231\n"
    "v_exp_f32 v232, v232\n"
    "v_exp_f32 v233, v233\n"
    "v_exp_f32 v234, v234\n"
    "v_exp_f32 v235, v235\n"
    "v_exp_f32 v236, v236\n"
    "v_exp_f32 v237, v237\n"
    "v_exp_f32 v238, v238\n"
    "v_exp_f32 v239, v239\n"
    "s_waitcnt lgkmcnt(0)\n"
    "v_smfmac_f32_32x32x32_bf16 v[176:191], v[168:171], v[224:231], v129\n"
    "v_exp_f32 v208, v208\n"
    "v_exp_f32 v209, v209\n"
    "v_exp_f32 v210, v210\n"
    "v_smfmac_f32_32x32x32_bf16 v[176:191], v[172:175], v[232:239], v129\n"
    "s_mov_b64 exec, s[42:43]\n"
    "ds_read_b128 v[168:171], v1 offset:29696\n"
    "ds_read_b128 v[172:175], v1 offset:29952\n"
    "s_mov_b64 exec, -1\n"
    "v_exp_f32 v211, v211\n"
    "v_exp_f32 v212, v212\n"
    "v_mfma_f32_32x32x16_bf16 v[224:239], v[164:167], v[160:163], v[192:207]\n"
    "ds_read_b128 v[164:167], v1 offset:32768\n"
    "v_exp_f32 v213, v213\n"
    "v_exp_f32 v214, v214\n"
    "v_exp_f32 v215, v215\n"
    "v_exp_f32 v216, v216\n"
    "v_exp_f32 v217, v217\n"
    "s_add_u32 m0, s79, 0x20000\n"
    "s_nop 0\n"
    "buffer_load_dwordx4 v130, s[20:23], s82 offen offset:0 lds\n"
    "v_exp_f32 v218, v218\n"
    "v_exp_f32 v219, v219\n"
    "v_exp_f32 v220, v220\n"
    "v_exp_f32 v221, v221\n"
    "v_exp_f32 v222, v222\n"
    "v_exp_f32 v223, v223\n"
    "s_waitcnt lgkmcnt(0)\n"
    "v_smfmac_f32_32x32x32_bf16 v[176:191], v[168:171], v[208:215], v129\n"
    "v_exp_f32 v224, v224\n"
    "v_exp_f32 v225, v225\n"
    "v_exp_f32 v226, v226\n"
    "v_smfmac_f32_32x32x32_bf16 v[176:191], v[172:175], v[216:223], v129\n"
    "s_mov_b64 exec, s[42:43]\n"
    "ds_read_b128 v[168:171], v1 offset:31744\n"
    "ds_read_b128 v[172:175], v1 offset:32000\n"
    "s_mov_b64 exec, -1\n"
    "v_exp_f32 v227, v227\n"
    "v_exp_f32 v228, v228\n"
    "v_mfma_f32_32x32x16_bf16 v[208:223], v[164:167], v[160:163], v[192:207]\n"
    "ds_read_b128 v[164:167], v1 offset:34816\n"
    "v_exp_f32 v229, v229\n"
    "v_exp_f32 v230, v230\n"
    "v_exp_f32 v231, v231\n"
    "v_exp_f32 v232, v232\n"
    "v_exp_f32 v233, v233\n"
    "buffer_load_dwordx4 v131, s[20:23], s82 offen offset:1024 lds\n"
    "v_exp_f32 v234, v234\n"
    "v_exp_f32 v235, v235\n"
    "v_exp_f32 v236, v236\n"
    "v_exp_f32 v237, v237\n"
    "v_exp_f32 v238, v238\n"
    "v_exp_f32 v239, v239\n"
    "s_waitcnt lgkmcnt(0)\n"
    "v_smfmac_f32_32x32x32_bf16 v[176:191], v[168:171], v[224:231], v129\n"
    "v_exp_f32 v208, v208\n"
    "v_exp_f32 v209, v209\n"
    "v_exp_f32 v210, v210\n"
    "v_smfmac_f32_32x32x32_bf16 v[176:191], v[172:175], v[232:239], v129\n"
    "s_mov_b64 exec, s[42:43]\n"
    "ds_read_b128 v[168:171], v1 offset:33792\n"
    "ds_read_b128 v[172:175], v1 offset:34048\n"
    "s_mov_b64 exec, -1\n"
    "v_exp_f32 v211, v211\n"
    "v_exp_f32 v212, v212\n"
    "v_mfma_f32_32x32x16_bf16 v[224:239], v[164:167], v[160:163], v[192:207]\n"
    "ds_read_b128 v[164:167], v1 offset:36864\n"
    "v_exp_f32 v213, v213\n"
    "v_exp_f32 v214, v214\n"
    "v_exp_f32 v215, v215\n"
    "v_exp_f32 v216, v216\n"
    "v_exp_f32 v217, v217\n"
    "buffer_load_dwordx4 v132, s[20:23], s82 offen offset:2048 lds\n"
    "v_exp_f32 v218, v218\n"
    "v_exp_f32 v219, v219\n"
    "v_exp_f32 v220, v220\n"
    "v_exp_f32 v221, v221\n"
    "v_exp_f32 v222, v222\n"
    "v_exp_f32 v223, v223\n"
    "s_waitcnt lgkmcnt(0)\n"
    "v_smfmac_f32_32x32x32_bf16 v[176:191], v[168:171], v[208:215], v129\n"
    "v_exp_f32 v224, v224\n"
    "v_exp_f32 v225, v225\n"
    "v_exp_f32 v226, v226\n"
    "v_smfmac_f32_32x32x32_bf16 v[176:191], v[172:175], v[216:223], v129\n"
    "s_mov_b64 exec, s[42:43]\n"
    "ds_read_b128 v[168:171], v1 offset:35840\n"
    "ds_read_b128 v[172:175], v1 offset:36096\n"
    "s_mov_b64 exec, -1\n"
    "v_exp_f32 v227, v227\n"
    "v_exp_f32 v228, v228\n"
    "v_mfma_f32_32x32x16_bf16 v[208:223], v[164:167], v[160:163], v[192:207]\n"
    "ds_read_b128 v[164:167], v1 offset:38912\n"
    "v_exp_f32 v229, v229\n"
    "v_exp_f32 v230, v230\n"
    "v_exp_f32 v231, v231\n"
    "v_exp_f32 v232, v232\n"
    "v_exp_f32 v233, v233\n"
    "buffer_load_dwordx4 v133, s[20:23], s82 offen offset:3072 lds\n"
    "v_exp_f32 v234, v234\n"
    "v_exp_f32 v235, v235\n"
    "v_exp_f32 v236, v236\n"
    "v_exp_f32 v237, v237\n"
    "v_exp_f32 v238, v238\n"
    "v_exp_f32 v239, v239\n"
    "s_waitcnt lgkmcnt(0)\n"
    "v_smfmac_f32_32x32x32_bf16 v[176:191], v[168:171], v[224:231], v129\n"
    "v_exp_f32 v208, v208\n"
    "v_exp_f32 v209, v209\n"
    "v_exp_f32 v210, v210\n"
    "v_smfmac_f32_32x32x32_bf16 v[176:191], v[172:175], v[232:239], v129\n"
    "s_mov_b64 exec, s[42:43]\n"
    "ds_read_b128 v[168:171], v1 offset:37888\n"
    "ds_read_b128 v[172:175], v1 offset:38144\n"
    "s_mov_b64 exec, -1\n"
    "v_exp_f32 v211, v211\n"
    "v_exp_f32 v212, v212\n"
    "v_mfma_f32_32x32x16_bf16 v[224:239], v[164:167], v[160:163], v[192:207]\n"
    "ds_read_b128 v[164:167], v1 offset:40960\n"
    "v_exp_f32 v213, v213\n"
    "v_exp_f32 v214, v214\n"
    "v_exp_f32 v215, v215\n"
    "v_exp_f32 v216, v216\n"
    "v_exp_f32 v217, v217\n"
    "v_exp_f32 v218, v218\n"
    "v_exp_f32 v219, v219\n"
    "v_exp_f32 v220, v220\n"
    "v_exp_f32 v221, v221\n"
    "v_exp_f32 v222, v222\n"
    "v_exp_f32 v223, v223\n"
    "s_waitcnt lgkmcnt(0)\n"
    "v_smfmac_f32_32x32x32_bf16 v[176:191], v[168:171], v[208:215], v129\n"
    "v_exp_f32 v224, v224\n"
    "v_exp_f32 v225, v225\n"
    "v_exp_f32 v226, v226\n"
    "v_smfmac_f32_32x32x32_bf16 v[176:191], v[172:175], v[216:223], v129\n"
    "s_mov_b64 exec, s[42:43]\n"
    "ds_read_b128 v[168:171], v1 offset:39936\n"
    "ds_read_b128 v[172:175], v1 offset:40192\n"
    "s_mov_b64 exec, -1\n"
    "v_exp_f32 v227, v227\n"
    "v_exp_f32 v228, v228\n"
    "v_mfma_f32_32x32x16_bf16 v[208:223], v[164:167], v[160:163], v[192:207]\n"
    "ds_read_b128 v[164:167], v1 offset:43008\n"
    "v_exp_f32 v229, v229\n"
    "v_exp_f32 v230, v230\n"
    "v_exp_f32 v231, v231\n"
    "v_exp_f32 v232, v232\n"
    "v_exp_f32 v233, v233\n"
    "v_exp_f32 v234, v234\n"
    "v_exp_f32 v235, v235\n"
    "v_exp_f32 v236, v236\n"
    "v_exp_f32 v237, v237\n"
    "v_exp_f32 v238, v238\n"
    "v_exp_f32 v239, v239\n"
    "s_waitcnt lgkmcnt(0)\n"
    "v_smfmac_f32_32x32x32_bf16 v[176:191], v[168:171], v[224:231], v129\n"
    "v_exp_f32 v208, v208\n"
    "v_exp_f32 v209, v209\n"
    "v_exp_f32 v210, v210\n"
    "v_smfmac_f32_32x32x32_bf16 v[176:191], v[172:175], v[232:239], v129\n"
    "s_mov_b64 exec, s[42:43]\n"
    "ds_read_b128 v[168:171], v1 offset:41984\n"
    "ds_read_b128 v[172:175], v1 offset:42240\n"
    "s_mov_b64 exec, -1\n"
    "v_exp_f32 v211, v211\n"
    "v_exp_f32 v212, v212\n"
    "v_mfma_f32_32x32x16_bf16 v[224:239], v[164:167], v[160:163], v[192:207]\n"
    "ds_read_b128 v[164:167], v1 offset:45056\n"
    "v_exp_f32 v213, v213\n"
    "v_exp_f32 v214, v214\n"
    "v_exp_f32 v215, v215\n"
    "v_exp_f32 v216, v216\n"
    "v_exp_f32 v217, v217\n"
    "buffer_load_dwordx2 v[138:139], v135, s[28:31], 0 offen sc1\n"
    "v_exp_f32 v218, v218\n"
    "v_exp_f32 v219, v219\n"
    "v_exp_f32 v220, v220\n"
    "v_exp_f32 v221, v221\n"
    "v_exp_f32 v222, v222\n"
    "v_exp_f32 v223, v223\n"
    "s_waitcnt lgkmcnt(0)\n"
    "v_smfmac_f32_32x32x32_bf16 v[176:191], v[168:171], v[208:215], v129\n"
    "v_exp_f32 v224, v224\n"
    "v_exp_f32 v225, v225\n"
    "v_exp_f32 v226, v226\n"
    "v_smfmac_f32_32x32x32_bf16 v[176:191], v[172:175], v[216:223], v129\n"
    "s_mov_b64 exec, s[42:43]\n"
    "ds_read_b128 v[168:171], v1 offset:44032\n"
    "ds_read_b128 v[172:175], v1 offset:44288\n"
    "s_mov_b64 exec, -1\n"
    "v_exp_f32 v227, v227\n"
    "v_exp_f32 v228, v228\n"
    "v_mfma_f32_32x32x16_bf16 v[208:223], v[164:167], v[160:163], v[192:207]\n"
    "ds_read_b128 v[164:167], v1 offset:47104\n"
    "v_exp_f32 v229, v229\n"
    "v_exp_f32 v230, v230\n"
    "v_exp_f32 v231, v231\n"
    "v_exp_f32 v232, v232\n"
    "v_exp_f32 v233, v233\n"
    "v_exp_f32 v234, v234\n"
    "v_exp_f32 v235, v235\n"
    "v_exp_f32 v236, v236\n"
    "v_exp_f32 v237, v237\n"
    "v_exp_f32 v238, v238\n"
    "v_exp_f32 v239, v239\n"
    "s_waitcnt lgkmcnt(0)\n"
    "v_smfmac_f32_32x32x32_bf16 v[176:191], v[168:171], v[224:231], v129\n"
    "v_exp_f32 v208, v208\n"
    "v_exp_f32 v209, v209\n"
    "v_exp_f32 v210, v210\n"
    "v_smfmac_f32_32x32x32_bf16 v[176:191], v[172:175], v[232:239], v129\n"
    "s_mov_b64 exec, s[42:43]\n"
    "ds_read_b128 v[168:171], v1 offset:46080\n"
    "ds_read_b128 v[172:175], v1 offset:46336\n"
    "s_mov_b64 exec, -1\n"
    "v_exp_f32 v211, v211\n"
    "v_exp_f32 v212, v212\n"
    "v_mfma_f32_32x32x16_bf16 v[224:239], v[164:167], v[160:163], v[192:207]\n"
    "ds_read_b128 v[164:167], v1 offset:49152\n"
    "v_exp_f32 v213, v213\n"
    "v_exp_f32 v214, v214\n"
    "v_exp_f32 v215, v215\n"
    "v_exp_f32 v216, v216\n"
    "v_exp_f32 v217, v217\n"
    "v_exp_f32 v218, v218\n"
    "v_exp_f32 v219, v219\n"
    "v_exp_f32 v220, v220\n"
    "v_exp_f32 v221, v221\n"
    "v_exp_f32 v222, v222\n"
    "v_exp_f32 v223, v223\n"
    "s_waitcnt lgkmcnt(0)\n"
    "v_smfmac_f32_32x32x32_bf16 v[176:191], v[168:171], v[208:215], v129\n"
    "v_exp_f32 v224, v224\n"
    "v_exp_f32 v225, v225\n"
    "v_exp_f32 v226, v226\n"
    "v_smfmac_f32_32x32x32_bf16 v[176:191], v[172:175], v[216:223], v129\n"
    "s_mov_b64 exec, s[42:43]\n"
    "ds_read_b128 v[168:171], v1 offset:48128\n"
    "ds_read_b128 v[172:175], v1 offset:48384\n"
    "s_mov_b64 exec, -1\n"
    "v_exp_f32 v227, v227\n"
    "v_exp_f32 v228, v228\n"
    "v_mfma_f32_32x32x16_bf16 v[208:223], v[164:167], v[160:163], v[192:207]\n"
    "ds_read_b128 v[164:167], v1 offset:51200\n"
    "v_exp_f32 v229, v229\n"
    "v_exp_f32 v230, v230\n"
    "v_exp_f32 v231, v231\n"
    "v_exp_f32 v232, v232\n"
    "v_exp_f32 v233, v233\n"
    "s_waitcnt vmcnt(0)\n"
    "v_cmp_ne_u64 vcc, s[40:41], v[138:139]\n"
    "s_mov_b32 s94, 0x20000\n"
    ".Lpoll_18:\n"
    "s_cbranch_vccz .Ldone_19\n"
    "s_sleep 1\n"
    "buffer_load_dwordx2 v[138:139], v135, s[28:31], 0 offen sc1\n"
    "s_waitcnt vmcnt(0)\n"
    "v_cmp_ne_u64 vcc, s[40:41], v[138:139]\n"
    "s_sub_u32 s94, s94, 1\n"
    "s_cmp_lg_u32 s94, 0\n"
    "s_cbranch_scc1 .Lpoll_18\n"
    ".Ldone_19:\n"
    "v_exp_f32 v234, v234\n"
    "v_exp_f32 v235, v235\n"
    "v_exp_f32 v236, v236\n"
    "v_exp_f32 v237, v237\n"
    "v_exp_f32 v238, v238\n"
    "v_exp_f32 v239, v239\n"
    "s_setprio 2\n"
    "s_waitcnt lgkmcnt(0)\n"
    "v_smfmac_f32_32x32x32_bf16 v[176:191], v[168:171], v[224:231], v129\n"
    "v_exp_f32 v208, v208\n"
    "v_exp_f32 v209, v209\n"
    "v_exp_f32 v210, v210\n"
    "v_smfmac_f32_32x32x32_bf16 v[176:191], v[172:175], v[232:239], v129\n"
    "s_mov_b64 exec, s[42:43]\n"
    "ds_read_b128 v[168:171], v1 offset:50176\n"
    "ds_read_b128 v[172:175], v1 offset:50432\n"
    "s_mov_b64 exec, -1\n"
    "v_exp_f32 v211, v211\n"
    "v_exp_f32 v212, v212\n"
    "v_mfma_f32_32x32x16_bf16 v[224:239], v[164:167], v[160:163], v[192:207]\n"
    "ds_read_b128 v[164:167], v1 offset:53248\n"
    "v_exp_f32 v213, v213\n"
    "v_exp_f32 v214, v214\n"
    "v_exp_f32 v215, v215\n"
    "v_exp_f32 v216, v216\n"
    "v_exp_f32 v217, v217\n"
    "buffer_load_dwordx4 v[8:11], v134, s[28:31], s86 offen offset:0 sc1\n"
    "v_exp_f32 v218, v218\n"
    "v_exp_f32 v219, v219\n"
    "v_exp_f32 v220, v220\n"
    "v_exp_f32 v221, v221\n"
    "v_exp_f32 v222, v222\n"
    "v_exp_f32 v223, v223\n"
    "s_waitcnt lgkmcnt(0)\n"
    "v_smfmac_f32_32x32x32_bf16 v[176:191], v[168:171], v[208:215], v129\n"
    "v_exp_f32 v224, v224\n"
    "v_exp_f32 v225, v225\n"
    "v_exp_f32 v226, v226\n"
    "v_smfmac_f32_32x32x32_bf16 v[176:191], v[172:175], v[216:223], v129\n"
    "s_mov_b64 exec, s[42:43]\n"
    "ds_read_b128 v[168:171], v1 offset:52224\n"
    "ds_read_b128 v[172:175], v1 offset:52480\n"
    "s_mov_b64 exec, -1\n"
    "v_exp_f32 v227, v227\n"
    "v_exp_f32 v228, v228\n"
    "v_mfma_f32_32x32x16_bf16 v[208:223], v[164:167], v[160:163], v[192:207]\n"
    "ds_read_b128 v[164:167], v1 offset:55296\n"
    "v_exp_f32 v229, v229\n"
    "v_exp_f32 v230, v230\n"
    "v_exp_f32 v231, v231\n"
    "v_exp_f32 v232, v232\n"
    "v_exp_f32 v233, v233\n"
    "buffer_load_dwordx4 v[12:15], v134, s[28:31], s86 offen offset:256 sc1\n"
    "v_exp_f32 v234, v234\n"
    "v_exp_f32 v235, v235\n"
    "v_exp_f32 v236, v236\n"
    "v_exp_f32 v237, v237\n"
    "v_exp_f32 v238, v238\n"
    "v_exp_f32 v239, v239\n"
    "s_waitcnt lgkmcnt(0)\n"
    "v_smfmac_f32_32x32x32_bf16 v[176:191], v[168:171], v[224:231], v129\n"
    "v_exp_f32 v208, v208\n"
    "v_exp_f32 v209, v209\n"
    "v_exp_f32 v210, v210\n"
    "v_smfmac_f32_32x32x32_bf16 v[176:191], v[172:175], v[232:239], v129\n"
    "s_mov_b64 exec, s[42:43]\n"
    "ds_read_b128 v[168:171], v1 offset:54272\n"
    "ds_read_b128 v[172:175], v1 offset:54528\n"
    "s_mov_b64 exec, -1\n"
    "v_exp_f32 v211, v211\n"
    "v_exp_f32 v212, v212\n"
    "v_mfma_f32_32x32x16_bf16 v[224:239], v[164:167], v[160:163], v[192:207]\n"
    "ds_read_b128 v[164:167], v1 offset:57344\n"
    "v_exp_f32 v213, v213\n"
    "v_exp_f32 v214, v214\n"
    "v_exp_f32 v215, v215\n"
    "v_exp_f32 v216, v216\n"
    "v_exp_f32 v217, v217\n"
    "buffer_load_dwordx4 v[16:19], v134, s[28:31], s86 offen offset:2048 sc1\n"
    "v_exp_f32 v218, v218\n"
    "v_exp_f32 v219, v219\n"
    "v_exp_f32 v220, v220\n"
    "v_exp_f32 v221, v221\n"
    "v_exp_f32 v222, v222\n"
    "v_exp_f32 v223, v223\n"
    "s_waitcnt lgkmcnt(0)\n"
    "v_smfmac_f32_32x32x32_bf16 v[176:191], v[168:171], v[208:215], v129\n"
    "v_exp_f32 v224, v224\n"
    "v_exp_f32 v225, v225\n"
    "v_exp_f32 v226, v226\n"
    "v_smfmac_f32_32x32x32_bf16 v[176:191], v[172:175], v[216:223], v129\n"
    "s_mov_b64 exec, s[42:43]\n"
    "ds_read_b128 v[168:171], v1 offset:56320\n"
    "ds_read_b128 v[172:175], v1 offset:56576\n"
    "s_mov_b64 exec, -1\n"
    "v_exp_f32 v227, v227\n"
    "v_exp_f32 v228, v228\n"
    "v_mfma_f32_32x32x16_bf16 v[208:223], v[164:167], v[160:163], v[192:207]\n"
    "ds_read_b128 v[164:167], v1 offset:59392\n"
    "v_exp_f32 v229, v229\n"
    "v_exp_f32 v230, v230\n"
    "v_exp_f32 v231, v231\n"
    "v_exp_f32 v232, v232\n"
    "v_exp_f32 v233, v233\n"
    "buffer_load_dwordx4 v[20:23], v134, s[28:31], s86 offen offset:2304 sc1\n"
    "v_exp_f32 v234, v234\n"
    "v_exp_f32 v235, v235\n"
    "v_exp_f32 v236, v236\n"
    "v_exp_f32 v237, v237\n"
    "v_exp_f32 v238, v238\n"
    "v_exp_f32 v239, v239\n"
    "s_waitcnt lgkmcnt(0)\n"
    "v_smfmac_f32_32x32x32_bf16 v[176:191], v[168:171], v[224:231], v129\n"
    "v_exp_f32 v208, v208\n"
    "v_exp_f32 v209, v209\n"
    "v_exp_f32 v210, v210\n"
    "v_smfmac_f32_32x32x32_bf16 v[176:191], v[172:175], v[232:239], v129\n"
    "s_mov_b64 exec, s[42:43]\n"
    "ds_read_b128 v[168:171], v1 offset:58368\n"
    "ds_read_b128 v[172:175], v1 offset:58624\n"
    "s_mov_b64 exec, -1\n"
    "v_exp_f32 v211, v211\n"
    "v_exp_f32 v212, v212\n"
    "v_mfma_f32_32x32x16_bf16 v[224:239], v[164:167], v[160:163], v[192:207]\n"
    "ds_read_b128 v[164:167], v1 offset:61440\n"
    "v_exp_f32 v213, v213\n"
    "v_exp_f32 v214, v214\n"
    "v_exp_f32 v215, v215\n"
    "v_exp_f32 v216, v216\n"
    "v_exp_f32 v217, v217\n"
    "s_add_u32 s86, s86, 0x1000\n"
    "buffer_load_dwordx4 v[24:27], v134, s[28:31], s86 offen offset:0 sc1\n"
    "v_exp_f32 v218, v218\n"
    "v_exp_f32 v219, v219\n"
    "v_exp_f32 v220, v220\n"
    "v_exp_f32 v221, v221\n"
    "v_exp_f32 v222, v222\n"
    "v_exp_f32 v223, v223\n"
    "s_waitcnt lgkmcnt(0)\n"
    "v_smfmac_f32_32x32x32_bf16 v[176:191], v[168:171], v[208:215], v129\n"
    "v_exp_f32 v224, v224\n"
    "v_exp_f32 v225, v225\n"
    "v_exp_f32 v226, v226\n"
    "v_smfmac_f32_32x32x32_bf16 v[176:191], v[172:175], v[216:223], v129\n"
    "s_mov_b64 exec, s[42:43]\n"
    "ds_read_b128 v[168:171], v1 offset:60416\n"
    "ds_read_b128 v[172:175], v1 offset:60672\n"
    "s_mov_b64 exec, -1\n"
    "v_exp_f32 v227, v227\n"
    "v_exp_f32 v228, v228\n"
    "v_mfma_f32_32x32x16_bf16 v[208:223], v[164:167], v[160:163], v[192:207]\n"
    "ds_read_b128 v[164:167], v1 offset:63488\n"
    "v_exp_f32 v229, v229\n"
    "v_exp_f32 v230, v230\n"
    "v_exp_f32 v231, v231\n"
    "v_exp_f32 v232, v232\n"
    "v_exp_f32 v233, v233\n"
    "buffer_load_dwordx4 v[28:31], v134, s[28:31], s86 offen offset:256 sc1\n"
    "v_exp_f32 v234, v234\n"
    "v_exp_f32 v235, v235\n"
    "v_exp_f32 v236, v236\n"
    "v_exp_f32 v237, v237\n"
    "v_exp_f32 v238, v238\n"
    "v_exp_f32 v239, v239\n"
    "s_waitcnt lgkmcnt(0)\n"
    "v_smfmac_f32_32x32x32_bf16 v[176:191], v[168:171], v[224:231], v129\n"
    "v_exp_f32 v208, v208\n"
    "v_exp_f32 v209, v209\n"
    "v_exp_f32 v210, v210\n"
    "v_smfmac_f32_32x32x32_bf16 v[176:191], v[172:175], v[232:239], v129\n"
    "s_mov_b64 exec, s[42:43]\n"
    "ds_read_b128 v[168:171], v1 offset:62464\n"
    "ds_read_b128 v[172:175], v1 offset:62720\n"
    "s_mov_b64 exec, -1\n"
    "v_exp_f32 v211, v211\n"
    "v_exp_f32 v212, v212\n"
    "v_mfma_f32_32x32x16_bf16 v[224:239], v[164:167], v[160:163], v[192:207]\n"
    "ds_read_b128 v[164:167], v128 offset:0\n"
    "v_exp_f32 v213, v213\n"
    "v_exp_f32 v214, v214\n"
    "v_exp_f32 v215, v215\n"
    "v_exp_f32 v216, v216\n"
    "v_exp_f32 v217, v217\n"
    "buffer_load_dwordx4 v[32:35], v134, s[28:31], s86 offen offset:2048 sc1\n"
    "v_exp_f32 v218, v218\n"
    "v_exp_f32 v219, v219\n"
    "v_exp_f32 v220, v220\n"
    "v_exp_f32 v221, v221\n"
    "v_exp_f32 v222, v222\n"
    "v_exp_f32 v223, v223\n"
    "s_waitcnt lgkmcnt(0)\n"
    "v_smfmac_f32_32x32x32_bf16 v[176:191], v[168:171], v[208:215], v129\n"
    "v_exp_f32 v224, v224\n"
    "v_exp_f32 v225, v225\n"
    "v_exp_f32 v226, v226\n"
    "v_smfmac_f32_32x32x32_bf16 v[176:191], v[172:175], v[216:223], v129\n"
    "s_mov_b64 exec, s[42:43]\n"
    "ds_read_b128 v[168:171], v1 offset:64512\n"
    "ds_read_b128 v[172:175], v1 offset:64768\n"
    "s_mov_b64 exec, -1\n"
    "v_exp_f32 v227, v227\n"
    "v_exp_f32 v228, v228\n"
    "v_mfma_f32_32x32x16_bf16 v[208:223], v[164:167], v[160:163], v[192:207]\n"
    "ds_read_b128 v[164:167], v128 offset:2048\n"
    "v_exp_f32 v229, v229\n"
    "v_exp_f32 v230, v230\n"
    "v_exp_f32 v231, v231\n"
    "v_exp_f32 v232, v232\n"
    "v_exp_f32 v233, v233\n"
    "buffer_load_dwordx4 v[36:39], v134, s[28:31], s86 offen offset:2304 sc1\n"
    "v_exp_f32 v234, v234\n"
    "v_exp_f32 v235, v235\n"
    "v_exp_f32 v236, v236\n"
    "v_exp_f32 v237, v237\n"
    "v_exp_f32 v238, v238\n"
    "v_exp_f32 v239, v239\n"
    "s_waitcnt lgkmcnt(0)\n"
    "v_smfmac_f32_32x32x32_bf16 v[176:191], v[168:171], v[224:231], v129\n"
    "v_exp_f32 v208, v208\n"
    "v_exp_f32 v209, v209\n"
    "v_exp_f32 v210, v210\n"
    "v_smfmac_f32_32x32x32_bf16 v[176:191], v[172:175], v[232:239], v129\n"
    "s_mov_b64 exec, s[42:43]\n"
    "ds_read_b128 v[168:171], v128 offset:1024\n"
    "ds_read_b128 v[172:175], v128 offset:1280\n"
    "s_mov_b64 exec, -1\n"
    "v_exp_f32 v211, v211\n"
    "v_exp_f32 v212, v212\n"
    "v_mfma_f32_32x32x16_bf16 v[224:239], v[164:167], v[160:163], v[192:207]\n"
    "ds_read_b128 v[164:167], v128 offset:4096\n"
    "v_exp_f32 v213, v213\n"
    "v_exp_f32 v214, v214\n"
    "v_exp_f32 v215, v215\n"
    "v_exp_f32 v216, v216\n"
    "v_exp_f32 v217, v217\n"
    "s_add_u32 s86, s86, 0x1000\n"
    "buffer_load_dwordx4 v[40:43], v134, s[28:31], s86 offen offset:0 sc1\n"
    "v_exp_f32 v218, v218\n"
    "v_exp_f32 v219, v219\n"
    "v_exp_f32 v220, v220\n"
    "v_exp_f32 v221, v221\n"
    "v_exp_f32 v222, v222\n"
    "v_exp_f32 v223, v223\n"
    "s_waitcnt lgkmcnt(0)\n"
    "v_smfmac_f32_32x32x32_bf16 v[176:191], v[168:171], v[208:215], v129\n"
    "v_exp_f32 v224, v224\n"
    "v_exp_f32 v225, v225\n"
    "v_exp_f32 v226, v226\n"
    "v_smfmac_f32_32x32x32_bf16 v[176:191], v[172:175], v[216:223], v129\n"
    "s_mov_b64 exec, s[42:43]\n"
    "ds_read_b128 v[168:171], v128 offset:3072\n"
    "ds_read_b128 v[172:175], v128 offset:3328\n"
    "s_mov_b64 exec, -1\n"
    "v_exp_f32 v227, v227\n"
    "v_exp_f32 v228, v228\n"
    "v_mfma_f32_32x32x16_bf16 v[208:223], v[164:167], v[160:163], v[192:207]\n"
    "ds_read_b128 v[164:167], v128 offset:6144\n"
    "v_exp_f32 v229, v229\n"
    "v_exp_f32 v230, v230\n"
    "v_exp_f32 v231, v231\n"
    "v_exp_f32 v232, v232\n"
    "v_exp_f32 v233, v233\n"
    "buffer_load_dwordx4 v[44:47], v134, s[28:31], s86 offen offset:256 sc1\n"
    "v_exp_f32 v234, v234\n"
    "v_exp_f32 v235, v235\n"
    "v_exp_f32 v236, v236\n"
    "v_exp_f32 v237, v237\n"
    "v_exp_f32 v238, v238\n"
    "v_exp_f32 v239, v239\n"
    "s_waitcnt lgkmcnt(0)\n"
    "v_smfmac_f32_32x32x32_bf16 v[176:191], v[168:171], v[224:231], v129\n"
    "v_exp_f32 v208, v208\n"
    "v_exp_f32 v209, v209\n"
    "v_exp_f32 v210, v210\n"
    "v_smfmac_f32_32x32x32_bf16 v[176:191], v[172:175], v[232:239], v129\n"
    "s_mov_b64 exec, s[42:43]\n"
    "ds_read_b128 v[168:171], v128 offset:5120\n"
    "ds_read_b128 v[172:175], v128 offset:5376\n"
    "s_mov_b64 exec, -1\n"
    "v_exp_f32 v211, v211\n"
    "v_exp_f32 v212, v212\n"
    "v_mfma_f32_32x32x16_bf16 v[224:239], v[164:167], v[160:163], v[192:207]\n"
    "ds_read_b128 v[164:167], v128 offset:8192\n"
    "v_exp_f32 v213, v213\n"
    "v_exp_f32 v214, v214\n"
    "v_exp_f32 v215, v215\n"
    "v_exp_f32 v216, v216\n"
    "v_exp_f32 v217, v217\n"
    "buffer_load_dwordx4 v[48:51], v134, s[28:31], s86 offen offset:2048 sc1\n"
    "v_exp_f32 v218, v218\n"
    "v_exp_f32 v219, v219\n"
    "v_exp_f32 v220, v220\n"
    "v_exp_f32 v221, v221\n"
    "v_exp_f32 v222, v222\n"
    "v_exp_f32 v223, v223\n"
    "s_waitcnt lgkmcnt(0)\n"
    "v_smfmac_f32_32x32x32_bf16 v[176:191], v[168:171], v[208:215], v129\n"
    "v_exp_f32 v224, v224\n"
    "v_exp_f32 v225, v225\n"
    "v_exp_f32 v226, v226\n"
    "v_smfmac_f32_32x32x32_bf16 v[176:191], v[172:175], v[216:223], v129\n"
    "s_mov_b64 exec, s[42:43]\n"
    "ds_read_b128 v[168:171], v128 offset:7168\n"
    "ds_read_b128 v[172:175], v128 offset:7424\n"
    "s_mov_b64 exec, -1\n"
    "v_exp_f32 v227, v227\n"
    "v_exp_f32 v228, v228\n"
    "v_mfma_f32_32x32x16_bf16 v[208:223], v[164:167], v[160:163], v[192:207]\n"
    "ds_read_b128 v[164:167], v128 offset:10240\n"
    "v_exp_f32 v229, v229\n"
    "v_exp_f32 v230, v230\n"
    "v_exp_f32 v231, v231\n"
    "v_exp_f32 v232, v232\n"
    "v_exp_f32 v233, v233\n"
    "buffer_load_dwordx4 v[52:55], v134, s[28:31], s86 offen offset:2304 sc1\n"
    "v_exp_f32 v234, v234\n"
    "v_exp_f32 v235, v235\n"
    "v_exp_f32 v236, v236\n"
    "v_exp_f32 v237, v237\n"
    "v_exp_f32 v238, v238\n"
    "v_exp_f32 v239, v239\n"
    "s_setprio 1\n"
    "s_waitcnt lgkmcnt(0)\n"
    "v_smfmac_f32_32x32x32_bf16 v[176:191], v[168:171], v[224:231], v129\n"
    "v_exp_f32 v208, v208\n"
    "v_exp_f32 v209, v209\n"
    "v_exp_f32 v210, v210\n"
    "v_smfmac_f32_32x32x32_bf16 v[176:191], v[172:175], v[232:239], v129\n"
    "s_mov_b64 exec, s[42:43]\n"
    "ds_read_b128 v[168:171], v128 offset:9216\n"
    "ds_read_b128 v[172:175], v128 offset:9472\n"
    "s_mov_b64 exec, -1\n"
    "v_exp_f32 v211, v211\n"
    "v_exp_f32 v212, v212\n"
    "v_mfma_f32_32x32x16_bf16 v[224:239], v[164:167], v[160:163], v[192:207]\n"
    "ds_read_b128 v[164:167], v128 offset:12288\n"
    "v_exp_f32 v213, v213\n"
    "v_exp_f32 v214, v214\n"
    "v_exp_f32 v215, v215\n"
    "v_exp_f32 v216, v216\n"
    "v_exp_f32 v217, v217\n"
    "s_add_u32 s86, s86, 0x1000\n"
    "buffer_load_dwordx4 v[56:59], v134, s[28:31], s86 offen offset:0 sc1\n"
    "v_exp_f32 v218, v218\n"
    "v_exp_f32 v219, v219\n"
    "v_exp_f32 v220, v220\n"
    "v_exp_f32 v221, v221\n"
    "v_exp_f32 v222, v222\n"
    "v_exp_f32 v223, v223\n"
    "s_waitcnt lgkmcnt(0)\n"
    "v_smfmac_f32_32x32x32_bf16 v[176:191], v[168:171], v[208:215], v129\n"
    "v_exp_f32 v224, v224\n"
    "v_exp_f32 v225, v225\n"
    "v_exp_f32 v226, v226\n"
    "v_smfmac_f32_32x32x32_bf16 v[176:191], v[172:175], v[216:223], v129\n"
    "s_mov_b64 exec, s[42:43]\n"
    "ds_read_b128 v[168:171], v128 offset:11264\n"
    "ds_read_b128 v[172:175], v128 offset:11520\n"
    "s_mov_b64 exec, -1\n"
    "v_exp_f32 v227, v227\n"
    "v_exp_f32 v228, v228\n"
    "v_mfma_f32_32x32x16_bf16 v[208:223], v[164:167], v[160:163], v[192:207]\n"
    "ds_read_b128 v[164:167], v128 offset:14336\n"
    "v_exp_f32 v229, v229\n"
    "v_exp_f32 v230, v230\n"
    "v_exp_f32 v231, v231\n"
    "v_exp_f32 v232, v232\n"
    "v_exp_f32 v233, v233\n"
    "buffer_load_dwordx4 v[60:63], v134, s[28:31], s86 offen offset:256 sc1\n"
    "v_exp_f32 v234, v234\n"
    "v_exp_f32 v235, v235\n"
    "v_exp_f32 v236, v236\n"
    "v_exp_f32 v237, v237\n"
    "v_exp_f32 v238, v238\n"
    "v_exp_f32 v239, v239\n"
    "s_waitcnt lgkmcnt(0)\n"
    "v_smfmac_f32_32x32x32_bf16 v[176:191], v[168:171], v[224:231], v129\n"
    "v_exp_f32 v208, v208\n"
    "v_exp_f32 v209, v209\n"
    "v_exp_f32 v210, v210\n"
    "v_smfmac_f32_32x32x32_bf16 v[176:191], v[172:175], v[232:239], v129\n"
    "s_mov_b64 exec, s[42:43]\n"
    "ds_read_b128 v[168:171], v128 offset:13312\n"
    "ds_read_b128 v[172:175], v128 offset:13568\n"
    "s_mov_b64 exec, -1\n"
    "v_exp_f32 v211, v211\n"
    "v_exp_f32 v212, v212\n"
    "v_mfma_f32_32x32x16_bf16 v[224:239], v[164:167], v[160:163], v[192:207]\n"
    "ds_read_b128 v[164:167], v128 offset:16384\n"
    "v_exp_f32 v213, v213\n"
    "v_exp_f32 v214, v214\n"
    "v_exp_f32 v215, v215\n"
    "v_exp_f32 v216, v216\n"
    "v_exp_f32 v217, v217\n"
    "buffer_load_dwordx4 v[64:67], v134, s[28:31], s86 offen offset:2048 sc1\n"
    "v_exp_f32 v218, v218\n"
    "v_exp_f32 v219, v219\n"
    "v_exp_f32 v220, v220\n"
    "v_exp_f32 v221, v221\n"
    "v_exp_f32 v222, v222\n"
    "v_exp_f32 v223, v223\n"
    "s_waitcnt lgkmcnt(0)\n"
    "v_smfmac_f32_32x32x32_bf16 v[176:191], v[168:171], v[208:215], v129\n"
    "v_exp_f32 v224, v224\n"
    "v_exp_f32 v225, v225\n"
    "v_exp_f32 v226, v226\n"
    "v_smfmac_f32_32x32x32_bf16 v[176:191], v[172:175], v[216:223], v129\n"
    "s_mov_b64 exec, s[42:43]\n"
    "ds_read_b128 v[168:171], v128 offset:15360\n"
    "ds_read_b128 v[172:175], v128 offset:15616\n"
    "s_mov_b64 exec, -1\n"
    "v_exp_f32 v227, v227\n"
    "v_exp_f32 v228, v228\n"
    "v_mfma_f32_32x32x16_bf16 v[208:223], v[164:167], v[160:163], v[192:207]\n"
    "ds_read_b128 v[164:167], v128 offset:18432\n"
    "v_exp_f32 v229, v229\n"
    "v_exp_f32 v230, v230\n"
    "v_exp_f32 v231, v231\n"
    "v_exp_f32 v232, v232\n"
    "v_exp_f32 v233, v233\n"
    "buffer_load_dwordx4 v[68:71], v134, s[28:31], s86 offen offset:2304 sc1\n"
    "v_exp_f32 v234, v234\n"
    "v_exp_f32 v235, v235\n"
    "v_exp_f32 v236, v236\n"
    "v_exp_f32 v237, v237\n"
    "v_exp_f32 v238, v238\n"
    "v_exp_f32 v239, v239\n"
    "s_waitcnt lgkmcnt(0)\n"
    "v_smfmac_f32_32x32x32_bf16 v[176:191], v[168:171], v[224:231], v129\n"
    "v_exp_f32 v208, v208\n"
    "v_exp_f32 v209, v209\n"
    "v_exp_f32 v210, v210\n"
    "v_smfmac_f32_32x32x32_bf16 v[176:191], v[172:175], v[232:239], v129\n"
    "s_mov_b64 exec, s[42:43]\n"
    "ds_read_b128 v[168:171], v128 offset:17408\n"
    "ds_read_b128 v[172:175], v128 offset:17664\n"
    "s_mov_b64 exec, -1\n"
    "v_exp_f32 v211, v211\n"
    "v_exp_f32 v212, v212\n"
    "v_mfma_f32_32x32x16_bf16 v[224:239], v[164:167], v[160:163], v[192:207]\n"
    "ds_read_b128 v[164:167], v128 offset:20480\n"
    "v_exp_f32 v213, v213\n"
    "v_exp_f32 v214, v214\n"
    "v_exp_f32 v215, v215\n"
    "v_exp_f32 v216, v216\n"
    "v_exp_f32 v217, v217\n"
    "s_add_u32 s86, s86, 0x1000\n"
    "buffer_load_dwordx4 v[72:75], v134, s[28:31], s86 offen offset:0 sc1\n"
    "v_exp_f32 v218, v218\n"
    "v_exp_f32 v219, v219\n"
    "v_exp_f32 v220, v220\n"
    "v_exp_f32 v221, v221\n"
    "v_exp_f32 v222, v222\n"
    "v_exp_f32 v223, v223\n"
    "s_waitcnt lgkmcnt(0)\n"
    "v_smfmac_f32_32x32x32_bf16 v[176:191], v[168:171], v[208:215], v129\n"
    "v_exp_f32 v224, v224\n"
    "v_exp_f32 v225, v225\n"
    "v_exp_f32 v226, v226\n"
    "v_smfmac_f32_32x32x32_bf16 v[176:191], v[172:175], v[216:223], v129\n"
    "s_mov_b64 exec, s[42:43]\n"
    "ds_read_b128 v[168:171], v128 offset:19456\n"
    "ds_read_b128 v[172:175], v128 offset:19712\n"
    "s_mov_b64 exec, -1\n"
    "v_exp_f32 v227, v227\n"
    "v_exp_f32 v228, v228\n"
    "v_mfma_f32_32x32x16_bf16 v[208:223], v[164:167], v[160:163], v[192:207]\n"
    "ds_read_b128 v[164:167], v128 offset:22528\n"
    "v_exp_f32 v229, v229\n"
    "v_exp_f32 v230, v230\n"
    "v_exp_f32 v231, v231\n"
    "v_exp_f32 v232, v232\n"
    "v_exp_f32 v233, v233\n"
    "buffer_load_dwordx4 v[76:79], v134, s[28:31], s86 offen offset:256 sc1\n"
    "v_exp_f32 v234, v234\n"
    "v_exp_f32 v235, v235\n"
    "v_exp_f32 v236, v236\n"
    "v_exp_f32 v237, v237\n"
    "v_exp_f32 v238, v238\n"
    "v_exp_f32 v239, v239\n"
    "s_waitcnt lgkmcnt(0)\n"
    "v_smfmac_f32_32x32x32_bf16 v[176:191], v[168:171], v[224:231], v129\n"
    "v_exp_f32 v208, v208\n"
    "v_exp_f32 v209, v209\n"
    "v_exp_f32 v210, v210\n"
    "v_smfmac_f32_32x32x32_bf16 v[176:191], v[172:175], v[232:239], v129\n"
    "s_mov_b64 exec, s[42:43]\n"
    "ds_read_b128 v[168:171], v128 offset:21504\n"
    "ds_read_b128 v[172:175], v128 offset:21760\n"
    "s_mov_b64 exec, -1\n"
    "v_exp_f32 v211, v211\n"
    "v_exp_f32 v212, v212\n"
    "v_mfma_f32_32x32x16_bf16 v[224:239], v[164:167], v[160:163], v[192:207]\n"
    "ds_read_b128 v[164:167], v128 offset:24576\n"
    "v_exp_f32 v213, v213\n"
    "v_exp_f32 v214, v214\n"
    "v_exp_f32 v215, v215\n"
    "v_exp_f32 v216, v216\n"
    "v_exp_f32 v217, v217\n"
    "buffer_load_dwordx4 v[80:83], v134, s[28:31], s86 offen offset:2048 sc1\n"
    "v_exp_f32 v218, v218\n"
    "v_exp_f32 v219, v219\n"
    "v_exp_f32 v220, v220\n"
    "v_exp_f32 v221, v221\n"
    "v_exp_f32 v222, v222\n"
    "v_exp_f32 v223, v223\n"
    "s_waitcnt lgkmcnt(0)\n"
    "v_smfmac_f32_32x32x32_bf16 v[176:191], v[168:171], v[208:215], v129\n"
    "v_exp_f32 v224, v224\n"
    "v_exp_f32 v225, v225\n"
    "v_exp_f32 v226, v226\n"
    "v_smfmac_f32_32x32x32_bf16 v[176:191], v[172:175], v[216:223], v129\n"
    "s_mov_b64 exec, s[42:43]\n"
    "ds_read_b128 v[168:171], v128 offset:23552\n"
    "ds_read_b128 v[172:175], v128 offset:23808\n"
    "s_mov_b64 exec, -1\n"
    "v_exp_f32 v227, v227\n"
    "v_exp_f32 v228, v228\n"
    "v_mfma_f32_32x32x16_bf16 v[208:223], v[164:167], v[160:163], v[192:207]\n"
    "ds_read_b128 v[164:167], v128 offset:26624\n"
    "v_exp_f32 v229, v229\n"
    "v_exp_f32 v230, v230\n"
    "v_exp_f32 v231, v231\n"
    "v_exp_f32 v232, v232\n"
    "v_exp_f32 v233, v233\n"
    "buffer_load_dwordx4 v[84:87], v134, s[28:31], s86 offen offset:2304 sc1\n"
    "v_exp_f32 v234, v234\n"
    "v_exp_f32 v235, v235\n"
    "v_exp_f32 v236, v236\n"
    "v_exp_f32 v237, v237\n"
    "v_exp_f32 v238, v238\n"
    "v_exp_f32 v239, v239\n"
    "s_setprio 0\n"
    "s_waitcnt lgkmcnt(0)\n"
    "v_smfmac_f32_32x32x32_bf16 v[176:191], v[168:171], v[224:231], v129\n"
    "v_exp_f32 v208, v208\n"
    "v_exp_f32 v209, v209\n"
    "v_exp_f32 v210, v210\n"
    "v_smfmac_f32_32x32x32_bf16 v[176:191], v[172:175], v[232:239], v129\n"
    "s_mov_b64 exec, s[42:43]\n"
    "ds_read_b128 v[168:171], v128 offset:25600\n"
    "ds_read_b128 v[172:175], v128 offset:25856\n"
    "s_mov_b64 exec, -1\n"
    "v_exp_f32 v211, v211\n"
    "v_exp_f32 v212, v212\n"
    "v_mfma_f32_32x32x16_bf16 v[224:239], v[164:167], v[160:163], v[192:207]\n"
    "ds_read_b128 v[164:167], v128 offset:28672\n"
    "v_exp_f32 v213, v213\n"
    "v_exp_f32 v214, v214\n"
    "v_exp_f32 v215, v215\n"
    "v_exp_f32 v216, v216\n"
    "v_exp_f32 v217, v217\n"
    "s_cmp_lt_u32 s57, 3\n"
    "v_mov_b64 v[88:89], 0\n"
    "v_mov_b64 v[90:91], 0\n"
    "v_mov_b64 v[92:93], 0\n"
    "v_mov_b64 v[94:95], 0\n"
    "s_cbranch_scc0 .Lskip_20\n"
    "s_add_u32 s86, s86, 0x1000\n"
    "buffer_load_dwordx4 v[88:91], v134, s[28:31], s86 offen offset:0 sc1\n"
    "buffer_load_dwordx4 v[92:95], v134, s[28:31], s86 offen offset:256 sc1\n"
    ".Lskip_20:\n"
    "v_exp_f32 v218, v218\n"
    "v_exp_f32 v219, v219\n"
    "v_exp_f32 v220, v220\n"
    "v_exp_f32 v221, v221\n"
    "v_exp_f32 v222, v222\n"
    "v_exp_f32 v223, v223\n"
    "s_waitcnt lgkmcnt(0)\n"
    "v_smfmac_f32_32x32x32_bf16 v[176:191], v[168:171], v[208:215], v129\n"
    "v_exp_f32 v224, v224\n"
    "v_exp_f32 v225, v225\n"
    "v_exp_f32 v226, v226\n"
    "v_smfmac_f32_32x32x32_bf16 v[176:191], v[172:175], v[216:223], v129\n"
    "s_mov_b64 exec, s[42:43]\n"
    "ds_read_b128 v[168:171], v128 offset:27648\n"
    "ds_read_b128 v[172:175], v128 offset:27904\n"
    "s_mov_b64 exec, -1\n"
    "v_exp_f32 v227, v227\n"
    "v_exp_f32 v228, v228\n"
    "v_mfma_f32_32x32x16_bf16 v[208:223], v[164:167], v[160:163], v[192:207]\n"
    "ds_read_b128 v[164:167], v128 offset:30720\n"
    "v_exp_f32 v229, v229\n"
    "v_exp_f32 v230, v230\n"
    "v_exp_f32 v231, v231\n"
    "v_exp_f32 v232, v232\n"
    "v_exp_f32 v233, v233\n"
    "buffer_load_dword v141, v140, s[28:31], s88 offen offset:64 sc1\n"
    "v_exp_f32 v234, v234\n"
    "v_exp_f32 v235, v235\n"
    "v_exp_f32 v236, v236\n"
    "v_exp_f32 v237, v237\n"
    "v_exp_f32 v238, v238\n"
    "v_exp_f32 v239, v239\n"
    "s_waitcnt lgkmcnt(0)\n"
    "v_smfmac_f32_32x32x32_bf16 v[176:191], v[168:171], v[224:231], v129\n"
    "v_exp_f32 v208, v208\n"
    "v_exp_f32 v209, v209\n"
    "v_exp_f32 v210, v210\n"
    "v_smfmac_f32_32x32x32_bf16 v[176:191], v[172:175], v[232:239], v129\n"
    "s_mov_b64 exec, s[42:43]\n"
    "ds_read_b128 v[168:171], v128 offset:29696\n"
    "ds_read_b128 v[172:175], v128 offset:29952\n"
    "s_mov_b64 exec, -1\n"
    "v_exp_f32 v211, v211\n"
    "v_exp_f32 v212, v212\n"
    "v_mfma_f32_32x32x16_bf16 v[224:239], v[164:167], v[160:163], v[192:207]\n"
    "ds_read_b128 v[164:167], v128 offset:32768\n"
    "v_exp_f32 v213, v213\n"
    "v_exp_f32 v214, v214\n"
    "v_exp_f32 v215, v215\n"
    "v_exp_f32 v216, v216\n"
    "v_exp_f32 v217, v217\n"
    "buffer_load_dword v140, v140, s[28:31], s88 offen sc1\n"
    "v_exp_f32 v218, v218\n"
    "v_exp_f32 v219, v219\n"
    "v_exp_f32 v220, v220\n"
    "v_exp_f32 v221, v221\n"
    "v_exp_f32 v222, v222\n"
    "v_exp_f32 v223, v223\n"
    "s_waitcnt lgkmcnt(0)\n"
    "v_smfmac_f32_32x32x32_bf16 v[176:191], v[168:171], v[208:215], v129\n"
    "v_exp_f32 v224, v224\n"
    "v_exp_f32 v225, v225\n"
    "v_exp_f32 v226, v226\n"
    "v_smfmac_f32_32x32x32_bf16 v[176:191], v[172:175], v[216:223], v129\n"
    "s_mov_b64 exec, s[42:43]\n"
    "ds_read_b128 v[168:171], v128 offset:31744\n"
    "ds_read_b128 v[172:175], v128 offset:32000\n"
    "s_mov_b64 exec, -1\n"
    "v_exp_f32 v227, v227\n"
    "v_exp_f32 v228, v228\n"
    "v_mfma_f32_32x32x16_bf16 v[208:223], v[164:167], v[160:163], v[192:207]\n"
    "ds_read_b128 v[164:167], v128 offset:34816\n"
    "v_exp_f32 v229, v229\n"
    "v_exp_f32 v230, v230\n"
    "v_exp_f32 v231, v231\n"
    "v_exp_f32 v232, v232\n"
    "v_exp_f32 v233, v233\n"
    "buffer_load_dword v142, v142, s[28:31], s89 offen sc1\n"
    "v_exp_f32 v234, v234\n"
    "v_exp_f32 v235, v235\n"
    "v_exp_f32 v236, v236\n"
    "v_exp_f32 v237, v237\n"
    "v_exp_f32 v238, v238\n"
    "v_exp_f32 v239, v239\n"
    "s_waitcnt lgkmcnt(1)\n"
    "s_barrier\n"
    "s_setprio 3\n"
    "s_waitcnt lgkmcnt(0)\n"
    "v_smfmac_f32_32x32x32_bf16 v[176:191], v[168:171], v[224:231], v129\n"
    "v_exp_f32 v208, v208\n"
    "v_exp_f32 v209, v209\n"
    "v_exp_f32 v210, v210\n"
    "v_smfmac_f32_32x32x32_bf16 v[176:191], v[172:175], v[232:239], v129\n"
    "s_mov_b64 exec, s[42:43]\n"
    "ds_read_b128 v[168:171], v128 offset:33792\n"
    "ds_read_b128 v[172:175], v128 offset:34048\n"
    "s_mov_b64 exec, -1\n"
    "v_exp_f32 v211, v211\n"
    "v_exp_f32 v212, v212\n"
    "v_mfma_f32_32x32x16_bf16 v[224:239], v[164:167], v[160:163], v[192:207]\n"
    "ds_read_b128 v[164:167], v128 offset:36864\n"
    "v_exp_f32 v213, v213\n"
    "v_exp_f32 v214, v214\n"
    "v_exp_f32 v215, v215\n"
    "v_exp_f32 v216, v216\n"
    "v_exp_f32 v217, v217\n"
    "s_add_u32 m0, s79, 0x0\n"
    "s_add_u32 s82, s82, 0x10000\n"
    "buffer_load_dwordx4 v130, s[20:23], s82 offen offset:0 lds\n"
    "v_exp_f32 v218, v218\n"
    "v_exp_f32 v219, v219\n"
    "v_exp_f32 v220, v220\n"
    "v_exp_f32 v221, v221\n"
    "v_exp_f32 v222, v222\n"
    "v_exp_f32 v223, v223\n"
    "s_waitcnt lgkmcnt(0)\n"
    "v_smfmac_f32_32x32x32_bf16 v[176:191], v[168:171], v[208:215], v129\n"
    "v_exp_f32 v224, v224\n"
    "v_exp_f32 v225, v225\n"
    "v_exp_f32 v226, v226\n"
    "v_smfmac_f32_32x32x32_bf16 v[176:191], v[172:175], v[216:223], v129\n"
    "s_mov_b64 exec, s[42:43]\n"
    "ds_read_b128 v[168:171], v128 offset:35840\n"
    "ds_read_b128 v[172:175], v128 offset:36096\n"
    "s_mov_b64 exec, -1\n"
    "v_exp_f32 v227, v227\n"
    "v_exp_f32 v228, v228\n"
    "v_mfma_f32_32x32x16_bf16 v[208:223], v[164:167], v[160:163], v[192:207]\n"
    "ds_read_b128 v[164:167], v128 offset:38912\n"
    "v_exp_f32 v229, v229\n"
    "v_exp_f32 v230, v230\n"
    "v_exp_f32 v231, v231\n"
    "v_exp_f32 v232, v232\n"
    "v_exp_f32 v233, v233\n"
    "buffer_load_dwordx4 v131, s[20:23], s82 offen offset:1024 lds\n"
    "v_exp_f32 v234, v234\n"
    "v_exp_f32 v235, v235\n"
    "v_exp_f32 v236, v236\n"
    "v_exp_f32 v237, v237\n"
    "v_exp_f32 v238, v238\n"
    "v_exp_f32 v239, v239\n"
    "s_waitcnt lgkmcnt(0)\n"
    "v_smfmac_f32_32x32x32_bf16 v[176:191], v[168:171], v[224:231], v129\n"
    "v_exp_f32 v208, v208\n"
    "v_exp_f32 v209, v209\n"
    "v_exp_f32 v210, v210\n"
    "v_smfmac_f32_32x32x32_bf16 v[176:191], v[172:175], v[232:239], v129\n"
    "s_mov_b64 exec, s[42:43]\n"
    "ds_read_b128 v[168:171], v128 offset:37888\n"
    "ds_read_b128 v[172:175], v128 offset:38144\n"
    "s_mov_b64 exec, -1\n"
    "v_exp_f32 v211, v211\n"
    "v_exp_f32 v212, v212\n"
    "v_mfma_f32_32x32x16_bf16 v[224:239], v[164:167], v[160:163], v[192:207]\n"
    "ds_read_b128 v[164:167], v128 offset:40960\n"
    "v_exp_f32 v213, v213\n"
    "v_exp_f32 v214, v214\n"
    "v_exp_f32 v215, v215\n"
    "v_exp_f32 v216, v216\n"
    "v_exp_f32 v217, v217\n"
    "buffer_load_dwordx4 v132, s[20:23], s82 offen offset:2048 lds\n"
    "v_exp_f32 v218, v218\n"
    "v_exp_f32 v219, v219\n"
    "v_exp_f32 v220, v220\n"
    "v_exp_f32 v221, v221\n"
    "v_exp_f32 v222, v222\n"
    "v_exp_f32 v223, v223\n"
    "s_waitcnt lgkmcnt(0)\n"
    "v_smfmac_f32_32x32x32_bf16 v[176:191], v[168:171], v[208:215], v129\n"
    "v_exp_f32 v224, v224\n"
    "v_exp_f32 v225, v225\n"
    "v_exp_f32 v226, v226\n"
    "v_smfmac_f32_32x32x32_bf16 v[176:191], v[172:175], v[216:223], v129\n"
    "s_mov_b64 exec, s[42:43]\n"
    "ds_read_b128 v[168:171], v128 offset:39936\n"
    "ds_read_b128 v[172:175], v128 offset:40192\n"
    "s_mov_b64 exec, -1\n"
    "v_exp_f32 v227, v227\n"
    "v_exp_f32 v228, v228\n"
    "v_mfma_f32_32x32x16_bf16 v[208:223], v[164:167], v[160:163], v[192:207]\n"
    "ds_read_b128 v[164:167], v128 offset:43008\n"
    "v_exp_f32 v229, v229\n"
    "v_exp_f32 v230, v230\n"
    "v_exp_f32 v231, v231\n"
    "v_exp_f32 v232, v232\n"
    "v_exp_f32 v233, v233\n"
    "buffer_load_dwordx4 v133, s[20:23], s82 offen offset:3072 lds\n"
    "v_exp_f32 v234, v234\n"
    "v_exp_f32 v235, v235\n"
    "v_exp_f32 v236, v236\n"
    "v_exp_f32 v237, v237\n"
    "v_exp_f32 v238, v238\n"
    "v_exp_f32 v239, v239\n"
    "s_setprio 2\n"
    "s_waitcnt lgkmcnt(0)\n"
    "v_smfmac_f32_32x32x32_bf16 v[176:191], v[168:171], v[224:231], v129\n"
    "v_exp_f32 v208, v208\n"
    "v_exp_f32 v209, v209\n"
    "v_exp_f32 v210, v210\n"
    "v_smfmac_f32_32x32x32_bf16 v[176:191], v[172:175], v[232:239], v129\n"
    "s_mov_b64 exec, s[42:43]\n"
    "ds_read_b128 v[168:171], v128 offset:41984\n"
    "ds_read_b128 v[172:175], v128 offset:42240\n"
    "s_mov_b64 exec, -1\n"
    "v_exp_f32 v211, v211\n"
    "v_exp_f32 v212, v212\n"
    "v_mfma_f32_32x32x16_bf16 v[224:239], v[164:167], v[160:163], v[192:207]\n"
    "ds_read_b128 v[164:167], v128 offset:45056\n"
    "v_exp_f32 v213, v213\n"
    "v_exp_f32 v214, v214\n"
    "v_exp_f32 v215, v215\n"
    "v_exp_f32 v216, v216\n"
    "v_exp_f32 v217, v217\n"
    "s_add_u32 m0, s79, 0x8000\n"
    "s_add_u32 s82, s82, 0x10000\n"
    "buffer_load_dwordx4 v130, s[20:23], s82 offen offset:0 lds\n"
    "v_exp_f32 v218, v218\n"
    "v_exp_f32 v219, v219\n"
    "v_exp_f32 v220, v220\n"
    "v_exp_f32 v221, v221\n"
    "v_exp_f32 v222, v222\n"
    "v_exp_f32 v223, v223\n"
    "s_waitcnt lgkmcnt(0)\n"
    "v_smfmac_f32_32x32x32_bf16 v[176:191], v[168:171], v[208:215], v129\n"
    "v_exp_f32 v224, v224\n"
    "v_exp_f32 v225, v225\n"
    "v_exp_f32 v226, v226\n"
    "v_smfmac_f32_32x32x32_bf16 v[176:191], v[172:175], v[216:223], v129\n"
    "s_mov_b64 exec, s[42:43]\n"
    "ds_read_b128 v[168:171], v128 offset:44032\n"
    "ds_read_b128 v[172:175], v128 offset:44288\n"
    "s_mov_b64 exec, -1\n"
    "v_exp_f32 v227, v227\n"
    "v_exp_f32 v228, v228\n"
    "v_mfma_f32_32x32x16_bf16 v[208:223], v[164:167], v[160:163], v[192:207]\n"
    "ds_read_b128 v[164:167], v128 offset:47104\n"
    "v_exp_f32 v229, v229\n"
    "v_exp_f32 v230, v230\n"
    "v_exp_f32 v231, v231\n"
    "v_exp_f32 v232, v232\n"
    "v_exp_f32 v233, v233\n"
    "buffer_load_dwordx4 v131, s[20:23], s82 offen offset:1024 lds\n"
    "v_exp_f32 v234, v234\n"
    "v_exp_f32 v235, v235\n"
    "v_exp_f32 v236, v236\n"
    "v_exp_f32 v237, v237\n"
    "v_exp_f32 v238, v238\n"
    "v_exp_f32 v239, v239\n"
    "s_waitcnt lgkmcnt(0)\n"
    "v_smfmac_f32_32x32x32_bf16 v[176:191], v[168:171], v[224:231], v129\n"
    "v_exp_f32 v208, v208\n"
    "v_exp_f32 v209, v209\n"
    "v_exp_f32 v210, v210\n"
    "v_smfmac_f32_32x32x32_bf16 v[176:191], v[172:175], v[232:239], v129\n"
    "s_mov_b64 exec, s[42:43]\n"
    "ds_read_b128 v[168:171], v128 offset:46080\n"
    "ds_read_b128 v[172:175], v128 offset:46336\n"
    "s_mov_b64 exec, -1\n"
    "v_exp_f32 v211, v211\n"
    "v_exp_f32 v212, v212\n"
    "v_mfma_f32_32x32x16_bf16 v[224:239], v[164:167], v[160:163], v[192:207]\n"
    "ds_read_b128 v[164:167], v128 offset:49152\n"
    "v_exp_f32 v213, v213\n"
    "v_exp_f32 v214, v214\n"
    "v_exp_f32 v215, v215\n"
    "v_exp_f32 v216, v216\n"
    "v_exp_f32 v217, v217\n"
    "buffer_load_dwordx4 v132, s[20:23], s82 offen offset:2048 lds\n"
    "v_exp_f32 v218, v218\n"
    "v_exp_f32 v219, v219\n"
    "v_exp_f32 v220, v220\n"
    "v_exp_f32 v221, v221\n"
    "v_exp_f32 v222, v222\n"
    "v_exp_f32 v223, v223\n"
    "s_waitcnt lgkmcnt(0)\n"
    "v_smfmac_f32_32x32x32_bf16 v[176:191], v[168:171], v[208:215], v129\n"
    "v_exp_f32 v224, v224\n"
    "v_exp_f32 v225, v225\n"
    "v_exp_f32 v226, v226\n"
    "v_smfmac_f32_32x32x32_bf16 v[176:191], v[172:175], v[216:223], v129\n"
    "s_mov_b64 exec, s[42:43]\n"
    "ds_read_b128 v[168:171], v128 offset:48128\n"
    "ds_read_b128 v[172:175], v128 offset:48384\n"
    "s_mov_b64 exec, -1\n"
    "v_exp_f32 v227, v227\n"
    "v_exp_f32 v228, v228\n"
    "v_mfma_f32_32x32x16_bf16 v[208:223], v[164:167], v[160:163], v[192:207]\n"
    "ds_read_b128 v[164:167], v128 offset:51200\n"
    "v_exp_f32 v229, v229\n"
    "v_exp_f32 v230, v230\n"
    "v_exp_f32 v231, v231\n"
    "v_exp_f32 v232, v232\n"
    "v_exp_f32 v233, v233\n"
    "buffer_load_dwordx4 v133, s[20:23], s82 offen offset:3072 lds\n"
    "v_exp_f32 v234, v234\n"
    "v_exp_f32 v235, v235\n"
    "v_exp_f32 v236, v236\n"
    "v_exp_f32 v237, v237\n"
    "v_exp_f32 v238, v238\n"
    "v_exp_f32 v239, v239\n"
    "s_setprio 1\n"
    "s_waitcnt lgkmcnt(0)\n"
    "v_smfmac_f32_32x32x32_bf16 v[176:191], v[168:171], v[224:231], v129\n"
    "v_exp_f32 v208, v208\n"
    "v_exp_f32 v209, v209\n"
    "v_exp_f32 v210, v210\n"
    "v_smfmac_f32_32x32x32_bf16 v[176:191], v[172:175], v[232:239], v129\n"
    "s_mov_b64 exec, s[42:43]\n"
    "ds_read_b128 v[168:171], v128 offset:50176\n"
    "ds_read_b128 v[172:175], v128 offset:50432\n"
    "s_mov_b64 exec, -1\n"
    "v_exp_f32 v211, v211\n"
    "v_exp_f32 v212, v212\n"
    "v_mfma_f32_32x32x16_bf16 v[224:239], v[164:167], v[160:163], v[192:207]\n"
    "ds_read_b128 v[164:167], v128 offset:53248\n"
    "v_exp_f32 v213, v213\n"
    "v_exp_f32 v214, v214\n"
    "v_exp_f32 v215, v215\n"
    "v_exp_f32 v216, v216\n"
    "v_exp_f32 v217, v217\n"
    "s_add_u32 m0, s79, 0x10000\n"
    "s_add_u32 s82, s82, 0x10000\n"
    "buffer_load_dwordx4 v130, s[20:23], s82 offen offset:0 lds\n"
    "v_exp_f32 v218, v218\n"
    "v_exp_f32 v219, v219\n"
    "v_exp_f32 v220, v220\n"
    "v_exp_f32 v221, v221\n"
    "v_exp_f32 v222, v222\n"
    "v_exp_f32 v223, v223\n"
    "s_waitcnt lgkmcnt(0)\n"
    "v_smfmac_f32_32x32x32_bf16 v[176:191], v[168:171], v[208:215], v129\n"
    "v_exp_f32 v224, v224\n"
    "v_exp_f32 v225, v225\n"
    "v_exp_f32 v226, v226\n"
    "v_smfmac_f32_32x32x32_bf16 v[176:191], v[172:175], v[216:223], v129\n"
    "s_mov_b64 exec, s[42:43]\n"
    "ds_read_b128 v[168:171], v128 offset:52224\n"
    "ds_read_b128 v[172:175], v128 offset:52480\n"
    "s_mov_b64 exec, -1\n"
    "v_exp_f32 v227, v227\n"
    "v_exp_f32 v228, v228\n"
    "v_mfma_f32_32x32x16_bf16 v[208:223], v[164:167], v[160:163], v[192:207]\n"
    "ds_read_b128 v[164:167], v128 offset:55296\n"
    "v_exp_f32 v229, v229\n"
    "v_exp_f32 v230, v230\n"
    "v_exp_f32 v231, v231\n"
    "v_exp_f32 v232, v232\n"
    "v_exp_f32 v233, v233\n"
    "buffer_load_dwordx4 v131, s[20:23], s82 offen offset:1024 lds\n"
    "v_exp_f32 v234, v234\n"
    "v_exp_f32 v235, v235\n"
    "v_exp_f32 v236, v236\n"
    "v_exp_f32 v237, v237\n"
    "v_exp_f32 v238, v238\n"
    "v_exp_f32 v239, v239\n"
    "s_waitcnt lgkmcnt(0)\n"
    "v_smfmac_f32_32x32x32_bf16 v[176:191], v[168:171], v[224:231], v129\n"
    "v_exp_f32 v208, v208\n"
    "v_exp_f32 v209, v209\n"
    "v_exp_f32 v210, v210\n"
    "v_smfmac_f32_32x32x32_bf16 v[176:191], v[172:175], v[232:239], v129\n"
    "s_mov_b64 exec, s[42:43]\n"
    "ds_read_b128 v[168:171], v128 offset:54272\n"
    "ds_read_b128 v[172:175], v128 offset:54528\n"
    "s_mov_b64 exec, -1\n"
    "v_exp_f32 v211, v211\n"
    "v_exp_f32 v212, v212\n"
    "v_mfma_f32_32x32x16_bf16 v[224:239], v[164:167], v[160:163], v[192:207]\n"
    "ds_read_b128 v[164:167], v128 offset:57344\n"
    "v_exp_f32 v213, v213\n"
    "v_exp_f32 v214, v214\n"
    "v_exp_f32 v215, v215\n"
    "v_exp_f32 v216, v216\n"
    "v_exp_f32 v217, v217\n"
    "buffer_load_dwordx4 v132, s[20:23], s82 offen offset:2048 lds\n"
    "v_exp_f32 v218, v218\n"
    "v_exp_f32 v219, v219\n"
    "v_exp_f32 v220, v220\n"
    "v_exp_f32 v221, v221\n"
    "v_exp_f32 v222, v222\n"
    "v_exp_f32 v223, v223\n"
    "s_waitcnt lgkmcnt(0)\n"
    "v_smfmac_f32_32x32x32_bf16 v[176:191], v[168:171], v[208:215], v129\n"
    "v_exp_f32 v224, v224\n"
    "v_exp_f32 v225, v225\n"
    "v_exp_f32 v226, v226\n"
    "v_smfmac_f32_32x32x32_bf16 v[176:191], v[172:175], v[216:223], v129\n"
    "s_mov_b64 exec, s[42:43]\n"
    "ds_read_b128 v[168:171], v128 offset:56320\n"
    "ds_read_b128 v[172:175], v128 offset:56576\n"
    "s_mov_b64 exec, -1\n"
    "v_exp_f32 v227, v227\n"
    "v_exp_f32 v228, v228\n"
    "v_mfma_f32_32x32x16_bf16 v[208:223], v[164:167], v[160:163], v[192:207]\n"
    "ds_read_b128 v[164:167], v128 offset:59392\n"
    "v_exp_f32 v229, v229\n"
    "v_exp_f32 v230, v230\n"
    "v_exp_f32 v231, v231\n"
    "v_exp_f32 v232, v232\n"
    "v_exp_f32 v233, v233\n"
    "buffer_load_dwordx4 v133, s[20:23], s82 offen offset:3072 lds\n"
    "v_exp_f32 v234, v234\n"
    "v_exp_f32 v235, v235\n"
    "v_exp_f32 v236, v236\n"
    "v_exp_f32 v237, v237\n"
    "v_exp_f32 v238, v238\n"
    "v_exp_f32 v239, v239\n"
    "s_setprio 0\n"
    "s_waitcnt lgkmcnt(0)\n"
    "v_smfmac_f32_32x32x32_bf16 v[176:191], v[168:171], v[224:231], v129\n"
    "v_exp_f32 v208, v208\n"
    "v_exp_f32 v209, v209\n"
    "v_exp_f32 v210, v210\n"
    "v_smfmac_f32_32x32x32_bf16 v[176:191], v[172:175], v[232:239], v129\n"
    "s_mov_b64 exec, s[42:43]\n"
    "ds_read_b128 v[168:171], v128 offset:58368\n"
    "ds_read_b128 v[172:175], v128 offset:58624\n"
    "s_mov_b64 exec, -1\n"
    "v_exp_f32 v211, v211\n"
    "v_exp_f32 v212, v212\n"
    "v_mfma_f32_32x32x16_bf16 v[224:239], v[164:167], v[160:163], v[192:207]\n"
    "ds_read_b128 v[164:167], v128 offset:61440\n"
    "v_exp_f32 v213, v213\n"
    "v_exp_f32 v214, v214\n"
    "v_exp_f32 v215, v215\n"
    "v_exp_f32 v216, v216\n"
    "v_exp_f32 v217, v217\n"
    "v_exp_f32 v218, v218\n"
    "v_exp_f32 v219, v219\n"
    "v_exp_f32 v220, v220\n"
    "v_exp_f32 v221, v221\n"
    "v_exp_f32 v222, v222\n"
    "v_exp_f32 v223, v223\n"
    "s_waitcnt lgkmcnt(0)\n"
    "v_smfmac_f32_32x32x32_bf16 v[176:191], v[168:171], v[208:215], v129\n"
    "v_exp_f32 v224, v224\n"
    "v_exp_f32 v225, v225\n"
    "v_exp_f32 v226, v226\n"
    "v_smfmac_f32_32x32x32_bf16 v[176:191], v[172:175], v[216:223], v129\n"
    "s_mov_b64 exec, s[42:43]\n"
    "ds_read_b128 v[168:171], v128 offset:60416\n"
    "ds_read_b128 v[172:175], v128 offset:60672\n"
    "s_mov_b64 exec, -1\n"
    "v_exp_f32 v227, v227\n"
    "v_exp_f32 v228, v228\n"
    "v_mfma_f32_32x32x16_bf16 v[208:223], v[164:167], v[160:163], v[192:207]\n"
    "ds_read_b128 v[164:167], v128 offset:63488\n"
    "v_exp_f32 v229, v229\n"
    "v_exp_f32 v230, v230\n"
    "v_exp_f32 v231, v231\n"
    "v_exp_f32 v232, v232\n"
    "v_exp_f32 v233, v233\n"
    "v_exp_f32 v234, v234\n"
    "v_exp_f32 v235, v235\n"
    "v_exp_f32 v236, v236\n"
    "v_exp_f32 v237, v237\n"
    "v_exp_f32 v238, v238\n"
    "v_exp_f32 v239, v239\n"
    "s_waitcnt lgkmcnt(0)\n"
    "v_smfmac_f32_32x32x32_bf16 v[176:191], v[168:171], v[224:231], v129\n"
    "v_exp_f32 v208, v208\n"
    "v_exp_f32 v209, v209\n"
    "v_exp_f32 v210, v210\n"
    "v_smfmac_f32_32x32x32_bf16 v[176:191], v[172:175], v[232:239], v129\n"
    "s_mov_b64 exec, s[42:43]\n"
    "ds_read_b128 v[168:171], v128 offset:62464\n"
    "ds_read_b128 v[172:175], v128 offset:62720\n"
    "s_mov_b64 exec, -1\n"
    "v_exp_f32 v211, v211\n"
    "v_exp_f32 v212, v212\n"
    "v_mfma_f32_32x32x16_bf16 v[224:239], v[164:167], v[160:163], v[192:207]\n"
    "v_exp_f32 v213, v213\n"
    "v_exp_f32 v214, v214\n"
    "v_exp_f32 v215, v215\n"
    "v_exp_f32 v216, v216\n"
    "v_exp_f32 v217, v217\n"
    "v_exp_f32 v218, v218\n"
    "v_exp_f32 v219, v219\n"
    "v_exp_f32 v220, v220\n"
    "v_exp_f32 v221, v221\n"
    "v_exp_f32 v222, v222\n"
    "v_exp_f32 v223, v223\n"
    "s_waitcnt lgkmcnt(0)\n"
    "v_smfmac_f32_32x32x32_bf16 v[176:191], v[168:171], v[208:215], v129\n"
    "v_exp_f32 v224, v224\n"
    "v_exp_f32 v225, v225\n"
    "v_exp_f32 v226, v226\n"
    "v_smfmac_f32_32x32x32_bf16 v[176:191], v[172:175], v[216:223], v129\n"
    "s_mov_b64 exec, s[42:43]\n"
    "ds_read_b128 v[168:171], v128 offset:64512\n"
    "ds_read_b128 v[172:175], v128 offset:64768\n"
    "s_mov_b64 exec, -1\n"
    "v_exp_f32 v227, v227\n"
    "v_exp_f32 v228, v228\n"
    "v_exp_f32 v229, v229\n"
    "v_exp_f32 v230, v230\n"
    "v_exp_f32 v231, v231\n"
    "v_exp_f32 v232, v232\n"
    "v_exp_f32 v233, v233\n"
    "v_exp_f32 v234, v234\n"
    "v_exp_f32 v235, v235\n"
    "v_exp_f32 v236, v236\n"
    "v_exp_f32 v237, v237\n"
    "v_exp_f32 v238, v238\n"
    "v_exp_f32 v239, v239\n"
    "s_waitcnt lgkmcnt(1)\n"
    "v_smfmac_f32_32x32x32_bf16 v[176:191], v[168:171], v[224:231], v129\n"
    "s_waitcnt lgkmcnt(0)\n"
    "v_smfmac_f32_32x32x32_bf16 v[176:191], v[172:175], v[232:239], v129\n"
    "s_setprio 0\n"
    "s_mov_b32 s94, 0x2b800000\n"
    "s_nop 9\n"
    "v_fma_f32 v6, |v176|, s94, v184\n"
    "v_fma_f32 v6, |v177|, s94, v6\n"
    "v_fma_f32 v6, |v178|, s94, v6\n"
    "v_fma_f32 v6, |v179|, s94, v6\n"
    "v_fma_f32 v6, |v180|, s94, v6\n"
    "v_fma_f32 v6, |v181|, s94, v6\n"
    "v_fma_f32 v6, |v182|, s94, v6\n"
    "v_fma_f32 v6, |v183|, s94, v6\n"
    "v_cmp_ngt_f32 vcc, 0x5d800000, v6\n"
    "s_cbranch_vccnz .Lslow\n"
    ".Lfast:\n"
    "v_rcp_f32 v6, v184\n"
    "s_add_u32 s94, s90, 0x700000\n"
    "s_waitcnt vmcnt(12)\n"
    "v_mul_f32 v6, v6, v142\n"
    "v_pk_mul_f32 v[176:177], v[176:177], v[6:7] op_sel_hi:[1,0]\n"
    "v_pk_mul_f32 v[178:179], v[178:179], v[6:7] op_sel_hi:[1,0]\n"
    "v_pk_mul_f32 v[180:181], v[180:181], v[6:7] op_sel_hi:[1,0]\n"
    "v_pk_mul_f32 v[182:183], v[182:183], v[6:7] op_sel_hi:[1,0]\n"
    "v_cvt_pk_bf16_f32 v240, v176, v177\n"
    "v_cvt_pk_bf16_f32 v241, v178, v179\n"
    "v_cvt_pk_bf16_f32 v242, v180, v181\n"
    "v_cvt_pk_bf16_f32 v243, v182, v183\n"
    "buffer_store_dwordx4 v[240:243], v1, s[28:31], s94 offen offset:0 sc1\n"
    "s_waitcnt vmcnt(1) lgkmcnt(0)\n"
    "s_barrier\n"
    "ds_read_b64_tr_b16 v[160:161], v146 offset:0\n"
    "ds_read_b64_tr_b16 v[162:163], v146 offset:512\n"
    "ds_read_b64_tr_b16 v[164:165], v149 offset:0\n"
    "ds_read_b64_tr_b16 v[166:167], v149 offset:512\n"
    "ds_read_b64_tr_b16 v[168:169], v152 offset:0\n"
    "ds_read_b64_tr_b16 v[170:171], v152 offset:512\n"
    "ds_read_b64_tr_b16 v[172:173], v155 offset:0\n"
    "ds_read_b64_tr_b16 v[174:175], v155 offset:512\n"
    "s_add_u32 m0, s79, 0x18000\n"
    "s_add_u32 s82, s82, 0x10000\n"
    "s_waitcnt lgkmcnt(6)\n"
    "v_mfma_f32_16x16x32_bf16 v[208:211], v[160:163], v[8:11], 0\n"
    "ds_read_b64_tr_b16 v[176:177], v146 offset:4096\n"
    "ds_read_b64_tr_b16 v[178:179], v146 offset:4608\n"
    "v_mfma_f32_16x16x32_bf16 v[212:215], v[160:163], v[12:15], 0\n"
    "buffer_load_dwordx4 v130, s[20:23], s82 offen offset:0 lds\n"
    "s_waitcnt lgkmcnt(6)\n"
    "v_mfma_f32_16x16x32_bf16 v[216:219], v[164:167], v[8:11], 0\n"
    "ds_read_b64_tr_b16 v[180:181], v149 offset:4096\n"
    "ds_read_b64_tr_b16 v[182:183], v149 offset:4608\n"
    "v_mfma_f32_16x16x32_bf16 v[220:223], v[164:167], v[12:15], 0\n"
    "buffer_load_dwordx4 v131, s[20:23], s82 offen offset:1024 lds\n"
    "s_waitcnt lgkmcnt(6)\n"
    "v_mfma_f32_16x16x32_bf16 v[224:227], v[168:171], v[8:11], 0\n"
    "ds_read_b64_tr_b16 v[184:185], v152 offset:4096\n"
    "ds_read_b64_tr_b16 v[186:187], v152 offset:4608\n"
    "v_mfma_f32_16x16x32_bf16 v[228:231], v[168:171], v[12:15], 0\n"
    "buffer_load_dwordx4 v132, s[20:23], s82 offen offset:2048 lds\n"
    "s_waitcnt lgkmcnt(6)\n"
    "v_mfma_f32_16x16x32_bf16 v[232:235], v[172:175], v[8:11], 0\n"
    "ds_read_b64_tr_b16 v[188:189], v155 offset:4096\n"
    "ds_read_b64_tr_b16 v[190:191], v155 offset:4608\n"
    "v_mfma_f32_16x16x32_bf16 v[236:239], v[172:175], v[12:15], 0\n"
    "buffer_load_dwordx4 v133, s[20:23], s82 offen offset:3072 lds\n"
    "s_waitcnt lgkmcnt(6)\n"
    "v_mfma_f32_16x16x32_bf16 v[208:211], v[176:179], v[16:19], v[208:211]\n"
    "ds_read_b64_tr_b16 v[160:161], v144 offset:0\n"
    "ds_read_b64_tr_b16 v[162:163], v144 offset:512\n"
    "v_mfma_f32_16x16x32_bf16 v[212:215], v[176:179], v[20:23], v[212:215]\n"
    "s_waitcnt lgkmcnt(6)\n"
    "v_mfma_f32_16x16x32_bf16 v[216:219], v[180:183], v[16:19], v[216:219]\n"
    "ds_read_b64_tr_b16 v[164:165], v147 offset:0\n"
    "ds_read_b64_tr_b16 v[166:167], v147 offset:512\n"
    "v_mfma_f32_16x16x32_bf16 v[220:223], v[180:183], v[20:23], v[220:223]\n"
    "s_waitcnt lgkmcnt(6)\n"
    "v_mfma_f32_16x16x32_bf16 v[224:227], v[184:187], v[16:19], v[224:227]\n"
    "ds_read_b64_tr_b16 v[168:169], v150 offset:0\n"
    "ds_read_b64_tr_b16 v[170:171], v150 offset:512\n"
    "v_mfma_f32_16x16x32_bf16 v[228:231], v[184:187], v[20:23], v[228:231]\n"
    "s_waitcnt lgkmcnt(6)\n"
    "v_mfma_f32_16x16x32_bf16 v[232:235], v[188:191], v[16:19], v[232:235]\n"
    "ds_read_b64_tr_b16 v[172:173], v153 offset:0\n"
    "ds_read_b64_tr_b16 v[174:175], v153 offset:512\n"
    "v_mfma_f32_16x16x32_bf16 v[236:239], v[188:191], v[20:23], v[236:239]\n"
    "s_waitcnt vmcnt(4)\n"
    "s_lshr_b32 s94, s90, 3\n"
    "s_add_u32 s94, s94, 0x20000\n"
    "s_mov_b64 exec, 1\n"
    "buffer_store_dwordx2 v[4:5], off, s[28:31], s94 sc1\n"
    "s_mov_b64 exec, -1\n"
    "s_barrier\n"
    "s_add_u32 m0, s85, 0x20000\n"
    "s_waitcnt lgkmcnt(6)\n"
    "v_mfma_f32_16x16x32_bf16 v[208:211], v[160:163], v[24:27], v[208:211]\n"
    "ds_read_b64_tr_b16 v[176:177], v144 offset:4096\n"
    "ds_read_b64_tr_b16 v[178:179], v144 offset:4608\n"
    "v_mfma_f32_16x16x32_bf16 v[212:215], v[160:163], v[28:31], v[212:215]\n"
    "buffer_load_dwordx4 v130, s[20:23], s83 offen offset:0 lds\n"
    "s_waitcnt lgkmcnt(6)\n"
    "v_mfma_f32_16x16x32_bf16 v[216:219], v[164:167], v[24:27], v[216:219]\n"
    "ds_read_b64_tr_b16 v[180:181], v147 offset:4096\n"
    "ds_read_b64_tr_b16 v[182:183], v147 offset:4608\n"
    "v_mfma_f32_16x16x32_bf16 v[220:223], v[164:167], v[28:31], v[220:223]\n"
    "buffer_load_dwordx4 v131, s[20:23], s83 offen offset:1024 lds\n"
    "s_waitcnt lgkmcnt(6)\n"
    "v_mfma_f32_16x16x32_bf16 v[224:227], v[168:171], v[24:27], v[224:227]\n"
    "ds_read_b64_tr_b16 v[184:185], v150 offset:4096\n"
    "ds_read_b64_tr_b16 v[186:187], v150 offset:4608\n"
    "v_mfma_f32_16x16x32_bf16 v[228:231], v[168:171], v[28:31], v[228:231]\n"
    "s_waitcnt lgkmcnt(6)\n"
    "v_mfma_f32_16x16x32_bf16 v[232:235], v[172:175], v[24:27], v[232:235]\n"
    "ds_read_b64_tr_b16 v[188:189], v153 offset:4096\n"
    "ds_read_b64_tr_b16 v[190:191], v153 offset:4608\n"
    "v_mfma_f32_16x16x32_bf16 v[236:239], v[172:175], v[28:31], v[236:239]\n"
    "s_waitcnt lgkmcnt(6)\n"
    "v_mfma_f32_16x16x32_bf16 v[208:211], v[176:179], v[32:35], v[208:211]\n"
    "ds_read_b64_tr_b16 v[160:161], v144 offset:32768\n"
    "ds_read_b64_tr_b16 v[162:163], v144 offset:33280\n"
    "v_mfma_f32_16x16x32_bf16 v[212:215], v[176:179], v[36:39], v[212:215]\n"
    "s_waitcnt lgkmcnt(6)\n"
    "v_mfma_f32_16x16x32_bf16 v[216:219], v[180:183], v[32:35], v[216:219]\n"
    "ds_read_b64_tr_b16 v[164:165], v147 offset:32768\n"
    "ds_read_b64_tr_b16 v[166:167], v147 offset:33280\n"
    "v_mfma_f32_16x16x32_bf16 v[220:223], v[180:183], v[36:39], v[220:223]\n"
    "s_waitcnt lgkmcnt(6)\n"
    "v_mfma_f32_16x16x32_bf16 v[224:227], v[184:187], v[32:35], v[224:227]\n"
    "ds_read_b64_tr_b16 v[168:169], v150 offset:32768\n"
    "ds_read_b64_tr_b16 v[170:171], v150 offset:33280\n"
    "v_mfma_f32_16x16x32_bf16 v[228:231], v[184:187], v[36:39], v[228:231]\n"
    "s_waitcnt lgkmcnt(6)\n"
    "v_mfma_f32_16x16x32_bf16 v[232:235], v[188:191], v[32:35], v[232:235]\n"
    "ds_read_b64_tr_b16 v[172:173], v153 offset:32768\n"
    "ds_read_b64_tr_b16 v[174:175], v153 offset:33280\n"
    "v_mfma_f32_16x16x32_bf16 v[236:239], v[188:191], v[36:39], v[236:239]\n"
    "s_barrier\n"
    "s_add_u32 m0, s79, 0x0\n"
    "s_waitcnt lgkmcnt(6)\n"
    "v_mfma_f32_16x16x32_bf16 v[208:211], v[160:163], v[40:43], v[208:211]\n"
    "ds_read_b64_tr_b16 v[176:177], v144 offset:36864\n"
    "ds_read_b64_tr_b16 v[178:179], v144 offset:37376\n"
    "v_mfma_f32_16x16x32_bf16 v[212:215], v[160:163], v[44:47], v[212:215]\n"
    "buffer_load_dwordx4 v130, s[24:27], s84 offen offset:0 lds\n"
    "s_waitcnt lgkmcnt(6)\n"
    "v_mfma_f32_16x16x32_bf16 v[216:219], v[164:167], v[40:43], v[216:219]\n"
    "ds_read_b64_tr_b16 v[180:181], v147 offset:36864\n"
    "ds_read_b64_tr_b16 v[182:183], v147 offset:37376\n"
    "v_mfma_f32_16x16x32_bf16 v[220:223], v[164:167], v[44:47], v[220:223]\n"
    "buffer_load_dwordx4 v131, s[24:27], s84 offen offset:1024 lds\n"
    "s_waitcnt lgkmcnt(6)\n"
    "v_mfma_f32_16x16x32_bf16 v[224:227], v[168:171], v[40:43], v[224:227]\n"
    "ds_read_b64_tr_b16 v[184:185], v150 offset:36864\n"
    "ds_read_b64_tr_b16 v[186:187], v150 offset:37376\n"
    "v_mfma_f32_16x16x32_bf16 v[228:231], v[168:171], v[44:47], v[228:231]\n"
    "buffer_load_dwordx4 v132, s[24:27], s84 offen offset:2048 lds\n"
    "s_waitcnt lgkmcnt(6)\n"
    "v_mfma_f32_16x16x32_bf16 v[232:235], v[172:175], v[40:43], v[232:235]\n"
    "ds_read_b64_tr_b16 v[188:189], v153 offset:36864\n"
    "ds_read_b64_tr_b16 v[190:191], v153 offset:37376\n"
    "v_mfma_f32_16x16x32_bf16 v[236:239], v[172:175], v[44:47], v[236:239]\n"
    "buffer_load_dwordx4 v133, s[24:27], s84 offen offset:3072 lds\n"
    "s_waitcnt lgkmcnt(6)\n"
    "v_mfma_f32_16x16x32_bf16 v[208:211], v[176:179], v[48:51], v[208:211]\n"
    "ds_read_b64_tr_b16 v[160:161], v145 offset:0\n"
    "ds_read_b64_tr_b16 v[162:163], v145 offset:512\n"
    "v_mfma_f32_16x16x32_bf16 v[212:215], v[176:179], v[52:55], v[212:215]\n"
    "s_waitcnt lgkmcnt(6)\n"
    "v_mfma_f32_16x16x32_bf16 v[216:219], v[180:183], v[48:51], v[216:219]\n"
    "ds_read_b64_tr_b16 v[164:165], v148 offset:0\n"
    "ds_read_b64_tr_b16 v[166:167], v148 offset:512\n"
    "v_mfma_f32_16x16x32_bf16 v[220:223], v[180:183], v[52:55], v[220:223]\n"
    "s_waitcnt lgkmcnt(6)\n"
    "v_mfma_f32_16x16x32_bf16 v[224:227], v[184:187], v[48:51], v[224:227]\n"
    "ds_read_b64_tr_b16 v[168:169], v151 offset:0\n"
    "ds_read_b64_tr_b16 v[170:171], v151 offset:512\n"
    "v_mfma_f32_16x16x32_bf16 v[228:231], v[184:187], v[52:55], v[228:231]\n"
    "s_waitcnt lgkmcnt(6)\n"
    "v_mfma_f32_16x16x32_bf16 v[232:235], v[188:191], v[48:51], v[232:235]\n"
    "ds_read_b64_tr_b16 v[172:173], v154 offset:0\n"
    "ds_read_b64_tr_b16 v[174:175], v154 offset:512\n"
    "v_mfma_f32_16x16x32_bf16 v[236:239], v[188:191], v[52:55], v[236:239]\n"
    "s_barrier\n"
    "s_add_u32 m0, s79, 0x8000\n"
    "s_add_u32 s84, s84, 0x10000\n"
    "s_waitcnt lgkmcnt(6)\n"
    "v_mfma_f32_16x16x32_bf16 v[208:211], v[160:163], v[56:59], v[208:211]\n"
    "ds_read_b64_tr_b16 v[176:177], v145 offset:4096\n"
    "ds_read_b64_tr_b16 v[178:179], v145 offset:4608\n"
    "v_mfma_f32_16x16x32_bf16 v[212:215], v[160:163], v[60:63], v[212:215]\n"
    "buffer_load_dwordx4 v130, s[24:27], s84 offen offset:0 lds\n"
    "s_waitcnt lgkmcnt(6)\n"
    "v_mfma_f32_16x16x32_bf16 v[216:219], v[164:167], v[56:59], v[216:219]\n"
    "ds_read_b64_tr_b16 v[180:181], v148 offset:4096\n"
    "ds_read_b64_tr_b16 v[182:183], v148 offset:4608\n"
    "v_mfma_f32_16x16x32_bf16 v[220:223], v[164:167], v[60:63], v[220:223]\n"
    "buffer_load_dwordx4 v131, s[24:27], s84 offen offset:1024 lds\n"
    "s_waitcnt lgkmcnt(6)\n"
    "v_mfma_f32_16x16x32_bf16 v[224:227], v[168:171], v[56:59], v[224:227]\n"
    "ds_read_b64_tr_b16 v[184:185], v151 offset:4096\n"
    "ds_read_b64_tr_b16 v[186:187], v151 offset:4608\n"
    "v_mfma_f32_16x16x32_bf16 v[228:231], v[168:171], v[60:63], v[228:231]\n"
    "buffer_load_dwordx4 v132, s[24:27], s84 offen offset:2048 lds\n"
    "s_waitcnt lgkmcnt(6)\n"
    "v_mfma_f32_16x16x32_bf16 v[232:235], v[172:175], v[56:59], v[232:235]\n"
    "ds_read_b64_tr_b16 v[188:189], v154 offset:4096\n"
    "ds_read_b64_tr_b16 v[190:191], v154 offset:4608\n"
    "v_mfma_f32_16x16x32_bf16 v[236:239], v[172:175], v[60:63], v[236:239]\n"
    "buffer_load_dwordx4 v133, s[24:27], s84 offen offset:3072 lds\n"
    "buffer_load_dwordx2 v[138:139], v136, s[28:31], 0 offen sc1\n"
    "s_waitcnt vmcnt(9)\n"
    "s_barrier\n"
    "s_waitcnt lgkmcnt(6)\n"
    "v_mfma_f32_16x16x32_bf16 v[208:211], v[176:179], v[64:67], v[208:211]\n"
    "ds_read_b64_tr_b16 v[160:161], v145 offset:32768\n"
    "ds_read_b64_tr_b16 v[162:163], v145 offset:33280\n"
    "v_mfma_f32_16x16x32_bf16 v[212:215], v[176:179], v[68:71], v[212:215]\n"
    "s_waitcnt lgkmcnt(6)\n"
    "v_mfma_f32_16x16x32_bf16 v[216:219], v[180:183], v[64:67], v[216:219]\n"
    "ds_read_b64_tr_b16 v[164:165], v148 offset:32768\n"
    "ds_read_b64_tr_b16 v[166:167], v148 offset:33280\n"
    "v_mfma_f32_16x16x32_bf16 v[220:223], v[180:183], v[68:71], v[220:223]\n"
    "s_waitcnt lgkmcnt(6)\n"
    "v_mfma_f32_16x16x32_bf16 v[224:227], v[184:187], v[64:67], v[224:227]\n"
    "ds_read_b64_tr_b16 v[168:169], v151 offset:32768\n"
    "ds_read_b64_tr_b16 v[170:171], v151 offset:33280\n"
    "v_mfma_f32_16x16x32_bf16 v[228:231], v[184:187], v[68:71], v[228:231]\n"
    "s_waitcnt lgkmcnt(6)\n"
    "v_mfma_f32_16x16x32_bf16 v[232:235], v[188:191], v[64:67], v[232:235]\n"
    "ds_read_b64_tr_b16 v[172:173], v154 offset:32768\n"
    "ds_read_b64_tr_b16 v[174:175], v154 offset:33280\n"
    "v_mfma_f32_16x16x32_bf16 v[236:239], v[188:191], v[68:71], v[236:239]\n"
    "s_waitcnt lgkmcnt(6)\n"
    "v_mfma_f32_16x16x32_bf16 v[208:211], v[160:163], v[72:75], v[208:211]\n"
    "ds_read_b64_tr_b16 v[176:177], v145 offset:36864\n"
    "ds_read_b64_tr_b16 v[178:179], v145 offset:37376\n"
    "v_mfma_f32_16x16x32_bf16 v[212:215], v[160:163], v[76:79], v[212:215]\n"
    "s_waitcnt lgkmcnt(6)\n"
    "v_mfma_f32_16x16x32_bf16 v[216:219], v[164:167], v[72:75], v[216:219]\n"
    "ds_read_b64_tr_b16 v[180:181], v148 offset:36864\n"
    "ds_read_b64_tr_b16 v[182:183], v148 offset:37376\n"
    "v_mfma_f32_16x16x32_bf16 v[220:223], v[164:167], v[76:79], v[220:223]\n"
    "s_waitcnt vmcnt(0)\n"
    "v_cmp_ne_u64 vcc, s[40:41], v[138:139]\n"
    "s_mov_b32 s94, 0x20000\n"
    ".Lpoll_21:\n"
    "s_cbranch_vccz .Ldone_22\n"
    "s_sleep 1\n"
    "buffer_load_dwordx2 v[138:139], v136, s[28:31], 0 offen sc1\n"
    "s_waitcnt vmcnt(0)\n"
    "v_cmp_ne_u64 vcc, s[40:41], v[138:139]\n"
    "s_sub_u32 s94, s94, 1\n"
    "s_cmp_lg_u32 s94, 0\n"
    "s_cbranch_scc1 .Lpoll_21\n"
    ".Ldone_22:\n"
    "s_add_u32 s94, s87, 0x700000\n"
    "s_add_u32 s95, s94, 0x1000\n"
    "buffer_load_dwordx4 v[96:99], v134, s[28:31], s94 offen offset:0 sc1\n"
    "buffer_load_dwordx4 v[100:103], v134, s[28:31], s94 offen offset:256 sc1\n"
    "buffer_load_dwordx4 v[104:107], v134, s[28:31], s94 offen offset:2048 sc1\n"
    "buffer_load_dwordx4 v[108:111], v134, s[28:31], s94 offen offset:2304 sc1\n"
    "buffer_load_dwordx4 v[112:115], v134, s[28:31], s95 offen offset:0 sc1\n"
    "buffer_load_dwordx4 v[116:119], v134, s[28:31], s95 offen offset:256 sc1\n"
    "buffer_load_dwordx4 v[120:123], v134, s[28:31], s95 offen offset:2048 sc1\n"
    "buffer_load_dwordx4 v[124:127], v134, s[28:31], s95 offen offset:2304 sc1\n"
    "v_sub_f32 v244, 1.0, v140\n"
    "v_sub_f32 v246, 1.0, v141\n"
    "s_waitcnt lgkmcnt(6)\n"
    "v_mfma_f32_16x16x32_bf16 v[224:227], v[168:171], v[72:75], v[224:227]\n"
    "ds_read_b64_tr_b16 v[184:185], v151 offset:36864\n"
    "ds_read_b64_tr_b16 v[186:187], v151 offset:37376\n"
    "v_mfma_f32_16x16x32_bf16 v[228:231], v[168:171], v[76:79], v[228:231]\n"
    "s_waitcnt lgkmcnt(6)\n"
    "v_mfma_f32_16x16x32_bf16 v[232:235], v[172:175], v[72:75], v[232:235]\n"
    "ds_read_b64_tr_b16 v[188:189], v154 offset:36864\n"
    "ds_read_b64_tr_b16 v[190:191], v154 offset:37376\n"
    "v_mfma_f32_16x16x32_bf16 v[236:239], v[172:175], v[76:79], v[236:239]\n"
    "s_waitcnt lgkmcnt(6)\n"
    "v_mfma_f32_16x16x32_bf16 v[208:211], v[176:179], v[80:83], v[208:211]\n"
    "ds_read_b64_tr_b16 v[160:161], v146 offset:0\n"
    "ds_read_b64_tr_b16 v[162:163], v146 offset:512\n"
    "v_mfma_f32_16x16x32_bf16 v[212:215], v[176:179], v[84:87], v[212:215]\n"
    "s_waitcnt lgkmcnt(6)\n"
    "v_mfma_f32_16x16x32_bf16 v[216:219], v[180:183], v[80:83], v[216:219]\n"
    "ds_read_b64_tr_b16 v[164:165], v149 offset:0\n"
    "ds_read_b64_tr_b16 v[166:167], v149 offset:512\n"
    "v_mfma_f32_16x16x32_bf16 v[220:223], v[180:183], v[84:87], v[220:223]\n"
    "s_waitcnt lgkmcnt(6)\n"
    "v_mfma_f32_16x16x32_bf16 v[224:227], v[184:187], v[80:83], v[224:227]\n"
    "ds_read_b64_tr_b16 v[168:169], v152 offset:0\n"
    "ds_read_b64_tr_b16 v[170:171], v152 offset:512\n"
    "v_mfma_f32_16x16x32_bf16 v[228:231], v[184:187], v[84:87], v[228:231]\n"
    "s_waitcnt lgkmcnt(6)\n"
    "v_mfma_f32_16x16x32_bf16 v[232:235], v[188:191], v[80:83], v[232:235]\n"
    "ds_read_b64_tr_b16 v[172:173], v155 offset:0\n"
    "ds_read_b64_tr_b16 v[174:175], v155 offset:512\n"
    "v_mfma_f32_16x16x32_bf16 v[236:239], v[188:191], v[84:87], v[236:239]\n"
    "s_waitcnt lgkmcnt(0)\n"
    "s_barrier\n"
    "v_mfma_f32_16x16x32_bf16 v[208:211], v[160:163], v[88:91], v[208:211]\n"
    "ds_read_b64_tr_b16 v[176:177], v144 offset:0\n"
    "ds_read_b64_tr_b16 v[178:179], v144 offset:512\n"
    "v_mfma_f32_16x16x32_bf16 v[212:215], v[160:163], v[92:95], v[212:215]\n"
    "v_mfma_f32_16x16x32_bf16 v[216:219], v[164:167], v[88:91], v[216:219]\n"
    "ds_read_b64_tr_b16 v[180:181], v144 offset:4096\n"
    "ds_read_b64_tr_b16 v[182:183], v144 offset:4608\n"
    "v_mfma_f32_16x16x32_bf16 v[220:223], v[164:167], v[92:95], v[220:223]\n"
    "v_mfma_f32_16x16x32_bf16 v[224:227], v[168:171], v[88:91], v[224:227]\n"
    "ds_read_b64_tr_b16 v[184:185], v144 offset:32768\n"
    "ds_read_b64_tr_b16 v[186:187], v144 offset:33280\n"
    "v_mfma_f32_16x16x32_bf16 v[228:231], v[168:171], v[92:95], v[228:231]\n"
    "v_mfma_f32_16x16x32_bf16 v[232:235], v[172:175], v[88:91], v[232:235]\n"
    "ds_read_b64_tr_b16 v[188:189], v144 offset:36864\n"
    "ds_read_b64_tr_b16 v[190:191], v144 offset:37376\n"
    "v_mfma_f32_16x16x32_bf16 v[236:239], v[172:175], v[92:95], v[236:239]\n"
    "v_pk_mul_f32 v[208:209], v[208:209], v[244:245] op_sel_hi:[1,0]\n"
    "v_pk_mul_f32 v[210:211], v[210:211], v[244:245] op_sel_hi:[1,0]\n"
    "v_pk_mul_f32 v[212:213], v[212:213], v[246:247] op_sel_hi:[1,0]\n"
    "v_pk_mul_f32 v[214:215], v[214:215], v[246:247] op_sel_hi:[1,0]\n"
    "s_waitcnt vmcnt(7) lgkmcnt(6)\n"
    "v_mfma_f32_16x16x32_bf16 v[208:211], v[176:179], v[96:99], v[208:211]\n"
    "ds_read_b64_tr_b16 v[160:161], v147 offset:0\n"
    "ds_read_b64_tr_b16 v[162:163], v147 offset:512\n"
    "s_waitcnt vmcnt(6)\n"
    "v_mfma_f32_16x16x32_bf16 v[212:215], v[176:179], v[100:103], v[212:215]\n"
    "s_waitcnt vmcnt(5) lgkmcnt(6)\n"
    "v_mfma_f32_16x16x32_bf16 v[208:211], v[180:183], v[104:107], v[208:211]\n"
    "ds_read_b64_tr_b16 v[164:165], v147 offset:4096\n"
    "ds_read_b64_tr_b16 v[166:167], v147 offset:4608\n"
    "s_waitcnt vmcnt(4)\n"
    "v_mfma_f32_16x16x32_bf16 v[212:215], v[180:183], v[108:111], v[212:215]\n"
    "s_waitcnt vmcnt(3) lgkmcnt(6)\n"
    "v_mfma_f32_16x16x32_bf16 v[208:211], v[184:187], v[112:115], v[208:211]\n"
    "ds_read_b64_tr_b16 v[168:169], v147 offset:32768\n"
    "ds_read_b64_tr_b16 v[170:171], v147 offset:33280\n"
    "s_waitcnt vmcnt(2)\n"
    "v_mfma_f32_16x16x32_bf16 v[212:215], v[184:187], v[116:119], v[212:215]\n"
    "s_waitcnt vmcnt(1) lgkmcnt(6)\n"
    "v_mfma_f32_16x16x32_bf16 v[208:211], v[188:191], v[120:123], v[208:211]\n"
    "ds_read_b64_tr_b16 v[172:173], v147 offset:36864\n"
    "ds_read_b64_tr_b16 v[174:175], v147 offset:37376\n"
    "s_waitcnt vmcnt(0)\n"
    "v_mfma_f32_16x16x32_bf16 v[212:215], v[188:191], v[124:127], v[212:215]\n"
    "v_pk_mul_f32 v[216:217], v[216:217], v[244:245] op_sel_hi:[1,0]\n"
    "v_pk_mul_f32 v[218:219], v[218:219], v[244:245] op_sel_hi:[1,0]\n"
    "v_pk_mul_f32 v[220:221], v[220:221], v[246:247] op_sel_hi:[1,0]\n"
    "v_pk_mul_f32 v[222:223], v[222:223], v[246:247] op_sel_hi:[1,0]\n"
    "s_waitcnt lgkmcnt(6)\n"
    "v_mfma_f32_16x16x32_bf16 v[216:219], v[160:163], v[96:99], v[216:219]\n"
    "ds_read_b64_tr_b16 v[176:177], v150 offset:0\n"
    "ds_read_b64_tr_b16 v[178:179], v150 offset:512\n"
    "v_mfma_f32_16x16x32_bf16 v[220:223], v[160:163], v[100:103], v[220:223]\n"
    "s_waitcnt lgkmcnt(6)\n"
    "v_mfma_f32_16x16x32_bf16 v[216:219], v[164:167], v[104:107], v[216:219]\n"
    "ds_read_b64_tr_b16 v[180:181], v150 offset:4096\n"
    "ds_read_b64_tr_b16 v[182:183], v150 offset:4608\n"
    "v_mfma_f32_16x16x32_bf16 v[220:223], v[164:167], v[108:111], v[220:223]\n"
    "ds_write_b128 v156, v[208:211] offset:0\n"
    "ds_write_b128 v156, v[212:215] offset:4352\n"
    "s_waitcnt lgkmcnt(8)\n"
    "v_mfma_f32_16x16x32_bf16 v[216:219], v[168:171], v[112:115], v[216:219]\n"
    "ds_read_b64_tr_b16 v[184:185], v150 offset:32768\n"
    "ds_read_b64_tr_b16 v[186:187], v150 offset:33280\n"
    "v_mfma_f32_16x16x32_bf16 v[220:223], v[168:171], v[116:119], v[220:223]\n"
    "s_waitcnt lgkmcnt(8)\n"
    "v_mfma_f32_16x16x32_bf16 v[216:219], v[172:175], v[120:123], v[216:219]\n"
    "ds_read_b64_tr_b16 v[188:189], v150 offset:36864\n"
    "ds_read_b64_tr_b16 v[190:191], v150 offset:37376\n"
    "v_mfma_f32_16x16x32_bf16 v[220:223], v[172:175], v[124:127], v[220:223]\n"
    "v_pk_mul_f32 v[224:225], v[224:225], v[244:245] op_sel_hi:[1,0]\n"
    "v_pk_mul_f32 v[226:227], v[226:227], v[244:245] op_sel_hi:[1,0]\n"
    "v_pk_mul_f32 v[228:229], v[228:229], v[246:247] op_sel_hi:[1,0]\n"
    "v_pk_mul_f32 v[230:231], v[230:231], v[246:247] op_sel_hi:[1,0]\n"
    "s_waitcnt lgkmcnt(8)\n"
    "v_mfma_f32_16x16x32_bf16 v[224:227], v[176:179], v[96:99], v[224:227]\n"
    "ds_read_b64_tr_b16 v[160:161], v153 offset:0\n"
    "ds_read_b64_tr_b16 v[162:163], v153 offset:512\n"
    "v_mfma_f32_16x16x32_bf16 v[228:231], v[176:179], v[100:103], v[228:231]\n"
    "s_waitcnt lgkmcnt(8)\n"
    "v_mfma_f32_16x16x32_bf16 v[224:227], v[180:183], v[104:107], v[224:227]\n"
    "ds_read_b64_tr_b16 v[164:165], v153 offset:4096\n"
    "ds_read_b64_tr_b16 v[166:167], v153 offset:4608\n"
    "v_mfma_f32_16x16x32_bf16 v[228:231], v[180:183], v[108:111], v[228:231]\n"
    "ds_write_b128 v156, v[216:219] offset:64\n"
    "ds_write_b128 v156, v[220:223] offset:4416\n"
    "s_waitcnt lgkmcnt(8)\n"
    "v_mfma_f32_16x16x32_bf16 v[224:227], v[184:187], v[112:115], v[224:227]\n"
    "ds_read_b64_tr_b16 v[168:169], v153 offset:32768\n"
    "ds_read_b64_tr_b16 v[170:171], v153 offset:33280\n"
    "v_mfma_f32_16x16x32_bf16 v[228:231], v[184:187], v[116:119], v[228:231]\n"
    "s_waitcnt lgkmcnt(8)\n"
    "v_mfma_f32_16x16x32_bf16 v[224:227], v[188:191], v[120:123], v[224:227]\n"
    "ds_read_b64_tr_b16 v[172:173], v153 offset:36864\n"
    "ds_read_b64_tr_b16 v[174:175], v153 offset:37376\n"
    "v_mfma_f32_16x16x32_bf16 v[228:231], v[188:191], v[124:127], v[228:231]\n"
    "v_pk_mul_f32 v[232:233], v[232:233], v[244:245] op_sel_hi:[1,0]\n"
    "v_pk_mul_f32 v[234:235], v[234:235], v[244:245] op_sel_hi:[1,0]\n"
    "v_pk_mul_f32 v[236:237], v[236:237], v[246:247] op_sel_hi:[1,0]\n"
    "v_pk_mul_f32 v[238:239], v[238:239], v[246:247] op_sel_hi:[1,0]\n"
    "s_waitcnt lgkmcnt(8)\n"
    "v_mfma_f32_16x16x32_bf16 v[232:235], v[160:163], v[96:99], v[232:235]\n"
    "v_mfma_f32_16x16x32_bf16 v[236:239], v[160:163], v[100:103], v[236:239]\n"
    "s_waitcnt lgkmcnt(6)\n"
    "v_mfma_f32_16x16x32_bf16 v[232:235], v[164:167], v[104:107], v[232:235]\n"
    "v_mfma_f32_16x16x32_bf16 v[236:239], v[164:167], v[108:111], v[236:239]\n"
    "ds_write_b128 v156, v[224:227] offset:128\n"
    "ds_write_b128 v156, v[228:231] offset:4480\n"
    "s_waitcnt lgkmcnt(4)\n"
    "v_mfma_f32_16x16x32_bf16 v[232:235], v[168:171], v[112:115], v[232:235]\n"
    "v_mfma_f32_16x16x32_bf16 v[236:239], v[168:171], v[116:119], v[236:239]\n"
    "s_waitcnt lgkmcnt(2)\n"
    "v_mfma_f32_16x16x32_bf16 v[232:235], v[172:175], v[120:123], v[232:235]\n"
    "v_mfma_f32_16x16x32_bf16 v[236:239], v[172:175], v[124:127], v[236:239]\n"
    "s_nop 6\n"
    "ds_write_b128 v156, v[232:235] offset:192\n"
    "ds_write_b128 v156, v[236:239] offset:4544\n"
    "s_waitcnt lgkmcnt(0)\n"
    "s_barrier\n"
    "ds_read_b128 v[8:11], v157 offset:0\n"
    "ds_read_b128 v[12:15], v157 offset:16\n"
    "ds_read_b128 v[16:19], v157 offset:17408\n"
    "ds_read_b128 v[20:23], v157 offset:17424\n"
    "ds_read_b128 v[24:27], v157 offset:34816\n"
    "ds_read_b128 v[28:31], v157 offset:34832\n"
    "ds_read_b128 v[32:35], v157 offset:52224\n"
    "ds_read_b128 v[36:39], v157 offset:52240\n"
    "s_waitcnt lgkmcnt(5)\n"
    "v_pk_add_f32 v[8:9], v[8:9], v[16:17]\n"
    "v_pk_add_f32 v[10:11], v[10:11], v[18:19]\n"
    "s_waitcnt lgkmcnt(4)\n"
    "v_pk_add_f32 v[12:13], v[12:13], v[20:21]\n"
    "v_pk_add_f32 v[14:15], v[14:15], v[22:23]\n"
    "s_waitcnt lgkmcnt(1)\n"
    "v_pk_add_f32 v[24:25], v[24:25], v[32:33]\n"
    "v_pk_add_f32 v[26:27], v[26:27], v[34:35]\n"
    "s_waitcnt lgkmcnt(0)\n"
    "v_pk_add_f32 v[28:29], v[28:29], v[36:37]\n"
    "v_pk_add_f32 v[30:31], v[30:31], v[38:39]\n"
    "v_pk_add_f32 v[8:9], v[8:9], v[24:25]\n"
    "v_pk_add_f32 v[10:11], v[10:11], v[26:27]\n"
    "v_pk_add_f32 v[12:13], v[12:13], v[28:29]\n"
    "v_pk_add_f32 v[14:15], v[14:15], v[30:31]\n"
    "v_cvt_pk_bf16_f32 v40, v8, v9\n"
    "v_cvt_pk_bf16_f32 v41, v10, v11\n"
    "v_cvt_pk_bf16_f32 v42, v12, v13\n"
    "v_cvt_pk_bf16_f32 v43, v14, v15\n"
    "buffer_store_dwordx4 v[40:43], v158, s[32:35], s91 offen offset:0 sc1\n"
    "s_or_b32 s94, s2, s48\n"
    "s_cbranch_scc1 .Lskip_23\n"
    "s_mov_b64 exec, 1\n"
    "buffer_store_dwordx2 v[4:5], off, s[28:31], s94 sc1\n"
    "s_mov_b64 exec, -1\n"
    ".Lskip_23:\n"
    "s_endpgm\n"
    ".Lslow:\n"
    "s_waitcnt vmcnt(0) lgkmcnt(0)\n"
    "s_nop 7\n"
    "s_nop 4\n"
    "s_lshl_b32 s94, s53, 17\n"
    "s_add_u32 s96, s94, 0x300000\n"
    "s_mov_b32 s94, s96\n"
    "s_add_u32 s95, s96, 0x20000\n"
    "v_mov_b32 v244, 0xff800000\n"
    ".Lslow1:\n"
    "buffer_load_dwordx4 v[164:167], v1, s[28:31], s94 offen sc1\n"
    "s_waitcnt vmcnt(0)\n"
    "v_mfma_f32_32x32x16_bf16 v[208:223], v[164:167], v[160:163], 0\n"
    "s_nop 11\n"
    "v_max3_f32 v6, v208, v209, v210\n"
    "v_max3_f32 v7, v211, v212, v213\n"
    "v_max3_f32 v6, v6, v214, v215\n"
    "v_max3_f32 v7, v7, v216, v217\n"
    "v_max3_f32 v6, v6, v218, v219\n"
    "v_max3_f32 v7, v7, v220, v221\n"
    "v_max3_f32 v6, v6, v222, v223\n"
    "v_max_f32 v6, v6, v7\n"
    "v_max_f32 v244, v244, v6\n"
    "s_add_u32 s94, s94, 0x800\n"
    "s_cmp_lg_u32 s94, s95\n"
    "s_cbranch_scc1 .Lslow1\n"
    "v_mov_b32 v6, v244\n"
    "v_mov_b32 v7, v6\n"
    "s_nop 1\n"
    "v_permlane32_swap_b32 v6, v7\n"
    "v_min_f32 v192, -v6, -v7\n"
    "v_mov_b32 v193, v192\n"
    "v_mov_b64 v[194:195], v[192:193]\n"
    "v_mov_b64 v[196:197], v[192:193]\n"
    "v_mov_b64 v[198:199], v[192:193]\n"
    "v_mov_b64 v[200:201], v[192:193]\n"
    "v_mov_b64 v[202:203], v[192:193]\n"
    "v_mov_b64 v[204:205], v[192:193]\n"
    "v_mov_b64 v[206:207], v[192:193]\n"
    "v_mov_b64 v[176:177], 0\n"
    "v_mov_b64 v[178:179], 0\n"
    "v_mov_b64 v[180:181], 0\n"
    "v_mov_b64 v[182:183], 0\n"
    "v_mov_b64 v[184:185], 0\n"
    "v_mov_b64 v[186:187], 0\n"
    "v_mov_b64 v[188:189], 0\n"
    "v_mov_b64 v[190:191], 0\n"
    ".Lslow2:\n"
    "buffer_load_dwordx4 v[164:167], v1, s[28:31], s96 offen sc1\n"
    "s_mov_b64 exec, s[42:43]\n"
    "buffer_load_dwordx4 v[168:171], v1, s[28:31], s96 offen offset:1024 sc1\n"
    "buffer_load_dwordx4 v[172:175], v1, s[28:31], s96 offen offset:1280 sc1\n"
    "s_mov_b64 exec, -1\n"
    "s_waitcnt vmcnt(2)\n"
    "v_mfma_f32_32x32x16_bf16 v[208:223], v[164:167], v[160:163], v[192:207]\n"
    "s_nop 11\n"
    "v_exp_f32 v208, v208\n"
    "v_exp_f32 v209, v209\n"
    "v_exp_f32 v210, v210\n"
    "v_exp_f32 v211, v211\n"
    "v_exp_f32 v212, v212\n"
    "v_exp_f32 v213, v213\n"
    "v_exp_f32 v214, v214\n"
    "v_exp_f32 v215, v215\n"
    "v_exp_f32 v216, v216\n"
    "v_exp_f32 v217, v217\n"
    "v_exp_f32 v218, v218\n"
    "v_exp_f32 v219, v219\n"
    "v_exp_f32 v220, v220\n"
    "v_exp_f32 v221, v221\n"
    "v_exp_f32 v222, v222\n"
    "v_exp_f32 v223, v223\n"
    "s_waitcnt vmcnt(1)\n"
    "v_smfmac_f32_32x32x32_bf16 v[176:191], v[168:171], v[208:215], v129\n"
    "s_waitcnt vmcnt(0)\n"
    "v_smfmac_f32_32x32x32_bf16 v[176:191], v[172:175], v[216:223], v129\n"
    "s_add_u32 s96, s96, 0x800\n"
    "s_waitcnt vmcnt(0) lgkmcnt(0)\n"
    "s_nop 7\n"
    "s_nop 4\n"
    "s_cmp_lg_u32 s96, s95\n"
    "s_cbranch_scc1 .Lslow2\n"
    "s_branch .Lfast\n"
    ".Lkend:\n"
    : "+{v0}"(tid)
    : "{s[0:1]}"(ka), "{s2}"(wgid), "{s3}"(lds0)
    : "memory", "vcc", "scc", "v1", "v2", "v3", "v4", "v5", "v6", "v7", "v8", "v9", "v10", "v11", "v12", "v13", "v14", "v15", "v16", "v17", "v18", "v19", "v20", "v21", "v22", "v23", "v24", "v25", "v26", "v27", "v28", "v29", "v30", "v31", "v32", "v33", "v34", "v35", "v36", "v37", "v38", "v39", "v40", "v41", "v42", "v43", "v44", "v45", "v46", "v47", "v48", "v49", "v50", "v51", "v52", "v53", "v54", "v55", "v56", "v57", "v58", "v59", "v60", "v61", "v62", "v63", "v64", "v65", "v66", "v67", "v68", "v69", "v70", "v71", "v72", "v73", "v74", "v75", "v76", "v77", "v78", "v79", "v80", "v81", "v82", "v83", "v84", "v85", "v86", "v87", "v88", "v89", "v90", "v91", "v92", "v93", "v94", "v95", "v96", "v97", "v98", "v99", "v100", "v101", "v102", "v103", "v104", "v105", "v106", "v107", "v108", "v109", "v110", "v111", "v112", "v113", "v114", "v115", "v116", "v117", "v118", "v119", "v120", "v121", "v122", "v123", "v124", "v125", "v126", "v127", "v128", "v129", "v130", "v131", "v132", "v133", "v134", "v135", "v136", "v137", "v138", "v139", "v140", "v141", "v142", "v143", "v144", "v145", "v146", "v147", "v148", "v149", "v150", "v151", "v152", "v153", "v154", "v155", "v156", "v157", "v158", "v159", "v160", "v161", "v162", "v163", "v164", "v165", "v166", "v167", "v168", "v169", "v170", "v171", "v172", "v173", "v174", "v175", "v176", "v177", "v178", "v179", "v180", "v181", "v182", "v183", "v184", "v185", "v186", "v187", "v188", "v189", "v190", "v191", "v192", "v193", "v194", "v195", "v196", "v197", "v198", "v199", "v200", "v201", "v202", "v203", "v204", "v205", "v206", "v207", "v208", "v209", "v210", "v211", "v212", "v213", "v214", "v215", "v216", "v217", "v218", "v219", "v220", "v221", "v222", "v223", "v224", "v225", "v226", "v227", "v228", "v229", "v230", "v231", "v232", "v233", "v234", "v235", "v236", "v237", "v238", "v239", "v240", "v241", "v242", "v243", "v244", "v245", "v246", "v247", "v248", "v249", "v250", "v251", "v252", "v253", "v254", "v255", "s4", "s5", "s6", "s7", "s8", "s9", "s10", "s11", "s12", "s13", "s14", "s15", "s16", "s17", "s18", "s19", "s20", "s21", "s22", "s23", "s24", "s25", "s26", "s27", "s28", "s29", "s30", "s31", "s33", "s34", "s35", "s36", "s37", "s38", "s39", "s40", "s41", "s42", "s43", "s44", "s45", "s46", "s47", "s48", "s49", "s50", "s51", "s52", "s53", "s54", "s55", "s56", "s57", "s58", "s59", "s60", "s61", "s62", "s63", "s64", "s65", "s66", "s67", "s68", "s69", "s70", "s71", "s72", "s73", "s74", "s75", "s76", "s77", "s78", "s79", "s80", "s81", "s82", "s83", "s84", "s85", "s86", "s87", "s88", "s89", "s90", "s91", "s92", "s93", "s94", "s95", "s96", "s97", "s98", "s99");
#endif
}

extern "C" void kernel_launch(void* const* d_in, const int* in_sizes, int n_in, void* d_out, int out_size, void* d_ws, size_t ws_size,
                              hipStream_t stream) {
  fused_block<<<256, 512, 0, stream>>>(d_in[0], d_in[1], d_in[2], d_in[4], d_in[5], d_in[6], d_in[7], d_out, d_ws);
}
```
